```python
import torch

def _softmax(x, axis=-1):
    return torch.softmax(x, dim=axis)

def _gelu(x):
    return torch.nn.functional.gelu(x, approximate='tanh')

def _silu(x):
    return torch.nn.functional.silu(x)

def _sigmoid(x):
    return torch.sigmoid(x)

def _layernorm(x, axis=-1, eps=1e-05):
    m = x.mean(dim=axis, keepdim=True)
    v = x.var(dim=axis, keepdim=True, unbiased=False)
    return (x - m) / torch.sqrt(v + eps)

def _scatter_add(tgt, idx, src, axis=0):
    return tgt.clone().index_add_(axis, idx, src)

def _segment_sum(src, seg, n):
    out = torch.zeros((n,) + src.shape[1:], dtype=src.dtype)
    return out.index_add_(0, seg, src)

def _pad(x, pw):
    flat = tuple((v for pair in reversed(pw) for v in pair))
    return torch.nn.functional.pad(x, flat)
kernel_name = 'adaLN_cond__sliding_block__swiglu_ffn__prompt'

def setup_inputs():

    _w = lambda a: (lambda t: t.to(torch.bfloat16) if t.is_floating_point() else t)(torch.as_tensor(a))

    def _A_setup():
        B, S, D = (1, 2048, 640)
        return {'x': _w((lambda *s: torch.randn(*s, dtype=torch.bfloat16) * 0.05)(B, S, D)), 'wqkv': _w((lambda *s: torch.randn(*s, dtype=torch.bfloat16) * 0.05)(D, 3 * D)), 'wo': _w((lambda *s: torch.randn(*s, dtype=torch.bfloat16) * 0.05)(D, D))}
    _A_in = _A_setup()

    def _B_setup():
        B, S, D, F = (1, 2048, 640, 1536)
        return {'x': _w((lambda *s: torch.randn(*s, dtype=torch.bfloat16) * 0.05)(B, S, D)), 'w_gate': _w((lambda *s: torch.randn(*s, dtype=torch.bfloat16) * 0.05)(D, F)), 'w_up': _w((lambda *s: torch.randn(*s, dtype=torch.bfloat16) * 0.05)(D, F)), 'w_down': _w((lambda *s: torch.randn(*s, dtype=torch.bfloat16) * 0.05)(F, D))}
    _B_in = _B_setup()
    return {'A_x': _A_in['x'], 'A_wqkv': _A_in['wqkv'], 'A_wo': _A_in['wo'], 'B_x': _B_in['x'], 'B_w_gate': _B_in['w_gate'], 'B_w_up': _B_in['w_up'], 'B_w_down': _B_in['w_down'], 'c': _w((lambda *s: torch.randn(*s, dtype=torch.bfloat16) * 0.1)(1, 640) * 8.0), 'w_mod': _w((lambda *s: torch.randn(*s, dtype=torch.bfloat16) * 0.1)(640, 6 * 640) * 640 ** (-0.5) * 8.0)}

def reference(A_x, A_wqkv, A_wo, B_x, B_w_gate, B_w_up, B_w_down, c, w_mod):

    def _A_ref(x, wqkv, wo):
        B, S, D = x.shape
        H = 32
        Dh = D // H
        W = 256
        qkv = (lambda x, *d: x.permute(*d))((x @ wqkv).reshape(B, S, 3, H, Dh), 2, 0, 3, 1, 4)
        q, k, v = (qkv[0], qkv[1], qkv[2])
        scores = torch.einsum('bhqd,bhkd->bhqk', q, k) * Dh ** (-0.5)
        idx = torch.arange(S)
        mask = (idx[:, None] - idx[None, :] >= 0) & (idx[:, None] - idx[None, :] < W)
        scores = torch.where(mask[None, None], scores, -1000000000.0)
        p = _softmax(scores, axis=-1)
        o = torch.einsum('bhqk,bhkd->bhqd', p, v)
        return (lambda x, *d: x.permute(*d))(o, 0, 2, 1, 3).reshape(B, S, D) @ wo

    def _B_ref(x, w_gate, w_up, w_down):
        g = _silu(x @ w_gate)
        u = x @ w_up
        return g * u @ w_down
    x = A_x
    mod = (_silu(c) @ w_mod).reshape(c.shape[0], 6, 1, -1)
    sa, ca, ga, sb, cb, gb = (mod[:, 0].clone(), mod[:, 1].clone(), mod[:, 2].clone(), mod[:, 3].clone(), mod[:, 4].clone(), mod[:, 5].clone())
    h = _layernorm(x, axis=-1) * (1 + ca) + sa
    x = x + ga * _A_ref(h, A_wqkv, A_wo)
    h = _layernorm(x, axis=-1) * (1 + cb) + sb
    x = x + gb * _B_ref(h, B_w_gate, B_w_up, B_w_down)
    return x
```

```cpp
#include <hip/hip_runtime.h>
#include <hip/hip_bf16.h>

typedef unsigned short u16;
typedef unsigned v4u __attribute__((ext_vector_type(4)));
typedef unsigned v2u __attribute__((ext_vector_type(2)));
typedef short v4s __attribute__((ext_vector_type(4)));
typedef float f32x2 __attribute__((ext_vector_type(2)));
typedef float f32x4 __attribute__((ext_vector_type(4)));
typedef float f32x16 __attribute__((ext_vector_type(16)));
typedef __bf16 bf16x2 __attribute__((ext_vector_type(2)));
typedef __bf16 bf16x8 __attribute__((ext_vector_type(8)));
typedef __amdgpu_buffer_rsrc_t rsrc_t;
#define LAS __attribute__((address_space(3)))
#define GAS __attribute__((address_space(1)))
#define DI __device__ __forceinline__
#define RLX_AGENT __ATOMIC_RELAXED, __HIP_MEMORY_SCOPE_AGENT

#define STAMPS 0

constexpr int WS_CTRL = 0;
constexpr int WS_MODP = 65536;
constexpr int WS_UV   = 131072;
constexpr int WS_ST   = 327680;
constexpr int WS_WT1  = 655360;
constexpr int WS_WT3  = WS_WT1 + 16 * 163840;
constexpr int WS_WT4  = WS_WT3 + 8 * 102400;
constexpr int WS_WT5  = WS_WT4 + 16 * 245760;
constexpr int WS_KV   = WS_WT5 + 8 * 245760;
constexpr int WS_OF   = WS_KV + 64 * 16 * 5120;
constexpr int WS_X2F  = WS_OF + 128 * 20 * 1024;
constexpr int WS_ACT  = WS_X2F + 64 * 40 * 1024;
constexpr int WS_END  = WS_ACT + 128 * 48 * 1024;
constexpr int WS_DBG  = WS_END;
constexpr int CT_E = 0, CT_FLAG = 4096  , CT_C1 = 16384  , CT_C2 = 49152, CT_C3 = CT_C2 + 2048,
              CT_C4 = CT_C3 + 2048, CT_CB = CT_C4 + 2048  ;
constexpr int L_WQ = 147456, L_RED = 157696, L_ST1 = 158720;
constexpr int L_BSRC = 122880;
constexpr int L_MODV = 155648;
constexpr int L_ONES = 162048;
constexpr int L_UVS = 122880;
constexpr int L_KVOWN = 65536, L_Q = 86016, L_QHI = 94208;
constexpr unsigned SPIN_CAP = 1u << 20;

struct Params { const u16 *x, *wqkv, *wo, *wg, *wu, *wd, *c, *wmod; u16* out; char* ws; };

DI f32x16 mfma32(v4u a, v4u b, f32x16 c) { return __builtin_amdgcn_mfma_f32_32x32x16_bf16(__builtin_bit_cast(bf16x8, a), __builtin_bit_cast(bf16x8, b), c, 0, 0, 0); }
DI f32x4 mfma16(v4u a, v4u b, f32x4 c) { return __builtin_amdgcn_mfma_f32_16x16x32_bf16(__builtin_bit_cast(bf16x8, a), __builtin_bit_cast(bf16x8, b), c, 0, 0, 0); }
DI unsigned pk2(float a, float b) { f32x2 v = {a, b}; return __builtin_bit_cast(unsigned, __builtin_convertvector(v, bf16x2)); }
DI float bflo(unsigned x) { return __builtin_bit_cast(float, x << 16); }
DI float bfhi(unsigned x) { return __builtin_bit_cast(float, x & 0xffff0000u); }
DI float bfr(float a) { return bflo(pk2(a, a)); }
DI f32x2 swp32(float v) {
  unsigned a = __builtin_bit_cast(unsigned, v), c = a; asm("" : "+v"(c));
  auto r = __builtin_amdgcn_permlane32_swap(a, c, false, false);
  unsigned r0 = r[0], r1 = r[1];
  return f32x2{__builtin_bit_cast(float, r0), __builtin_bit_cast(float, r1)};
}
DI f32x2 swp16(float v) {
  unsigned a = __builtin_bit_cast(unsigned, v), c = a; asm("" : "+v"(c));
  auto r = __builtin_amdgcn_permlane16_swap(a, c, false, false);
  unsigned r0 = r[0], r1 = r[1];
  return f32x2{__builtin_bit_cast(float, r0), __builtin_bit_cast(float, r1)};
}
DI float swap32_add(float v) { f32x2 r = swp32(v); return r.x + r.y; }
DI float swap16_add(float v) { f32x2 r = swp16(v); return r.x + r.y; }
DI float swap32_max(float v) { f32x2 r = swp32(v); return fmaxf(r.x, r.y); }
DI float swap32_hi(float v) { return swp32(v).y; }
DI float ror8_add(float v) { return v + __builtin_bit_cast(float, __builtin_amdgcn_update_dpp(0, __builtin_bit_cast(int, v), 0x128, 0xf, 0xf, true)); }

template <bool SC1> DI void dma(unsigned lds_addr, unsigned voff, rsrc_t rs, unsigned soff) {
  if (SC1) asm volatile("s_mov_b32 m0, %0\n\ts_nop 0\n\tbuffer_load_dwordx4 %1, %2, %3 offen sc1 lds" ::"s"(lds_addr), "v"(voff), "s"(rs), "s"(soff) : "memory");
  else     asm volatile("s_mov_b32 m0, %0\n\ts_nop 0\n\tbuffer_load_dwordx4 %1, %2, %3 offen lds" ::"s"(lds_addr), "v"(voff), "s"(rs), "s"(soff) : "memory");
}
DI void ld16(v4u& d, unsigned voff, rsrc_t rs, unsigned soff) { asm volatile("buffer_load_dwordx4 %0, %1, %2, %3 offen sc1" : "=v"(d) : "v"(voff), "s"(rs), "s"(soff) : "memory"); }
DI void ld8p(v2u& d, unsigned voff, rsrc_t rs, unsigned soff) { asm volatile("buffer_load_dwordx2 %0, %1, %2, %3 offen" : "=v"(d) : "v"(voff), "s"(rs), "s"(soff) : "memory"); }
DI void ld4(unsigned& d, unsigned voff, rsrc_t rs, unsigned soff) { asm volatile("buffer_load_dword %0, %1, %2, %3 offen sc1" : "=v"(d) : "v"(voff), "s"(rs), "s"(soff) : "memory"); }
template <int N> DI void waitv() { asm volatile("s_waitcnt vmcnt(%0)" ::"n"(N) : "memory"); }
template <class T> DI void tie(T& a) { asm volatile("" : "+v"(a)); }
DI void bar() { asm volatile("s_waitcnt lgkmcnt(0)\n\ts_barrier" ::: "memory"); }
DI void st16(v4u v, rsrc_t rs, unsigned off) { __builtin_amdgcn_raw_buffer_store_b128(v, rs, off, 0, 16); }
DI void st8(v2u v, rsrc_t rs, unsigned off) { __builtin_amdgcn_raw_buffer_store_b64(v, rs, off, 0, 16); }
DI v4u trpair(LAS char* L, unsigned a0, unsigned a1) {
  v4s lo = __builtin_amdgcn_ds_read_tr16_b64_v4i16((LAS v4s*)(L + a0));
  v4s hi = __builtin_amdgcn_ds_read_tr16_b64_v4i16((LAS v4s*)(L + a1));
  v2u l = __builtin_bit_cast(v2u, lo), h = __builtin_bit_cast(v2u, hi);
  return v4u{l.x, l.y, h.x, h.y};
}
DI v4u lds16(LAS char* L, unsigned a) { return *(LAS v4u*)(L + a); }
DI f32x4 ldsf4(LAS char* L, unsigned a) { return *(LAS f32x4*)(L + a); }

template <int N> DI unsigned affine2(unsigned x, float av, float sv) {
  float t0, t1;
  asm("v_lshlrev_b32 %0, 16, %2\n\tv_and_b32 %1, 0xffff0000, %2\n\t"
      "v_mul_f32_dpp %0, %3, %0 row_newbcast:%5 row_mask:0xf bank_mask:0xf\n\tv_mul_f32_dpp %1, %3, %1 row_newbcast:%6 row_mask:0xf bank_mask:0xf\n\t"
      "v_add_f32_dpp %0, %4, %0 row_newbcast:%5 row_mask:0xf bank_mask:0xf\n\tv_add_f32_dpp %1, %4, %1 row_newbcast:%6 row_mask:0xf bank_mask:0xf"
      : "=&v"(t0), "=&v"(t1) : "v"(x), "v"(av), "v"(sv), "n"(N), "n"(N + 1));
  return pk2(t0, t1);
}

constexpr int issued_before(int e, int PS, int R0, int TOT) { int v = e == 0 ? R0 : (PS * (e - 1) + 120) / 8; v = v < R0 ? R0 : v; return v > TOT ? TOT : v; }
constexpr int need_rounds(int e, int PS, int TOT) { int v = (PS * (e + 1) + 7) / 8; return v > TOT ? TOT : v; }

#define STAMP(i)

DI void poll(const GAS unsigned* addr, unsigned base, unsigned want, bool active) {
  for (unsigned s = 0; s < SPIN_CAP; ++s) {
    unsigned v = __hip_atomic_load(addr, RLX_AGENT);
    if (__all(!active || (v - base) >= want)) break;
  }
  __builtin_amdgcn_fence(__ATOMIC_ACQUIRE, "wavefront");
}

__global__ __launch_bounds__(512, 2) void fused_block(Params P) {
  __shared__ __attribute__((aligned(16))) char lds_raw[163840];
  LAS char* L = (LAS char*)lds_raw;
  const unsigned LB = (unsigned)(size_t)L;
  const int tid = threadIdx.x, lane = tid & 63;
  const int w = __builtin_amdgcn_readfirstlane(tid >> 6);
  const int b = blockIdx.x;
  const int p = (b & 7) * 2 + (b >> 7), j = (b >> 3) & 15;
  const int m = w & 3, kh = w >> 2, h = lane >> 5, r32 = lane & 31;
  const int g4 = lane >> 4, i16 = lane & 15, q4 = (lane & 15) >> 2, p4 = lane & 3, blk = (lane >> 4) & 1;
  const rsrc_t rws = __builtin_amdgcn_make_buffer_rsrc((void*)P.ws, 0, WS_END, 0x00020000);
  const unsigned l16 = lane * 16;
  STAMP(0);

  unsigned cbase = 0; const GAS unsigned* caddr = (const GAS unsigned*)P.ws;
  if (w == 0) {
    int off = CT_E;
    if (lane == 1) off = CT_C1 + (((p >= 1 ? p - 1 : 0) * 16 + j) << 7);
    if (lane == 2) off = CT_C1 + (((p >= 2 ? p - 2 : 0) * 16 + j) << 7);
    if (lane == 3) off = CT_C2 + (p << 7);
    if (lane == 4) off = CT_C3 + (p << 7);
    if (lane == 5) off = CT_C4 + (p << 7);
    if (lane >= 8 && lane < 16) off = CT_CB + ((lane - 8) << 7);
    caddr = (const GAS unsigned*)(P.ws + off);
    cbase = __hip_atomic_load(caddr, RLX_AGENT);
  }

  v4u xf[20];
  if (w < 4) {
    if (b < 240) {
      const int cc = b % 60, kq = b / 60, c8 = tid & 7, rg = tid >> 3;
      v4u wm[5]; u16 cv[5]; v4u wq[3];
#pragma unroll
      for (int i = 0; i < 5; ++i) { int k = 160 * kq + rg + 32 * i; wm[i] = *(const GAS v4u*)((const char*)P.wmod + (size_t)k * 7680 + cc * 128 + c8 * 16); cv[i] = P.c[k]; }
      const int s = b / 6, q = b % 6;
#pragma unroll
      for (int i = 0; i < 3; ++i) { int c = tid + 256 * i; if (c >= 640) c = tid; wq[i] = *(const GAS v4u*)((const char*)P.wqkv + (size_t)(16 * s + c / 40) * 3840 + q * 640 + (c % 40) * 16); }
      float acc[8] = {0, 0, 0, 0, 0, 0, 0, 0};
#pragma unroll
      for (int i = 0; i < 5; ++i) {
        float cf = bflo(cv[i]);
        float sv = bfr(cf * __builtin_amdgcn_rcpf(1.f + __builtin_amdgcn_exp2f(-1.44269504f * cf)));
#pragma unroll
        for (int e = 0; e < 4; ++e) { acc[2 * e] += sv * bflo(wm[i][e]); acc[2 * e + 1] += sv * bfhi(wm[i][e]); }
      }
#pragma unroll
      for (int e = 0; e < 8; ++e) acc[e] = swap32_add(swap16_add(ror8_add(acc[e])));
      if (lane < 8) {
        *(LAS f32x4*)(L + L_RED + w * 256 + lane * 32) = f32x4{acc[0], acc[1], acc[2], acc[3]};
        *(LAS f32x4*)(L + L_RED + w * 256 + lane * 32 + 16) = f32x4{acc[4], acc[5], acc[6], acc[7]};
      }
#pragma unroll
      for (int i = 0; i < 3; ++i) { int c = tid + 256 * i; if (c < 640) *(LAS v4u*)(L + L_WQ + c * 16) = wq[i]; }
    }
    bar();
    if (b < 240) {
      const int cc = b % 60, kq = b / 60, s = b / 6, q = b % 6;
      if (w == 0 && lane < 16) {
        f32x4 v = ldsf4(L, L_RED + lane * 16) + ldsf4(L, L_RED + 256 + lane * 16) + ldsf4(L, L_RED + 512 + lane * 16) + ldsf4(L, L_RED + 768 + lane * 16);
        st16(__builtin_bit_cast(v4u, v), rws, WS_MODP + (kq * 3840 + cc * 64 + lane * 4) * 4);
      }
      const int part = q >> 1, jb = 8 * (q & 1);
      const int tmain = part == 0 ? 3 : part - 1;
      const unsigned dst0 = WS_WT1 + (s % 20) * 8192 + (s / 20) * 4096;
      const unsigned rowa = L_WQ + (8 * h + q4) * 640;
#pragma unroll
      for (int u = 0; u < 2; ++u) {
        const int jj = 2 * w + u;
        const unsigned a = rowa + (40 * jj + 20 * blk + 4 * p4) * 2;
        v4u f = trpair(L, a, a + 4 * 640);
        st16(f, rws, dst0 + (jb + jj) * 163840 + tmain * 1024 + l16);
      }
      {
        const int jj = 2 * w + blk;
        const unsigned a = rowa + (40 * jj + 16 + 20 * (p4 & 1)) * 2;
        v4u f = trpair(L, a, a + 4 * 640);
        if (i16 >= 8) f = v4u{0, 0, 0, 0};
        const int r = (part == 0 ? 16 : part == 1 ? 0 : 8) + i16;
        if (part == 0 || i16 < 8) st16(f, rws, dst0 + (jb + jj) * 163840 + 2048 + (h * 32 + r) * 16);
      }
    }
    waitv<0>();
    bar();
    if (w == 0) {
      unsigned tag = __builtin_amdgcn_readfirstlane(cbase) + 1u;
      if (lane < 8) __hip_atomic_store((GAS unsigned*)(P.ws + CT_FLAG + lane * 1024 + b * 4), tag, RLX_AGENT);
    }
    STAMP(1);
  } else {
    const rsrc_t rx = __builtin_amdgcn_make_buffer_rsrc((void*)P.x, 0, 2048 * 1280, 0x00020000);
    const int row8 = lane >> 3, slot = lane & 7;
#pragma unroll
    for (int oo = 0; oo < 4; ++oo) {
      const unsigned voff = row8 * 1280 + ((slot ^ (((oo & 1) << 2) | (row8 >> 1))) << 4);
#pragma unroll
      for (int l = 0; l < 9; ++l) dma<false>(LB + (l * 16 + 4 * m + oo) * 1024, voff, rx, (128 * p + 32 * m + 8 * oo) * 1280 + l * 128);
    }
#pragma unroll
    for (int i = 0; i < 4; ++i) xf[16 + i] = *(const GAS v4u*)((const char*)P.x + (size_t)(128 * p + 32 * m + r32) * 1280 + 1152 + i * 32 + h * 16);
    bar();
    bar();
    waitv<0>();
  }
  bar();
  STAMP(2);
  {
    const unsigned xb = kh * 5 * 16384 + (4 * m + (r32 >> 3)) * 1024 + (r32 & 7) * 128;
    const unsigned fx = h ^ ((r32 >> 1) & 7);
#pragma unroll
    for (int i = 0; i < 20; ++i)
      if (i < 16 || kh == 0) xf[i] = lds16(L, xb + (i >> 2) * 16384 + (((2 * (i & 3)) ^ fx) << 4));
    f32x16 aG = {}, aS = {};
    const v4u ones = {0x3f803f80u, 0x3f803f80u, 0x3f803f80u, 0x3f803f80u};
#pragma unroll
    for (int i = 0; i < 20; ++i) { aG = mfma32(xf[i], xf[i], aG); aS = mfma32(ones, xf[i], aS); }
    float sq = 0.f;
#pragma unroll
    for (int r = 0; r < 16; ++r) sq = ((r & 3) + 8 * (r >> 2) + 4 * h == r32) ? aG[r] : sq;
    sq = swap32_add(sq);
    if (lane < 32) *(LAS f32x2*)(L + L_ST1 + (kh * 128 + m * 32 + lane) * 8) = f32x2{aS[0], sq};
    bar();
    f32x2 s0 = *(LAS f32x2*)(L + L_ST1 + (m * 32 + r32) * 8), s1 = *(LAS f32x2*)(L + L_ST1 + (128 + m * 32 + r32) * 8);
    const float mean = (s0.x + s1.x) * (1.f / 640.f);
    const float var = fmaxf((s0.y + s1.y) * (1.f / 640.f) - mean * mean, 0.f);
    const float rstd = __builtin_amdgcn_rsqf(var + 1e-5f), nm = -mean * rstd;
#pragma unroll
    for (int i = 0; i < 20; ++i)
#pragma unroll
      for (int e = 0; e < 4; ++e) xf[i][e] = pk2(fmaf(bflo(xf[i][e]), rstd, nm), fmaf(bfhi(xf[i][e]), rstd, nm));
  }
  STAMP(3);
  if (w == 0) {
    const unsigned tag = __builtin_amdgcn_readfirstlane(cbase) + 1u;
    for (unsigned s = 0; s < SPIN_CAP; ++s) {
      v4u f = __builtin_amdgcn_raw_buffer_load_b128(rws, CT_FLAG + (b & 7) * 1024 + l16, 0, 16);
      if (__all(f.x == tag && f.y == tag && f.z == tag && f.w == tag)) break;
    }
    __builtin_amdgcn_fence(__ATOMIC_ACQUIRE, "wavefront");
  }
  bar();
  STAMP(4);

  const int c8 = j & 7, rh = j >> 3;
  {
    int col = 4 * tid;
    if (tid >= 320) col = 1280 + 80 * c8 + 4 * (tid - 320);
    if (tid >= 340) col = 3200 + 80 * c8 + 4 * (tid - 340);
    if (tid >= 360) col = 2560 + 80 * ((b >> 3) & 7) + 4 * (tid - 360);
    if (tid >= 380) col = 1920 + 80 * ((b >> 3) & 7) + 4 * (tid - 380);
    if (tid >= 400) col = 0;
    v4u mp[4];
#pragma unroll
    for (int i = 0; i < 4; ++i) ld16(mp[i], col * 4, rws, WS_MODP + i * 15360);
    const unsigned w1 = WS_WT1 + j * 163840 + w * 1024;
#pragma unroll
    for (int r = 0; r < 15; ++r) dma<true>(LB + (r * 8 + w) * 1024, l16, rws, w1 + r * 8192);
    {
      const char* src; int nch, cpr, rstride;
      if (b < 128) { src = (const char*)((b >> 6) ? P.wu : P.wg) + (size_t)(80 * ((b >> 3) & 7)) * 3072 + (b & 7) * 384; cpr = 24; nch = 1920; rstride = 3072; }
      else if (b < 224) { src = (const char*)P.wd + (size_t)(32 * ((b - 128) >> 1)) * 1280 + (b & 1) * 640; cpr = 40; nch = 1280; rstride = 1280; }
      else { src = (const char*)P.wo + (size_t)(160 * ((b - 224) >> 3)) * 1280 + (b & 7) * 160; cpr = 10; nch = 1600; rstride = 1280; }
      const rsrc_t rb = __builtin_amdgcn_make_buffer_rsrc((void*)src, 0, 0x00400000, 0x00020000);
#pragma unroll
      for (int i = 0; i < 4; ++i) {
        int c = (i * 8 + w) * 64 + lane; if (c >= nch) c = 0;
        dma<false>(LB + L_BSRC + (i * 8 + w) * 1024, (c / cpr) * rstride + (c % cpr) * 16, rb, 0);
      }
    }
    waitv<19>();
#pragma unroll
    for (int i = 0; i < 4; ++i) tie(mp[i]);
    {
      f32x4 v = (__builtin_bit_cast(f32x4, mp[0]) + __builtin_bit_cast(f32x4, mp[1])) + (__builtin_bit_cast(f32x4, mp[2]) + __builtin_bit_cast(f32x4, mp[3]));
      const bool isc = (tid >= 160 && tid < 320) || (tid >= 360 && tid < 380);
#pragma unroll
      for (int e = 0; e < 4; ++e) { float t = bfr(v[e]); v[e] = isc ? bfr(1.f + t) : t; }
      if (tid < 400) *(LAS f32x4*)(L + L_MODV + tid * 16) = v;
      if (tid == 511) *(LAS v4u*)(L + L_ONES) = v4u{0x00003f80u, 0u, 0u, 0u};
    }
    bar();
    float av[10], sv[10];
#pragma unroll
    for (int i = 0; i < 10; ++i) {
      const int k = 320 * kh + 32 * i + 16 * (i16 >> 3) + 8 * h + (i16 & 7);
      sv[i] = *(LAS float*)(L + L_MODV + k * 4);
      av[i] = *(LAS float*)(L + L_MODV + 2560 + k * 4);
    }
    STAMP(5);
    f32x16 acc[4] = {};
    const unsigned wb = kh * 4096 + l16;
#pragma unroll
    for (int e = 0; e < 10; ++e) {
      constexpr int PS = 16, R0 = 15, TOT = 20;
      const int iss = issued_before(e, PS, R0, TOT), nd = need_rounds(e, PS, TOT);
      switch (iss - nd + (nd <= R0 ? 4 : 0)) {
#define WCASE(n) case n: waitv<n>(); break;
        WCASE(0) WCASE(1) WCASE(2) WCASE(3) WCASE(4) WCASE(5) WCASE(6) WCASE(7) WCASE(8) WCASE(9) WCASE(10) WCASE(11) WCASE(12) WCASE(13) WCASE(14) WCASE(15) WCASE(16) WCASE(17)
        default: waitv<0>();
      }
      bar();
#pragma unroll
      for (int r = iss; r < issued_before(e + 1, PS, R0, TOT); ++r) dma<true>(LB + ((r * 8) % 120 + w) * 1024, l16, rws, w1 + r * 8192);
#pragma unroll
      for (int u = 0; u < 2; ++u) {
        const int i = 2 * e + u;
        v4u hf;
        if (u == 0) { hf[0] = affine2<0>(xf[i][0], av[e], sv[e]); hf[1] = affine2<2>(xf[i][1], av[e], sv[e]); hf[2] = affine2<4>(xf[i][2], av[e], sv[e]); hf[3] = affine2<6>(xf[i][3], av[e], sv[e]); }
        else { hf[0] = affine2<8>(xf[i][0], av[e], sv[e]); hf[1] = affine2<10>(xf[i][1], av[e], sv[e]); hf[2] = affine2<12>(xf[i][2], av[e], sv[e]); hf[3] = affine2<14>(xf[i][3], av[e], sv[e]); }
#pragma unroll
        for (int t = 0; t < 4; ++t) acc[t] = mfma32(lds16(L, wb + ((i * 8) % 120 + t) * 1024), hf, acc[t]);
      }
    }
    STAMP(6);
    bar();
    f32x16 ka = kh ? acc[1] : acc[0], kb = kh ? acc[3] : acc[2], sa = kh ? acc[0] : acc[1], sb = kh ? acc[2] : acc[3];
#pragma unroll
    for (int qd = 0; qd < 4; ++qd) {
      *(LAS f32x4*)(L + w * 8192 + qd * 1024 + l16) = f32x4{sa[4 * qd], sa[4 * qd + 1], sa[4 * qd + 2], sa[4 * qd + 3]};
      *(LAS f32x4*)(L + w * 8192 + 4096 + qd * 1024 + l16) = f32x4{sb[4 * qd], sb[4 * qd + 1], sb[4 * qd + 2], sb[4 * qd + 3]};
    }
    bar();
#pragma unroll
    for (int qd = 0; qd < 4; ++qd) {
      f32x4 u = ldsf4(L, (w ^ 4) * 8192 + qd * 1024 + l16), v = ldsf4(L, (w ^ 4) * 8192 + 4096 + qd * 1024 + l16);
#pragma unroll
      for (int e = 0; e < 4; ++e) { ka[4 * qd + e] += u[e]; kb[4 * qd + e] += v[e]; }
    }
    const float qs = 0.22360679775f * 1.44269504089f;
    const unsigned kvg = WS_KV + ((4 * p + m) * 16 + j) * 5120, kvl = L_KVOWN + m * 5120;
    v4u c0 = {pk2(ka[0], ka[1]), pk2(ka[2], ka[3]), pk2(ka[4], ka[5]), pk2(ka[6], ka[7])};
    v4u c1 = {pk2(ka[8], ka[9]), pk2(ka[10], ka[11]), pk2(ka[12], ka[13]), pk2(ka[14], ka[15])};
    if (kh == 0) {
      v4u c2 = {pk2(kb[0], kb[1]), pk2(kb[2], kb[3]), pk2(kb[4], kb[5]), pk2(kb[6], kb[7])};
      st16(c0, rws, kvg + l16); st16(c1, rws, kvg + 1024 + l16); st16(c2, rws, kvg + 2048 + l16);
      *(LAS v4u*)(L + kvl + l16) = c0; *(LAS v4u*)(L + kvl + 1024 + l16) = c1; *(LAS v4u*)(L + kvl + 2048 + l16) = c2;
      *(LAS v2u*)(L + L_QHI + m * 512 + lane * 8) = v2u{pk2(kb[8] * qs, kb[9] * qs), pk2(kb[10] * qs, kb[11] * qs)};
    } else {
      const unsigned vo = r32 * 32 + h * 16;
      st16(c0, rws, kvg + 3072 + vo); st16(c1, rws, kvg + 4096 + vo);
      *(LAS v4u*)(L + kvl + 3072 + vo) = c0; *(LAS v4u*)(L + kvl + 4096 + vo) = c1;
      *(LAS v4u*)(L + L_Q + m * 2048 + l16) = v4u{pk2(kb[0] * qs, kb[1] * qs), pk2(kb[2] * qs, kb[3] * qs), pk2(kb[4] * qs, kb[5] * qs), pk2(kb[6] * qs, kb[7] * qs)};
      *(LAS v4u*)(L + L_Q + m * 2048 + 1024 + l16) = v4u{pk2(kb[8] * qs, kb[9] * qs), pk2(kb[10] * qs, kb[11] * qs), pk2(kb[12] * qs, kb[13] * qs), pk2(kb[14] * qs, kb[15] * qs)};
    }
    waitv<0>();
    bar();
    if (tid == 0) __hip_atomic_fetch_add((GAS unsigned*)(P.ws + CT_C1 + ((p * 16 + j) << 7)), 1u, RLX_AGENT);
    STAMP(7);
  }

  if (b < 128) {
    if (w < 6) {
      const int mat = b >> 6, kp = (b >> 3) & 7, cg = b & 7;
      const int tl = 2 * w + blk;
      const int jd = 2 * cg + tl / 6, t = tl % 6;
      float us = 0.f, vs = 0.f;
#pragma unroll
      for (int sg = 0; sg < 5; ++sg) {
        const int k0 = sg < 4 ? 32 * (sg >> 1) + 4 * (2 * (sg & 1) + h) : 64 + 8 * h;
        const int k1 = sg < 4 ? k0 + 16 : k0 + 4;
        const unsigned ca = L_BSRC + q4 * 384 + (16 * tl + 4 * p4) * 2;
        v4u f = trpair(L, ca + k0 * 384, ca + k1 * 384);
        f32x4 a0 = ldsf4(L, L_MODV + 5760 + k0 * 4), a1 = ldsf4(L, L_MODV + 5760 + k1 * 4);
        f32x4 b0 = ldsf4(L, L_MODV + 6080 + k0 * 4), b1 = ldsf4(L, L_MODV + 6080 + k1 * 4);
        v4u o;
        o[0] = pk2(bflo(f[0]) * a0[0], bfhi(f[0]) * a0[1]); o[1] = pk2(bflo(f[1]) * a0[2], bfhi(f[1]) * a0[3]);
        o[2] = pk2(bflo(f[2]) * a1[0], bfhi(f[2]) * a1[1]); o[3] = pk2(bflo(f[3]) * a1[2], bfhi(f[3]) * a1[3]);
        vs += bflo(f[0]) * b0[0] + bfhi(f[0]) * b0[1] + bflo(f[1]) * b0[2] + bfhi(f[1]) * b0[3] + bflo(f[2]) * b1[0] + bfhi(f[2]) * b1[1] + bflo(f[3]) * b1[2] + bfhi(f[3]) * b1[3];
        us += (bflo(o[0]) + bfhi(o[0])) + (bflo(o[1]) + bfhi(o[1])) + (bflo(o[2]) + bfhi(o[2])) + (bflo(o[3]) + bfhi(o[3]));
        const int s = 5 * kp + sg;
        st16(o, rws, WS_WT4 + jd * 245760 + ((s % 20) * 12 + (s / 20) * 6 + t) * 1024 + (h * 32 + mat * 16 + i16) * 16);
      }
      us = swap32_add(us); vs = swap32_add(vs);
      const int row = (jd * 6 + t) * 32 + mat * 16 + i16;
      __builtin_amdgcn_raw_buffer_store_b32(__builtin_bit_cast(unsigned, h ? vs : us), rws, WS_UV + ((kp * 2 + h) * 3072 + row) * 4, 0, 16);
    }
  } else if (b < 224) {
    const int st = (b - 128) >> 1, ch = b & 1;
    for (int tt = w; tt < 20; tt += 8) {
      const unsigned a = L_BSRC + (16 * (g4 >> 1) + 4 * (g4 & 1) + q4) * 640 + (16 * tt + 4 * p4) * 2;
      v4u f = trpair(L, a, a + 8 * 640);
      st16(f, rws, WS_WT5 + (4 * ch + tt / 5) * 245760 + ((st >> 1) * 10 + (st & 1) * 5 + tt % 5) * 1024 + l16);
    }
  } else {
    const int kr = (b - 224) >> 3, cw = b & 7;
    for (int u = w; u < 25; u += 8) {
      const int sl = u / 5, t = u % 5, c = 4 * sl + g4, jl = c / 5, ii = c % 5;
      const int r0 = ii < 4 ? 40 * jl + 20 * (ii >> 1) + 4 * (ii & 1) : 40 * jl + 16;
      const int r1 = ii < 4 ? r0 + 8 : r0 + 20;
      const unsigned ca = L_BSRC + q4 * 160 + (16 * t + 4 * p4) * 2;
      v4u f = trpair(L, ca + r0 * 160, ca + r1 * 160);
      const int s = 5 * kr + sl;
      st16(f, rws, WS_WT3 + cw * 102400 + ((s >> 1) * 10 + (s & 1) * 5 + t) * 1024 + l16);
    }
  }
  STAMP(8);
  if (w == 0) poll(caddr, cbase, 1u, (lane == 1 && p >= 1) || (lane == 2 && p >= 2));
  bar();
  STAMP(9);
  {
    v4u kv[5];
    const int gt = 4 * p - 8 + w;
    if (gt >= 0) {
#pragma unroll
      for (int i = 0; i < 5; ++i) ld16(kv[i], l16, rws, WS_KV + (gt * 16 + j) * 5120 + i * 1024);
    }
    waitv<0>();
    if (gt >= 0) {
#pragma unroll
      for (int i = 0; i < 5; ++i) { tie(kv[i]); *(LAS v4u*)(L + w * 5120 + i * 1024 + l16) = kv[i]; }
    }
    bar();
    if (tid == 0) __hip_atomic_fetch_add((GAS unsigned*)(P.ws + CT_CB + ((b & 7) << 7)), 1u, RLX_AGENT);
    STAMP(10);
    const int hd = kh;
    const v4u q0 = lds16(L, L_Q + m * 2048 + hd * 1024 + l16);
    v2u qh = *(LAS v2u*)(L + L_QHI + m * 512 + lane * 8);
    if (h != hd) qh = v2u{0, 0};
    const v4u q1 = {qh.x, qh.y, 0, 0};
    f32x16 S[9];
    const int nskip = 8 - 4 * p - m;
    const f32x16 zero = {};
#pragma unroll
    for (int i = 0; i < 9; ++i) {
      if (i >= nskip) {
        const int u = m + i;
        const unsigned tb = (u < 8 ? u * 5120 : L_KVOWN + (u - 8) * 5120) + l16;
        S[i] = mfma32(lds16(L, tb + hd * 1024), q0, zero);
        S[i] = mfma32(lds16(L, tb + 2048), q1, S[i]);
      } else {
#pragma unroll
        for (int r = 0; r < 16; ++r) S[i][r] = -3.0e38f;
      }
    }
#pragma unroll
    for (int r = 0; r < 16; ++r) {
      const bool le = (r & 3) + 8 * (r >> 2) + 4 * h <= r32;
      S[0][r] = le ? -3.0e38f : S[0][r];
      S[8][r] = le ? S[8][r] : -3.0e38f;
    }
    float mx = -3.0e38f;
#pragma unroll
    for (int i = 0; i < 9; ++i)
#pragma unroll
      for (int r = 0; r < 16; r += 2) mx = fmaxf(fmaxf(S[i][r], S[i][r + 1]), mx);
    mx = swap32_max(mx);
    f32x16 O = {};
    const unsigned va = blk == 0 ? 3072 + hd * 1024 + (4 * h + q4) * 32 + (p4 & 1) * 16 + (p4 >> 1) * 8 : 2048 + hd * 512 + (4 * h + q4) * 16 + 8;
    const unsigned vstep = blk == 0 ? 256 : 128;
    const bool cst = blk == 1 && p4 != 0;
#pragma unroll
    for (int i = 0; i < 9; ++i) {
      if (i >= nskip) {
        const int u = m + i;
        const unsigned tb = (u < 8 ? u * 5120 : L_KVOWN + (u - 8) * 5120);
#pragma unroll
        for (int s = 0; s < 2; ++s) {
          v4u pf;
#pragma unroll
          for (int e = 0; e < 4; ++e) pf[e] = pk2(__builtin_amdgcn_exp2f(S[i][8 * s + 2 * e] - mx), __builtin_amdgcn_exp2f(S[i][8 * s + 2 * e + 1] - mx));
          const unsigned a0 = cst ? L_ONES : tb + va + (2 * s) * vstep, a1 = cst ? L_ONES : tb + va + (2 * s + 1) * vstep;
          O = mfma32(trpair(L, a0, a1), pf, O);
        }
      }
    }
    const float inv = __builtin_amdgcn_rcpf(swap32_hi(O[8]));
    const int tok = 128 * p + 32 * m + r32;
    const unsigned ob = WS_OF + (tok >> 4) * 20480 + (tok & 15) * 16;
    const int c = 5 * j + 2 * hd + h, c2 = 5 * j + 4;
    STAMP(11);
    st16(v4u{pk2(O[0] * inv, O[1] * inv), pk2(O[2] * inv, O[3] * inv), pk2(O[4] * inv, O[5] * inv), pk2(O[6] * inv, O[7] * inv)}, rws, ob + (c >> 2) * 1024 + (c & 3) * 256);
    if (h == 0) st8(v2u{pk2(O[8] * inv, O[9] * inv), pk2(O[10] * inv, O[11] * inv)}, rws, ob + (c2 >> 2) * 1024 + (c2 & 3) * 256 + hd * 8);
  }
  if (w == 0) poll(caddr, cbase, 32u, lane >= 8 && lane < 16);
  bar();
  const int tt = 8 * p + 4 * rh + m;
  unsigned x2k[6];
  {
    const unsigned w3 = WS_WT3 + c8 * 102400 + w * 1024;
    const rsrc_t rx = __builtin_amdgcn_make_buffer_rsrc((void*)P.x, 0, 2048 * 1280, 0x00020000);
    v2u xr[3];
    const unsigned xo = (16 * tt + i16) * 1280 + (80 * c8 + 4 * g4) * 2;
    if (w != 0) {
#pragma unroll
      for (int r = 0; r < 13; ++r) dma<true>(LB + (r * 8 + w) * 1024, l16, rws, w3 + r * 8192);
      waitv<13>();
    } else waitv<0>();
    bar();
    if (w == 0) {
      if (lane == 0) __hip_atomic_fetch_add((GAS unsigned*)(P.ws + CT_C2 + (p << 7)), 1u, RLX_AGENT);
      STAMP(12);
      poll(caddr, cbase, 16u, lane == 3);
#pragma unroll
      for (int r = 0; r < 13; ++r) dma<true>(LB + (r * 8 + w) * 1024, l16, rws, w3 + r * 8192);
    }
    bar();
    STAMP(13);
    v4u af[10];
#pragma unroll
    for (int i = 0; i < 10; ++i) ld16(af[i], l16, rws, WS_OF + (tt * 20 + 2 * i + kh) * 1024);
    ld8p(xr[0], xo, rx, kh * 64); ld8p(xr[1], xo, rx, kh * 64 + 32); ld8p(xr[2], xo, rx, 128);
    f32x4 acc[5] = {};
    const unsigned wb = kh * 5120 + l16;
#pragma unroll
    for (int e = 0; e < 5; ++e) {
      switch (e) { case 0: waitv<11>(); break; case 1: waitv<9>(); break; case 2: waitv<7>(); break; case 3: waitv<5>(); break; default: waitv<3>(); }
      tie(af[2 * e]); tie(af[2 * e + 1]);
      if (e == 0) bar();
#pragma unroll
      for (int u = 0; u < 2; ++u)
#pragma unroll
        for (int t = 0; t < 5; ++t) acc[t] = mfma16(lds16(L, wb + ((2 * e + u) * 10 + t) * 1024), af[2 * e + u], acc[t]);
    }
    STAMP(14);
    const unsigned xs = 106496 + m * 5120;
    if (kh == 0) { *(LAS f32x4*)(L + xs + l16) = acc[2]; *(LAS f32x4*)(L + xs + 1024 + l16) = acc[3]; }
    else { *(LAS f32x4*)(L + xs + 2048 + l16) = acc[0]; *(LAS f32x4*)(L + xs + 3072 + l16) = acc[1]; *(LAS f32x4*)(L + xs + 4096 + l16) = acc[4]; }
    bar();
    waitv<0>();
    tie(xr[0]); tie(xr[1]); tie(xr[2]);
    f32x4 r0, r1, r2 = {};
    if (kh == 0) { r0 = acc[0] + ldsf4(L, xs + 2048 + l16); r1 = acc[1] + ldsf4(L, xs + 3072 + l16); r2 = acc[4] + ldsf4(L, xs + 4096 + l16); }
    else { r0 = acc[2] + ldsf4(L, xs + l16); r1 = acc[3] + ldsf4(L, xs + 1024 + l16); }
    const f32x4 ga0 = ldsf4(L, L_MODV + 5120 + (32 * kh + 4 * g4) * 4), ga1 = ldsf4(L, L_MODV + 5120 + (32 * kh + 16 + 4 * g4) * 4), ga2 = ldsf4(L, L_MODV + 5120 + (64 + 4 * g4) * 4);
    x2k[0] = pk2(bflo(xr[0].x) + ga0[0] * r0[0], bfhi(xr[0].x) + ga0[1] * r0[1]); x2k[1] = pk2(bflo(xr[0].y) + ga0[2] * r0[2], bfhi(xr[0].y) + ga0[3] * r0[3]);
    x2k[2] = pk2(bflo(xr[1].x) + ga1[0] * r1[0], bfhi(xr[1].x) + ga1[1] * r1[1]); x2k[3] = pk2(bflo(xr[1].y) + ga1[2] * r1[2], bfhi(xr[1].y) + ga1[3] * r1[3]);
    x2k[4] = pk2(bflo(xr[2].x) + ga2[0] * r2[0], bfhi(xr[2].x) + ga2[1] * r2[1]); x2k[5] = pk2(bflo(xr[2].y) + ga2[2] * r2[2], bfhi(xr[2].y) + ga2[3] * r2[3]);
    if (kh) { x2k[4] = 0; x2k[5] = 0; }
    float s1 = 0.f, s2 = 0.f;
#pragma unroll
    for (int e = 0; e < 6; ++e) { float a = bflo(x2k[e]), c = bfhi(x2k[e]); s1 += a + c; s2 += a * a + c * c; }
    s1 = swap32_add(swap16_add(s1)); s2 = swap32_add(swap16_add(s2));
    const unsigned xb = WS_X2F + ((tt >> 1) * 40 + 5 * c8) * 1024 + ((tt & 1) * 16 + i16) * 16;
    st16(v4u{x2k[0], x2k[1], x2k[2], x2k[3]}, rws, xb + (2 * kh + (g4 >> 1)) * 1024 + (g4 & 1) * 512);
    if (kh == 0) st8(v2u{x2k[4], x2k[5]}, rws, xb + 4096 + (g4 >> 1) * 512 + (g4 & 1) * 8);
    if (g4 == 0) st8(v2u{__builtin_bit_cast(unsigned, s1), __builtin_bit_cast(unsigned, s2)}, rws, WS_ST + (16 * tt + i16) * 128 + (2 * c8 + kh) * 8);
  }
  {
    const unsigned w4 = WS_WT4 + j * 245760 + w * 1024;
    bar();
    if (w != 0) {
#pragma unroll
      for (int r = 0; r < 15; ++r) dma<true>(LB + (r * 8 + w) * 1024, l16, rws, w4 + r * 8192);
      waitv<15>();
    } else waitv<0>();
    bar();
    if (w == 0) {
      if (lane == 0) __hip_atomic_fetch_add((GAS unsigned*)(P.ws + CT_C3 + (p << 7)), 1u, RLX_AGENT);
      STAMP(15);
      poll(caddr, cbase, 16u, lane == 4);
#pragma unroll
      for (int r = 0; r < 15; ++r) dma<true>(LB + (r * 8 + w) * 1024, l16, rws, w4 + r * 8192);
    }
    bar();
    STAMP(16);
    v4u af[20], stv[4]; unsigned uv[8];
#pragma unroll
    for (int i = 0; i < 20; ++i) ld16(af[i], l16, rws, WS_X2F + ((4 * p + m) * 40 + 20 * kh + i) * 1024);
#pragma unroll
    for (int i = 0; i < 4; ++i) ld16(stv[i], (32 * m + r32) * 128 + h * 64 + i * 16, rws, WS_ST + p * 16384);
    {
      const int ti = tid < 384 ? tid : 0;
      const unsigned uo = ((ti / 192) * 3072 + j * 192 + ti % 192) * 4;
#pragma unroll
      for (int i = 0; i < 8; ++i) ld4(uv[i], uo, rws, WS_UV + i * 24576);
    }
    f32x16 acc[6] = {};
    const unsigned wb = kh * 6144 + l16;
#pragma unroll
    for (int e = 0; e < 10; ++e) {
      constexpr int PS = 24, R0 = 15, TOT = 30;
      const int iss = issued_before(e, PS, R0, TOT), nd = need_rounds(e, PS, TOT);
      switch (nd <= R0 ? (18 - 2 * e) + 12 + (iss - R0) : iss - nd) {
        WCASE(0) WCASE(3) WCASE(6) WCASE(9) WCASE(12) WCASE(28) WCASE(29) WCASE(30) WCASE(31)
        default: waitv<0>();
      }
      tie(af[2 * e]); tie(af[2 * e + 1]);
      bar();
#pragma unroll
      for (int r = iss; r < issued_before(e + 1, PS, R0, TOT); ++r) dma<true>(LB + ((r * 8) % 120 + w) * 1024, l16, rws, w4 + r * 8192);
#pragma unroll
      for (int u = 0; u < 2; ++u)
#pragma unroll
        for (int t = 0; t < 6; ++t) acc[t] = mfma32(lds16(L, wb + (((2 * e + u) * 12) % 120 + t) * 1024), af[2 * e + u], acc[t]);
    }
    STAMP(17);
#pragma unroll
    for (int i = 0; i < 4; ++i) tie(stv[i]);
#pragma unroll
    for (int i = 0; i < 8; ++i) tie(uv[i]);
    bar();
    {
      float s = 0.f;
#pragma unroll
      for (int i = 0; i < 8; ++i) s += __builtin_bit_cast(float, uv[i]);
      if (tid < 384) *(LAS float*)(L + L_UVS + tid * 4) = s;
    }
#pragma unroll
    for (int t = 0; t < 3; ++t) {
      const f32x16 sd = kh ? acc[t] : acc[3 + t];
#pragma unroll
      for (int qd = 0; qd < 4; ++qd) *(LAS f32x4*)(L + w * 12288 + (t * 4 + qd) * 1024 + l16) = f32x4{sd[4 * qd], sd[4 * qd + 1], sd[4 * qd + 2], sd[4 * qd + 3]};
    }
    float s1 = 0.f, s2 = 0.f;
#pragma unroll
    for (int i = 0; i < 4; ++i) { f32x4 v = __builtin_bit_cast(f32x4, stv[i]); s1 += v[0] + v[2]; s2 += v[1] + v[3]; }
    s1 = swap32_add(s1); s2 = swap32_add(s2);
    const float mean = s1 * (1.f / 640.f);
    const float rstd = __builtin_amdgcn_rsqf(fmaxf(s2 * (1.f / 640.f) - mean * mean, 0.f) + 1e-5f), nm = -mean * rstd;
    bar();
    const int tok = 128 * p + 32 * m + r32;
    const unsigned ab = WS_ACT + ((tok >> 4) * 48 + 3 * j) * 1024 + h * 256 + (tok & 15) * 16;
#pragma unroll
    for (int t = 0; t < 3; ++t) {
      f32x16 kp = kh ? acc[3 + t] : acc[t];
      const int tg = 3 * kh + t;
      float a[8];
#pragma unroll
      for (int qd = 0; qd < 4; ++qd) {
        const f32x4 o = ldsf4(L, (w ^ 4) * 12288 + (t * 4 + qd) * 1024 + l16);
        const f32x4 uu = ldsf4(L, L_UVS + (tg * 32 + 8 * qd + 4 * h) * 4), vv = ldsf4(L, L_UVS + 768 + (tg * 32 + 8 * qd + 4 * h) * 4);
#pragma unroll
        for (int e = 0; e < 4; ++e) kp[4 * qd + e] = fmaf(kp[4 * qd + e] + o[e], rstd, fmaf(nm, uu[e], vv[e]));
      }
#pragma unroll
      for (int e = 0; e < 8; ++e) { const float gv = kp[e]; a[e] = gv * __builtin_amdgcn_rcpf(1.f + __builtin_amdgcn_exp2f(-1.44269504f * gv)) * kp[8 + e]; }
      st16(v4u{pk2(a[0], a[1]), pk2(a[2], a[3]), pk2(a[4], a[5]), pk2(a[6], a[7])}, rws, ab + (tg >> 1) * 1024 + (tg & 1) * 512);
    }
  }
  {
    const unsigned w5 = WS_WT5 + c8 * 245760 + w * 1024;
    bar();
    if (w != 0) {
#pragma unroll
      for (int r = 0; r < 15; ++r) dma<true>(LB + (r * 8 + w) * 1024, l16, rws, w5 + r * 8192);
      waitv<15>();
    } else waitv<0>();
    bar();
    if (w == 0) {
      if (lane == 0) __hip_atomic_fetch_add((GAS unsigned*)(P.ws + CT_C4 + (p << 7)), 1u, RLX_AGENT);
      STAMP(18);
      poll(caddr, cbase, 16u, lane == 5);
#pragma unroll
      for (int r = 0; r < 15; ++r) dma<true>(LB + (r * 8 + w) * 1024, l16, rws, w5 + r * 8192);
    }
    bar();
    STAMP(19);
    v4u af[24];
#pragma unroll
    for (int i = 0; i < 24; ++i) ld16(af[i], l16, rws, WS_ACT + (tt * 48 + 2 * i + kh) * 1024);
    f32x4 acc[5] = {};
    const unsigned wb = kh * 5120 + l16;
#pragma unroll
    for (int e = 0; e < 12; ++e) {
      constexpr int PS = 20, R0 = 15, TOT = 30;
      const int iss = issued_before(e, PS, R0, TOT), nd = need_rounds(e, PS, TOT);
      switch (nd <= R0 ? (22 - 2 * e) + (iss - R0) : iss - nd) {
        WCASE(0) WCASE(1) WCASE(2) WCASE(3) WCASE(4) WCASE(5) WCASE(6) WCASE(7) WCASE(8) WCASE(9) WCASE(10) WCASE(11) WCASE(12) WCASE(13) WCASE(14) WCASE(15) WCASE(16) WCASE(17)
        WCASE(18) WCASE(19) WCASE(20) WCASE(21) WCASE(22) WCASE(23)
        default: waitv<0>();
      }
      tie(af[2 * e]); tie(af[2 * e + 1]);
      bar();
#pragma unroll
      for (int r = iss; r < issued_before(e + 1, PS, R0, TOT); ++r) dma<true>(LB + ((r * 8) % 120 + w) * 1024, l16, rws, w5 + r * 8192);
#pragma unroll
      for (int u = 0; u < 2; ++u)
#pragma unroll
        for (int t = 0; t < 5; ++t) acc[t] = mfma16(lds16(L, wb + (((2 * e + u) * 10) % 120 + t) * 1024), af[2 * e + u], acc[t]);
    }
    STAMP(20);
    const unsigned xs = L_BSRC + m * 5120;
    if (kh == 0) { *(LAS f32x4*)(L + xs + l16) = acc[2]; *(LAS f32x4*)(L + xs + 1024 + l16) = acc[3]; }
    else { *(LAS f32x4*)(L + xs + 2048 + l16) = acc[0]; *(LAS f32x4*)(L + xs + 3072 + l16) = acc[1]; *(LAS f32x4*)(L + xs + 4096 + l16) = acc[4]; }
    bar();
    f32x4 r0, r1, r2 = {};
    if (kh == 0) { r0 = acc[0] + ldsf4(L, xs + 2048 + l16); r1 = acc[1] + ldsf4(L, xs + 3072 + l16); r2 = acc[4] + ldsf4(L, xs + 4096 + l16); }
    else { r0 = acc[2] + ldsf4(L, xs + l16); r1 = acc[3] + ldsf4(L, xs + 1024 + l16); }
    const f32x4 g0 = ldsf4(L, L_MODV + 5440 + (32 * kh + 4 * g4) * 4), g1 = ldsf4(L, L_MODV + 5440 + (32 * kh + 16 + 4 * g4) * 4), g2 = ldsf4(L, L_MODV + 5440 + (64 + 4 * g4) * 4);
    GAS char* o = (GAS char*)P.out + (size_t)(16 * tt + i16) * 1280 + (80 * c8 + 4 * g4) * 2;
    *(GAS v2u*)(o + kh * 64) = v2u{pk2(bflo(x2k[0]) + g0[0] * r0[0], bfhi(x2k[0]) + g0[1] * r0[1]), pk2(bflo(x2k[1]) + g0[2] * r0[2], bfhi(x2k[1]) + g0[3] * r0[3])};
    *(GAS v2u*)(o + kh * 64 + 32) = v2u{pk2(bflo(x2k[2]) + g1[0] * r1[0], bfhi(x2k[2]) + g1[1] * r1[1]), pk2(bflo(x2k[3]) + g1[2] * r1[2], bfhi(x2k[3]) + g1[3] * r1[3])};
    if (kh == 0) *(GAS v2u*)(o + 128) = v2u{pk2(bflo(x2k[4]) + g2[0] * r2[0], bfhi(x2k[4]) + g2[1] * r2[1]), pk2(bflo(x2k[5]) + g2[2] * r2[2], bfhi(x2k[5]) + g2[3] * r2[3])};
    if (b == 0 && tid == 0) __hip_atomic_store((GAS unsigned*)(P.ws + CT_E), cbase + 1u, RLX_AGENT);
    STAMP(21);
  }
}

extern "C" void kernel_launch(void* const* d_in, const int* in_sizes, int n_in, void* d_out, int out_size, void* d_ws, size_t ws_size,
                              hipStream_t stream) {
  Params P;
  P.x = (const u16*)d_in[0]; P.wqkv = (const u16*)d_in[1]; P.wo = (const u16*)d_in[2];
  P.wg = (const u16*)d_in[4]; P.wu = (const u16*)d_in[5]; P.wd = (const u16*)d_in[6];
  P.c = (const u16*)d_in[7]; P.wmod = (const u16*)d_in[8];
  P.out = (u16*)d_out; P.ws = (char*)d_ws;
  fused_block<<<256, 512, 0, stream>>>(P);
}
```

```cpp
#include <hip/hip_runtime.h>
#include <hip/hip_bf16.h>
#include <utility>

typedef unsigned short u16;
typedef unsigned v4u __attribute__((ext_vector_type(4)));
typedef unsigned v2u __attribute__((ext_vector_type(2)));
typedef short v4s __attribute__((ext_vector_type(4)));
typedef float f32x2 __attribute__((ext_vector_type(2)));
typedef float f32x4 __attribute__((ext_vector_type(4)));
typedef float f32x16 __attribute__((ext_vector_type(16)));
typedef __bf16 bf16x2 __attribute__((ext_vector_type(2)));
typedef __bf16 bf16x8 __attribute__((ext_vector_type(8)));
typedef __amdgpu_buffer_rsrc_t rsrc_t;
#define LAS __attribute__((address_space(3)))
#define GAS __attribute__((address_space(1)))
#define DI __device__ __forceinline__
#define RLX_AGENT __ATOMIC_RELAXED, __HIP_MEMORY_SCOPE_AGENT

#define STAMPS 0

constexpr int WS_CTRL = 0;
constexpr int WS_MODP = 65536;
constexpr int WS_UV   = 131072;
constexpr int WS_ST   = 327680;
constexpr int WS_WT1  = 655360;
constexpr int WS_WT3  = WS_WT1 + 16 * 163840;
constexpr int WS_WT4  = WS_WT3 + 8 * 102400;
constexpr int WS_WT5  = WS_WT4 + 16 * 245760;
constexpr int WS_KV   = WS_WT5 + 8 * 245760;
constexpr int WS_OF   = WS_KV + 64 * 16 * 5120;
constexpr int WS_X2F  = WS_OF + 128 * 20 * 1024;
constexpr int WS_ACT  = WS_X2F + 64 * 40 * 1024;
constexpr int WS_XH   = WS_ACT + 128 * 48 * 1024;
constexpr int WS_END  = WS_XH + 64 * 40 * 1024;
constexpr int WS_DBG  = WS_END;
constexpr int CT_E = 0, CT_FLAG = 4096  , CT_C1 = 16384  , CT_C2 = 49152, CT_C3 = CT_C2 + 2048,
              CT_C4 = CT_C3 + 2048, CT_CB = CT_C4 + 2048  , CT_CW = CT_CB + 1024;
constexpr int L_WQ = 0, L_RED = 10240;
constexpr int L_BSRC = 122880;
constexpr int L_MODV = 155648;
constexpr int L_CNT = 162816;
constexpr int L_UVS = 155648;
constexpr int KVT = 6144;
constexpr int L_Q = 73728, L_QHI = 81920, L_X1 = 86016, L_W3A = 86016, L_X3 = 40960;
constexpr unsigned SPIN_CAP = 1u << 20;

#if defined(__HIP_DEVICE_COMPILE__)
#pragma clang attribute push (__attribute__((target("no-packed-fp32-ops"))), apply_to = function)
#endif
struct Params { const u16 *x, *wqkv, *wo, *wg, *wu, *wd, *c, *wmod; u16* out; char* ws; };

DI f32x16 mfma32(v4u a, v4u b, f32x16 c) { return __builtin_amdgcn_mfma_f32_32x32x16_bf16(__builtin_bit_cast(bf16x8, a), __builtin_bit_cast(bf16x8, b), c, 0, 0, 0); }
DI f32x4 mfma16(v4u a, v4u b, f32x4 c) { return __builtin_amdgcn_mfma_f32_16x16x32_bf16(__builtin_bit_cast(bf16x8, a), __builtin_bit_cast(bf16x8, b), c, 0, 0, 0); }
DI unsigned pk2(float a, float b) { f32x2 v = {a, b}; return __builtin_bit_cast(unsigned, __builtin_convertvector(v, bf16x2)); }
DI float bflo(unsigned x) { return __builtin_bit_cast(float, x << 16); }
DI float bfhi(unsigned x) { return __builtin_bit_cast(float, x & 0xffff0000u); }
DI float bfr(float a) { return bflo(pk2(a, a)); }
DI f32x2 swp32(float v) {
  unsigned a = __builtin_bit_cast(unsigned, v), c = a; asm("" : "+v"(c));
  auto r = __builtin_amdgcn_permlane32_swap(a, c, false, false);
  unsigned r0 = r[0], r1 = r[1];
  return f32x2{__builtin_bit_cast(float, r0), __builtin_bit_cast(float, r1)};
}
DI f32x2 swp16(float v) {
  unsigned a = __builtin_bit_cast(unsigned, v), c = a; asm("" : "+v"(c));
  auto r = __builtin_amdgcn_permlane16_swap(a, c, false, false);
  unsigned r0 = r[0], r1 = r[1];
  return f32x2{__builtin_bit_cast(float, r0), __builtin_bit_cast(float, r1)};
}
DI float swap32_add(float v) { f32x2 r = swp32(v); return r.x + r.y; }
DI float swap16_add(float v) { f32x2 r = swp16(v); return r.x + r.y; }
DI float swap32_max(float v) { f32x2 r = swp32(v); return fmaxf(r.x, r.y); }
DI float swap32_hi(float v) { return swp32(v).y; }
DI bool all64(bool c) { return __builtin_amdgcn_ballot_w64(!c) == 0ull; }
DI float ror8_add(float v) { return v + __builtin_bit_cast(float, __builtin_amdgcn_update_dpp(0, __builtin_bit_cast(int, v), 0x128, 0xf, 0xf, true)); }

template <bool SC1> DI void dma(unsigned lds_addr, unsigned voff, rsrc_t rs, unsigned soff) {
  if (SC1) asm volatile("s_mov_b32 m0, %0\n\ts_nop 0\n\tbuffer_load_dwordx4 %1, %2, %3 offen sc1 lds" ::"s"(lds_addr), "v"(voff), "s"(rs), "s"(soff) : "memory");
  else     asm volatile("s_mov_b32 m0, %0\n\ts_nop 0\n\tbuffer_load_dwordx4 %1, %2, %3 offen lds" ::"s"(lds_addr), "v"(voff), "s"(rs), "s"(soff) : "memory");
}
DI void ld16(v4u& d, unsigned voff, rsrc_t rs, unsigned soff) { asm volatile("buffer_load_dwordx4 %0, %1, %2, %3 offen sc1" : "=v"(d) : "v"(voff), "s"(rs), "s"(soff) : "memory"); }
DI void ld16p(v4u& d, unsigned voff, rsrc_t rs, unsigned soff) { asm volatile("buffer_load_dwordx4 %0, %1, %2, %3 offen" : "=v"(d) : "v"(voff), "s"(rs), "s"(soff) : "memory"); }
DI void ld8p(v2u& d, unsigned voff, rsrc_t rs, unsigned soff) { asm volatile("buffer_load_dwordx2 %0, %1, %2, %3 offen" : "=v"(d) : "v"(voff), "s"(rs), "s"(soff) : "memory"); }
DI void ld4(unsigned& d, unsigned voff, rsrc_t rs, unsigned soff) { asm volatile("buffer_load_dword %0, %1, %2, %3 offen sc1" : "=v"(d) : "v"(voff), "s"(rs), "s"(soff) : "memory"); }
template <int N> DI void waitv() { asm volatile("s_waitcnt vmcnt(%0)" ::"n"(N) : "memory"); }
template <class T> DI void tie(T& a) { asm volatile("" : "+v"(a)); }
DI void rbar() { asm volatile("s_barrier" ::: "memory"); }
DI void bar() { asm volatile("s_waitcnt lgkmcnt(0)\n\ts_barrier" ::: "memory"); }
DI void st16(v4u v, rsrc_t rs, unsigned off) { __builtin_amdgcn_raw_buffer_store_b128(v, rs, off, 0, 16); }
DI void st8(v2u v, rsrc_t rs, unsigned off) { __builtin_amdgcn_raw_buffer_store_b64(v, rs, off, 0, 16); }
DI v4u trpair(LAS char* L, unsigned a0, unsigned a1) {
  v4s lo = __builtin_amdgcn_ds_read_tr16_b64_v4i16((LAS v4s*)(L + a0));
  v4s hi = __builtin_amdgcn_ds_read_tr16_b64_v4i16((LAS v4s*)(L + a1));
  v2u l = __builtin_bit_cast(v2u, lo), h = __builtin_bit_cast(v2u, hi);
  return v4u{l.x, l.y, h.x, h.y};
}
DI v4u lds16(LAS char* L, unsigned a) { return *(LAS v4u*)(L + a); }
DI f32x4 ldsf4(LAS char* L, unsigned a) { return *(LAS f32x4*)(L + a); }

template <int... I, class F> DI void sfor_impl(std::integer_sequence<int, I...>, F&& f) { (f(std::integral_constant<int, I>{}), ...); }
template <int N, class F> DI void sfor(F&& f) { sfor_impl(std::make_integer_sequence<int, N>{}, f); }
#define SB() __builtin_amdgcn_sched_barrier(0)

template <int N> using IC = std::integral_constant<int, N>;
DI float dot2(unsigned a, unsigned b, float c) { return __builtin_amdgcn_fdot2_f32_bf16(__builtin_bit_cast(bf16x2, a), __builtin_bit_cast(bf16x2, b), c, false); }
DI f32x2 xswp32(float a, float c) { auto r = __builtin_amdgcn_permlane32_swap(__builtin_bit_cast(unsigned, a), __builtin_bit_cast(unsigned, c), false, false); unsigned r0 = r[0], r1 = r[1]; return f32x2{__builtin_bit_cast(float, r0), __builtin_bit_cast(float, r1)}; }
DI f32x2 xswp16(float a, float c) { auto r = __builtin_amdgcn_permlane16_swap(__builtin_bit_cast(unsigned, a), __builtin_bit_cast(unsigned, c), false, false); unsigned r0 = r[0], r1 = r[1]; return f32x2{__builtin_bit_cast(float, r0), __builtin_bit_cast(float, r1)}; }

template <class S> constexpr int sch_tot(int e) { int t = S::A0; for (int i = 0; i < e; ++i) t += S::nA(i) + S::nW(i) + S::nX(i); return t; }
template <class S> constexpr int sch_W(int e) { int t = S::R0; for (int i = 0; i < e; ++i) t += S::nW(i); return t; }
template <class S> constexpr int sch_A(int e) { int t = S::A0; for (int i = 0; i < e; ++i) t += S::nA(i); return t; }
template <class S> constexpr int sch_posA(int a) { if (a < S::A0) return a; for (int i = 0;; ++i) if (a < sch_A<S>(i + 1)) return sch_tot<S>(i) + (a - sch_A<S>(i)); }
template <class S> constexpr int sch_posW(int r) { if (r < S::R0) return r - S::R0; for (int i = 0;; ++i) if (r < sch_W<S>(i + 1)) return sch_tot<S>(i) + S::nA(i) + (r - sch_W<S>(i)); }
template <class S> constexpr int sch_wait(int e) {
  int nd = (S::PS * (e + 1) + S::LOOK + 3) / 4; if (nd > S::TOT) nd = S::TOT;
  int pw = sch_posW<S>(nd - 1), na = S::APS * (e + 1) - 1 + S::ALOOK, pa = sch_posA<S>(na < S::NA ? na : S::NA - 1);
  return sch_tot<S>(e) - 1 - (pw > pa ? pw : pa);
}
constexpr int cmin(int a, int b) { return a < b ? a : b; }
template <int PS_, int R0_, int RATE, int TOT_> struct Ring {
  static constexpr int upto(int e) { int v = R0_; for (int i = 0; i <= e; ++i) v = cmin(cmin(TOT_, (PS_ * i + 60) / 4), v + RATE); return v; }
  static constexpr int n(int e) { return upto(e) - (e ? upto(e - 1) : R0_); } };
struct S1 { static constexpr int PS = 8, LOOK = 4, R0 = 3, TOT = 20, A0 = 3, APS = 2, ALOOK = 1, NA = 20;
  static constexpr int nA(int e) { return e < 2 ? 4 : e < 6 ? 2 : e == 6 ? 1 : 0; } static constexpr int nW(int e) { return e < 2 ? 4 : e < 6 ? 2 : e == 6 ? 1 : 0; } static constexpr int nX(int) { return 0; } };
struct S4 { static constexpr int PS = 12, LOOK = 4, R0 = 10, TOT = 30, A0 = 6, APS = 2, ALOOK = 0, NA = 20;
  static constexpr int nA(int e) { return e < 7 ? 2 : 0; } static constexpr int nW(int e) { return Ring<PS, R0, 4, TOT>::n(e); }
  static constexpr int nX(int e) { return e == 4 ? 4 : e == 5 ? 6 : e == 6 ? 5 : e > 6 ? 3 : 0; } };
struct S5 { static constexpr int PS = 10, LOOK = 6, R0 = 15, TOT = 30, A0 = 10, APS = 2, ALOOK = 0, NA = 24;
  static constexpr int nA(int e) { return e < 7 ? 2 : 0; } static constexpr int nW(int e) { return Ring<PS, R0, 3, TOT>::n(e); } static constexpr int nX(int) { return 0; } };
template <class S> constexpr int sch_X(int e) { int t = 0; for (int i = 0; i < e; ++i) t += S::nX(i); return t; }

#define STAMP(i)

DI void poll(const GAS unsigned* addr, unsigned base, unsigned want, bool active) {
  for (unsigned s = 0; s < SPIN_CAP; ++s) {
    unsigned v = __hip_atomic_load(addr, RLX_AGENT);
    if (all64(!active || (v - base) >= want)) break;
  }
  __builtin_amdgcn_fence(__ATOMIC_ACQUIRE, "wavefront");
}
DI unsigned lds_arrive(LAS char* L, int off, int lane) {
  unsigned o = 0;
  asm volatile("s_waitcnt lgkmcnt(0)" ::: "memory");
  if (lane == 0) o = __hip_atomic_fetch_add((LAS unsigned*)(L + off), 1u, __ATOMIC_RELAXED, __HIP_MEMORY_SCOPE_WORKGROUP);
  o = __builtin_amdgcn_readfirstlane(o);
  asm volatile("" ::: "memory");
  return o;
}
DI void lds_wait(LAS char* L, int off, unsigned want) {
  for (unsigned s = 0; s < SPIN_CAP && *(volatile LAS unsigned*)(L + off) < want; ++s) __builtin_amdgcn_s_sleep(1);
  asm volatile("" ::: "memory");
}

__global__ __launch_bounds__(512, 2) void fused_block(Params P) {
  __shared__ __attribute__((aligned(16))) char lds_raw[163840];
  LAS char* L = (LAS char*)lds_raw;
  const unsigned LB = (unsigned)(size_t)L;
  const int tid = threadIdx.x, lane = tid & 63;
  const int w = __builtin_amdgcn_readfirstlane(tid >> 6);
  const int b = blockIdx.x;
  const int p = 4 * ((b & 7) >> 1) + ((b >> 3) & 3), j = 8 * (b & 1) + (b >> 5);
  const int m = w & 3, kh = w >> 2, h = lane >> 5, r32 = lane & 31;
  const int g4 = lane >> 4, i16 = lane & 15, q4 = (lane & 15) >> 2, p4 = lane & 3, blk = (lane >> 4) & 1;
  const rsrc_t rws = __builtin_amdgcn_make_buffer_rsrc((void*)P.ws, 0, WS_END, 0x00020000);
  const rsrc_t rx = __builtin_amdgcn_make_buffer_rsrc((void*)P.x, 0, 2048 * 1280, 0x00020000);
  const unsigned l16 = lane * 16;
  const unsigned ringL = kh * 61440 + l16, ringD = LB + kh * 61440 + m * 1024;
  STAMP(0);
  if (tid < 2) *(LAS v4u*)(L + L_CNT + tid * 16) = v4u{0u, 0u, 0u, 0u};
  bar();
  unsigned cbase = 0;
  const GAS unsigned* caddr = (const GAS unsigned*)P.ws;
  if (__builtin_expect(w < 4, 1)) {
    const int bj = b < 240 ? b : 0;
    const int cc = bj % 60, kq = bj / 60, s = bj / 6, q = bj % 6;
    v4u wm[5]; u16 cv[5]; v4u wq[3];
    const GAS char* pm = (const GAS char*)P.wmod + (size_t)(160 * kq + (tid >> 3)) * 7680 + cc * 128 + (tid & 7) * 16;
    const GAS u16* pc = (const GAS u16*)P.c + 160 * kq + (tid >> 3);
#pragma unroll
    for (int i = 0; i < 5; ++i) { wm[i] = *(const GAS v4u*)(pm + i * 245760); cv[i] = pc[32 * i]; }
    const int lr = (lane * 205) >> 11, lc = lane - 10 * lr;
    const GAS char* pq = (const GAS char*)P.wqkv + (size_t)(16 * s + (lr < 6 ? lr : 5)) * 3840 + q * 640 + w * 160 + lc * 16;
    wq[0] = *(const GAS v4u*)pq; wq[1] = *(const GAS v4u*)(pq + 6 * 3840); wq[2] = *(const GAS v4u*)(pq + (lr < 4 ? 12 : 10) * 3840);
    asm volatile("" ::: "memory");
    if (w == 0) {
      int off = CT_E;
      if (lane == 1) off = CT_C1 + (((p >= 1 ? p - 1 : 0) * 16 + j) << 7);
      if (lane == 2) off = CT_C1 + (((p >= 2 ? p - 2 : 0) * 16 + j) << 7);
      if (lane == 3) off = CT_C2 + (p << 7);
      if (lane == 4) off = CT_C3 + (p << 7);
      if (lane == 5) off = CT_C4 + (p << 7);
      if (lane == 6) off = CT_CW;
      if (lane >= 8 && lane < 16) off = CT_CB + ((lane - 8) << 7);
      caddr = (const GAS unsigned*)(P.ws + off);
    }
    cbase = __hip_atomic_load(caddr, RLX_AGENT);
    if (b < 240) {
      float acc[8] = {0, 0, 0, 0, 0, 0, 0, 0};
#pragma unroll
      for (int i = 0; i < 5; ++i) {
        float cf = bflo(cv[i]);
        float sv = bfr(cf * __builtin_amdgcn_rcpf(1.f + __builtin_amdgcn_exp2f(-1.44269504f * cf)));
#pragma unroll
        for (int e = 0; e < 4; ++e) { acc[2 * e] += sv * bflo(wm[i][e]); acc[2 * e + 1] += sv * bfhi(wm[i][e]); }
      }
      float u[4], v[2];
#pragma unroll
      for (int e = 0; e < 4; ++e) { f32x2 r = xswp32(acc[e], acc[e + 4]); u[e] = r.x + r.y; }
#pragma unroll
      for (int e = 0; e < 2; ++e) { f32x2 r = xswp16(u[e], u[e + 2]); v[e] = ror8_add(r.x + r.y); }
      if (!(lane & 8)) *(LAS f32x2*)(L + L_RED + w * 256 + (lane & 7) * 32 + g4 * 8) = f32x2{v[0], v[1]};
      const unsigned wql = L_WQ + w * 2560;
      if (lr < 6) { *(LAS v4u*)(L + wql + l16) = wq[0]; *(LAS v4u*)(L + wql + 960 + l16) = wq[1]; }
      if (lr < 4) *(LAS v4u*)(L + wql + 1920 + l16) = wq[2];
      if (lds_arrive(L, L_CNT, lane) == 3 && lane < 16) {
        f32x4 t = (ldsf4(L, L_RED + l16) + ldsf4(L, L_RED + 256 + l16)) + (ldsf4(L, L_RED + 512 + l16) + ldsf4(L, L_RED + 768 + l16));
        st16(__builtin_bit_cast(v4u, t), rws, WS_MODP + (kq * 3840 + cc * 64 + lane * 4) * 4);
      }
      const int part = q >> 1, jb = 8 * (q & 1) + 2 * w;
      const int tmain = part == 0 ? 3 : part - 1;
      const unsigned dst0 = WS_WT1 + jb * 163840 + (s >= 20 ? 81920 + (s - 20) * 4096 : s * 4096);
      const unsigned rowa = wql + (8 * h + q4) * 160;
      const unsigned a = rowa + (20 * blk + 4 * p4) * 2, a2 = rowa + (40 * blk + 16 + 20 * (p4 & 1)) * 2;
      v4u f0 = trpair(L, a, a + 640), f1 = trpair(L, a + 80, a + 720), f2 = trpair(L, a2, a2 + 640);
      st16(f0, rws, dst0 + tmain * 1024 + l16);
      st16(f1, rws, dst0 + 163840 + tmain * 1024 + l16);
      if (i16 >= 8) f2 = v4u{0, 0, 0, 0};
      const int r = (part == 0 ? 16 : part == 1 ? 0 : 8) + i16;
      if (part == 0 || i16 < 8) st16(f2, rws, dst0 + blk * 163840 + 2048 + (h * 32 + r) * 16);
    }
  } else if (w == 4) {
    const int r8 = lane & 7, cg = lane >> 3, tok = 8 * b + r8;
    const GAS char* px = (const GAS char*)P.x + (size_t)tok * 1280 + cg * 16;
    v4u xv[10];
#pragma unroll
    for (int i = 0; i < 10; ++i) xv[i] = *(const GAS v4u*)(px + i * 128);
    cbase = __hip_atomic_load(caddr, RLX_AGENT);
    float s1 = 0.f, s2 = 0.f, s3 = 0.f, s4 = 0.f;
#pragma unroll
    for (int i = 0; i < 10; ++i) {
      s1 = dot2(xv[i][0], 0x3f803f80u, s1); s2 = dot2(xv[i][0], xv[i][0], s2); s3 = dot2(xv[i][1], 0x3f803f80u, s3); s4 = dot2(xv[i][1], xv[i][1], s4);
      s1 = dot2(xv[i][2], 0x3f803f80u, s1); s2 = dot2(xv[i][2], xv[i][2], s2); s3 = dot2(xv[i][3], 0x3f803f80u, s3); s4 = dot2(xv[i][3], xv[i][3], s4);
    }
    s1 = swap32_add(swap16_add(ror8_add(s1 + s3))); s2 = swap32_add(swap16_add(ror8_add(s2 + s4)));
    const float mean = s1 * (1.f / 640.f);
    const float rstd = __builtin_amdgcn_rsqf(fmaxf(s2 * (1.f / 640.f) - mean * mean, 0.f) + 1e-5f), nm = -mean * rstd;
    const unsigned xo = WS_XH + (tok >> 5) * 40960 + cg * 512 + (tok & 31) * 16;
#pragma unroll
    for (int i = 0; i < 10; ++i) {
      v4u o;
#pragma unroll
      for (int e = 0; e < 4; ++e) o[e] = pk2(fmaf(bflo(xv[i][e]), rstd, nm), fmaf(bfhi(xv[i][e]), rstd, nm));
      st16(o, rws, xo + i * 4096);
    }
  } else {
    const int v = w - 5;
    unsigned d = 0;
    if (w == 7) {
      const GAS char* t = lane < 16 ? (const GAS char*)P.ws + lane * 2000000 : lane < 18 ? (const GAS char*)P.out + (lane - 16) * 2000000 : (const GAS char*)P.ws;
      asm volatile("global_load_dword %0, %1, off" : "=v"(d) : "v"(t) : "memory");
    }
    const char* src; int np, cpr, rstride;
    if (b < 128) { src = (const char*)((b >> 6) ? P.wu : P.wg) + (size_t)(80 * ((b >> 3) & 7)) * 3072 + (b & 7) * 384; cpr = 24; np = 30; rstride = 3072; }
    else if (b < 224) { src = (const char*)P.wd + (size_t)(32 * ((b - 128) >> 1)) * 1280 + (b & 1) * 640; cpr = 40; np = 20; rstride = 1280; }
    else { src = (const char*)P.wo + (size_t)(160 * ((b - 224) >> 3)) * 1280 + (b & 7) * 160; cpr = 10; np = 25; rstride = 1280; }
    const rsrc_t rb = __builtin_amdgcn_make_buffer_rsrc((void*)src, 0, 0x00400000, 0x00020000);
    int row = (v * 64 + lane) / cpr, cl = (v * 64 + lane) % cpr;
    const int dr = 192 / cpr, dc = 192 % cpr;
    for (int i = v; i < np; i += 3) {
      dma<false>(LB + L_BSRC + i * 1024, row * rstride + cl * 16, rb, 0);
      row += dr; cl += dc; if (cl >= cpr) { cl -= cpr; ++row; }
    }
    waitv<0>();
    tie(d);
    if (b >= 128) {
      lds_arrive(L, L_CNT + 8, lane);
      lds_wait(L, L_CNT + 8, 3u);
      if (b < 224) {
        const int st = (b - 128) >> 1, ch = b & 1;
        for (int tt = v; tt < 20; tt += 3) {
          const unsigned a = L_BSRC + (16 * (g4 >> 1) + 4 * (g4 & 1) + q4) * 640 + (16 * tt + 4 * p4) * 2;
          v4u f = trpair(L, a, a + 8 * 640);
          st16(f, rws, WS_WT5 + (4 * ch + tt / 5) * 245760 + (st & 1) * 122880 + ((st >> 1) * 5 + tt % 5) * 1024 + l16);
        }
      } else {
        const int kr = (b - 224) >> 3, cw = b & 7;
        for (int u = v; u < 25; u += 3) {
          const int sl = u / 5, t = u % 5, c = 4 * sl + g4, jl = c / 5, ii = c % 5;
          const int r0 = ii < 4 ? 40 * jl + 20 * (ii >> 1) + 4 * (ii & 1) : 40 * jl + 16;
          const int r1 = ii < 4 ? r0 + 8 : r0 + 20;
          const unsigned ca = L_BSRC + q4 * 160 + (16 * t + 4 * p4) * 2;
          v4u f = trpair(L, ca + r0 * 160, ca + r1 * 160);
          const int s = 5 * kr + sl;
          st16(f, rws, WS_WT3 + cw * 102400 + (s & 1) * 51200 + ((s >> 1) * 5 + t) * 1024 + l16);
        }
      }
      waitv<0>();
    }
  }
  const int c8 = 4 * (b & 1) + ((b >> 5) & 3), rh = b >> 7;
  int col = 4 * tid;
  if (tid >= 320) col = 1280 + 80 * c8 + 4 * (tid - 320);
  if (tid >= 340) col = 3200 + 80 * c8 + 4 * (tid - 340);
  if (tid >= 360) col = 2560 + 80 * ((b >> 3) & 7) + 4 * (tid - 360);
  if (tid >= 380) col = 1920 + 80 * ((b >> 3) & 7) + 4 * (tid - 380);
  if (tid >= 400) col = 0;
  unsigned mdst = L_MODV + (tid < 160 ? tid * 32 + 16 : tid < 320 ? (tid - 160) * 32 : tid * 16);
  col *= 4; tie(col); tie(mdst);
  if (w <= 4) {
    const unsigned tag = __builtin_amdgcn_readfirstlane(cbase) + 1u;
    waitv<0>();
    if (lds_arrive(L, L_CNT + 4, lane) == 4 && lane < 8) __hip_atomic_store((GAS unsigned*)(P.ws + CT_FLAG + lane * 1024 + b * 4), tag, RLX_AGENT);
    STAMP(1);
    if (w == 4) {
      for (unsigned s = 0; s < SPIN_CAP; ++s) {
        v4u f; ld16(f, l16, rws, CT_FLAG + (b & 7) * 1024); waitv<0>(); tie(f);
        if (all64(f.x == tag && f.y == tag && f.z == tag && f.w == tag)) break;
        __builtin_amdgcn_s_sleep(2);
      }
      __builtin_amdgcn_fence(__ATOMIC_ACQUIRE, "wavefront");
    }
  }
  bar();
  STAMP(4);

  {
    v4u mp[4], xf[20];
    sfor<4>([&](auto I) { ld16(mp[I], col, rws, WS_MODP + I * 15360); });
    const unsigned w1 = WS_WT1 + j * 163840 + kh * 81920 + m * 1024;
    const unsigned ab = WS_XH + ((4 * p + m) * 40 + 20 * kh) * 1024;
    sfor<S1::R0>([&](auto R) { dma<true>(ringD + R * 4096, l16, rws, w1 + R * 4096); });
    sfor<S1::A0>([&](auto I) { ld16(xf[I], l16, rws, ab + I * 1024); });
    if (tid == 320 && b >= 128) __hip_atomic_fetch_add((GAS unsigned*)(P.ws + (b < 224 ? CT_CB + ((b & 7) << 7) : CT_CW)), 1u, RLX_AGENT);
    waitv<S1::R0 + S1::A0>();
    sfor<4>([&](auto I) { tie(mp[I]); });
    {
      f32x4 v = (__builtin_bit_cast(f32x4, mp[0]) + __builtin_bit_cast(f32x4, mp[1])) + (__builtin_bit_cast(f32x4, mp[2]) + __builtin_bit_cast(f32x4, mp[3]));
      const bool isc = (tid >= 160 && tid < 320) || (tid >= 360 && tid < 380);
#pragma unroll
      for (int e = 0; e < 4; ++e) { float t = bfr(v[e]); v[e] = isc ? bfr(1.f + t) : t; }
      if (tid < 400) *(LAS f32x4*)(L + mdst) = v;
    }
    bar();
    STAMP(5);
    f32x16 acc[4] = {};
    const unsigned wbK = ringL + kh * 1024, wbS = ringL + 1024 - kh * 1024;
    unsigned cb = L_MODV + (80 * kh + 2 * h) * 32; tie(cb);
    constexpr int D = 4;
    v4u wf[80], hf[20]; f32x4 ca[40], cs[40];
    auto rdw = [&](auto N) { wf[N] = lds16(L, ((N % 4) < 2 ? wbK : wbS) + (((N / 4) * 4) % 60 + (N & 1) * 2) * 1024); };
    auto rdc = [&](auto R) { ca[R] = ldsf4(L, cb + (R / 2) * 128 + (R & 1) * 32); cs[R] = ldsf4(L, cb + (R / 2) * 128 + (R & 1) * 32 + 16); };
    auto aff = [&](auto I, auto E) {
      constexpr int e = E, r = 2 * I + (e >> 1), o = 2 * (e & 1);
      unsigned v = pk2(fmaf(bflo(xf[I][e]), ca[r][o], cs[r][o]), fmaf(bfhi(xf[I][e]), ca[r][o + 1], cs[r][o + 1]));
      tie(v); hf[I][e] = v;
    };
    sfor<4>(rdc);
    sfor<10>([&](auto E) {
      constexpr int e = E;
      waitv<sch_wait<S1>(e)>();
      if constexpr (e == 0) tie(xf[0]);
      tie(xf[2 * e + 1]); if constexpr (e < 9) tie(xf[2 * e + 2]);
      rbar();
      if constexpr (e == 0) { sfor<D>(rdw); sfor<4>([&](auto E2) { aff(IC<0>{}, E2); }); }
      sfor<8>([&](auto K) {
        constexpr int n = 8 * e + K, i = n / 4, t = n % 4;
        if constexpr (n + D < 80) rdw(IC<n + D>{});
        if constexpr ((t & 1) && i + 2 < 20) rdc(IC<2 * (i + 2) + (t >> 1)>{});
        acc[t] = mfma32(wf[n], hf[i], acc[t]); tie(acc[t]);
        SB();
        if constexpr (i + 1 < 20) aff(IC<i + 1>{}, IC<t>{});
        {
          constexpr int nA = S1::nA(e), nW = S1::nW(e), v = nA + nW > 4 ? K : (K & 1) ? K / 2 : 99;
          if constexpr (v < nA) { constexpr int a = sch_A<S1>(e) + v; ld16(xf[a], l16, rws, ab + a * 1024); }
          else if constexpr (v < nA + nW) { constexpr int r = sch_W<S1>(e) + v - nA; dma<true>(ringD + (r % 15) * 4096, l16, rws, w1 + r * 4096); }
        }
        SB();
      });
    });
    rbar();
    STAMP(6);
    f32x16 ka = acc[0], kb = acc[1]; const f32x16 sa = acc[2], sb = acc[3];
#pragma unroll
    for (int qd = 0; qd < 4; ++qd) *(LAS f32x4*)(L + L_X1 + w * 4096 + qd * 1024 + l16) = f32x4{sa[4 * qd], sa[4 * qd + 1], sa[4 * qd + 2], sa[4 * qd + 3]};
    bar();
#pragma unroll
    for (int qd = 0; qd < 4; ++qd) {
      f32x4 u = ldsf4(L, L_X1 + (w ^ 4) * 4096 + qd * 1024 + l16);
#pragma unroll
      for (int e = 0; e < 4; ++e) ka[4 * qd + e] += u[e];
    }
#pragma unroll
    for (int qd = 0; qd < 4; ++qd) *(LAS f32x4*)(L + L_X1 + (w ^ 4) * 4096 + qd * 1024 + l16) = f32x4{sb[4 * qd], sb[4 * qd + 1], sb[4 * qd + 2], sb[4 * qd + 3]};
    const unsigned kvg = WS_KV + ((4 * p + m) * 16 + j) * 5120, kvl = (8 + m) * KVT;
    {
      v4u c0 = {pk2(ka[0], ka[1]), pk2(ka[2], ka[3]), pk2(ka[4], ka[5]), pk2(ka[6], ka[7])};
      v4u c1 = {pk2(ka[8], ka[9]), pk2(ka[10], ka[11]), pk2(ka[12], ka[13]), pk2(ka[14], ka[15])};
      const unsigned go = kh ? 3072 + r32 * 32 + h * 16 : l16, lo = kh ? 3072 + r32 * 48 + h * 16 : l16, ls = kh ? 1536 : 1024;
      st16(c0, rws, kvg + go); st16(c1, rws, kvg + go + 1024);
      *(LAS v4u*)(L + kvl + lo) = c0; *(LAS v4u*)(L + kvl + lo + ls) = c1;
    }
    bar();
#pragma unroll
    for (int qd = 0; qd < 4; ++qd) {
      f32x4 u = ldsf4(L, L_X1 + w * 4096 + qd * 1024 + l16);
#pragma unroll
      for (int e = 0; e < 4; ++e) kb[4 * qd + e] += u[e];
    }
    const float qs = 0.22360679775f * 1.44269504089f;
    if (kh == 0) {
      v4u c2 = {pk2(kb[0], kb[1]), pk2(kb[2], kb[3]), pk2(kb[4], kb[5]), pk2(kb[6], kb[7])};
      st16(c2, rws, kvg + 2048 + l16);
      *(LAS v4u*)(L + kvl + 2048 + l16) = v4u{c2.x, c2.y, 0u, 0u};
      *(LAS v4u*)(L + kvl + 3072 + h * 1536 + r32 * 48 + 32) = v4u{c2.z, c2.w, 0x3f80u, 0u};
      *(LAS v2u*)(L + L_QHI + m * 512 + lane * 8) = v2u{pk2(kb[8] * qs, kb[9] * qs), pk2(kb[10] * qs, kb[11] * qs)};
    } else {
      *(LAS v4u*)(L + L_Q + m * 2048 + l16) = v4u{pk2(kb[0] * qs, kb[1] * qs), pk2(kb[2] * qs, kb[3] * qs), pk2(kb[4] * qs, kb[5] * qs), pk2(kb[6] * qs, kb[7] * qs)};
      *(LAS v4u*)(L + L_Q + m * 2048 + 1024 + l16) = v4u{pk2(kb[8] * qs, kb[9] * qs), pk2(kb[10] * qs, kb[11] * qs), pk2(kb[12] * qs, kb[13] * qs), pk2(kb[14] * qs, kb[15] * qs)};
    }
    waitv<0>();
    bar();
    if (tid == 0) __hip_atomic_fetch_add((GAS unsigned*)(P.ws + CT_C1 + ((p * 16 + j) << 7)), 1u, RLX_AGENT);
    STAMP(7);
  }

  if (b < 128) {
    if (w < 6) {
      const int mat = b >> 6, kp = (b >> 3) & 7, cg = b & 7;
      const int tl = 2 * w + blk;
      const int jd = 2 * cg + tl / 6, t = tl % 6;
      float us = 0.f, vs = 0.f;
#pragma unroll
      for (int sg = 0; sg < 5; ++sg) {
        const int k0 = sg < 4 ? 32 * (sg >> 1) + 4 * (2 * (sg & 1) + h) : 64 + 8 * h;
        const int k1 = sg < 4 ? k0 + 16 : k0 + 4;
        const unsigned ca = L_BSRC + q4 * 384 + (16 * tl + 4 * p4) * 2;
        v4u f = trpair(L, ca + k0 * 384, ca + k1 * 384);
        f32x4 a0 = ldsf4(L, L_MODV + 5760 + k0 * 4), a1 = ldsf4(L, L_MODV + 5760 + k1 * 4);
        f32x4 b0 = ldsf4(L, L_MODV + 6080 + k0 * 4), b1 = ldsf4(L, L_MODV + 6080 + k1 * 4);
        v4u o;
        o[0] = pk2(bflo(f[0]) * a0[0], bfhi(f[0]) * a0[1]); o[1] = pk2(bflo(f[1]) * a0[2], bfhi(f[1]) * a0[3]);
        o[2] = pk2(bflo(f[2]) * a1[0], bfhi(f[2]) * a1[1]); o[3] = pk2(bflo(f[3]) * a1[2], bfhi(f[3]) * a1[3]);
        vs += bflo(f[0]) * b0[0] + bfhi(f[0]) * b0[1] + bflo(f[1]) * b0[2] + bfhi(f[1]) * b0[3] + bflo(f[2]) * b1[0] + bfhi(f[2]) * b1[1] + bflo(f[3]) * b1[2] + bfhi(f[3]) * b1[3];
        us = dot2(o[0], 0x3f803f80u, us); us = dot2(o[1], 0x3f803f80u, us); us = dot2(o[2], 0x3f803f80u, us); us = dot2(o[3], 0x3f803f80u, us);
        const int s = 5 * kp + sg;
        st16(o, rws, WS_WT4 + jd * 245760 + (s / 20) * 122880 + ((s % 20) * 6 + t) * 1024 + (h * 32 + mat * 16 + i16) * 16);
      }
      us = swap32_add(us); vs = swap32_add(vs);
      const int row = (jd * 6 + t) * 32 + mat * 16 + i16;
      __builtin_amdgcn_raw_buffer_store_b32(__builtin_bit_cast(unsigned, h ? vs : us), rws, WS_UV + ((kp * 2 + h) * 3072 + row) * 4, 0, 16);
    }
  }
  STAMP(8);
  if (w == 0) poll(caddr, cbase, lane == 6 ? 32u : 1u, (lane == 1 && p >= 1) || (lane == 2 && p >= 2) || lane == 6);
  bar();
  STAMP(9);
  const unsigned w3 = WS_WT3 + c8 * 102400 + kh * 51200 + m * 1024;
  {
    v4u kv[5];
    const int gt = 4 * p - 8 + w;
    const unsigned ko = gt >= 0 ? WS_KV + (gt * 16 + j) * 5120 : 0x7ff00000u;
    sfor<5>([&](auto I) { ld16(kv[I], l16, rws, ko + I * 1024); });
    waitv<0>();
    sfor<5>([&](auto I) { tie(kv[I]); });
    {
      const unsigned tb = w * KVT, vo = 3072 + (lane >> 1) * 48 + (lane & 1) * 16;
      *(LAS v4u*)(L + tb + l16) = kv[0]; *(LAS v4u*)(L + tb + 1024 + l16) = kv[1];
      *(LAS v4u*)(L + tb + 2048 + l16) = v4u{kv[2].x, kv[2].y, gt >= 0 ? 0u : 0xfe96u, 0u};
      *(LAS v4u*)(L + tb + 3072 + h * 1536 + r32 * 48 + 32) = v4u{kv[2].z, kv[2].w, 0x3f80u, 0u};
      *(LAS v4u*)(L + tb + vo) = kv[3]; *(LAS v4u*)(L + tb + vo + 1536) = kv[4];
    }
    bar();
    if (tid == 0 && b < 128) __hip_atomic_fetch_add((GAS unsigned*)(P.ws + CT_CB + ((b & 7) << 7)), 1u, RLX_AGENT);
    STAMP(10);
    const int hd = kh;
    const v4u q0 = lds16(L, L_Q + m * 2048 + hd * 1024 + l16);
    v2u qh = *(LAS v2u*)(L + L_QHI + m * 512 + lane * 8);
    if (h != hd) qh = v2u{0, 0};
    const v4u q1 = {qh.x, qh.y, 0x3f80u, 0};
    const f32x16 zero = {};
    const unsigned ka = m * KVT + hd * 1024 + l16, kb = m * KVT + 2048 + l16;
    auto qk = [&](auto I, f32x16 c) { return mfma32(lds16(L, kb + I * KVT), q1, mfma32(lds16(L, ka + I * KVT), q0, c)); };
    auto vmax = [](float a, float c) { return __builtin_elementwise_maximum(a, c); };
    auto tmax = [&](const f32x16& v) { float t = vmax(v[0], v[1]);
#pragma unroll
      for (int r = 2; r < 16; r += 2) t = vmax(vmax(v[r], v[r + 1]), t);
      return t; };
    f32x16 Sm;
    {
      const f32x16 s0 = qk(IC<0>{}, zero), s8 = qk(IC<8>{}, zero);
#pragma unroll
      for (int r = 0; r < 16; ++r) Sm[r] = (r & 3) + 8 * (r >> 2) + 4 * h <= r32 ? s8[r] : s0[r];
    }
    float mx = tmax(Sm);
    sfor<7>([&](auto I) {
      mx = vmax(mx, tmax(qk(IC<I + 1>{}, zero)));
      dma<true>(LB + L_W3A + kh * 32768 + m * 1024 + I * 4096, l16, rws, w3 + I * 4096);
    });
    dma<true>(LB + L_W3A + kh * 32768 + m * 1024 + 7 * 4096, l16, rws, w3 + 7 * 4096);
    { f32x2 t = swp32(mx); mx = vmax(t.x, t.y); }
    f32x16 negm;
#pragma unroll
    for (int r = 0; r < 16; ++r) negm[r] = -mx;
    tie(negm);
    f32x16 O = {};
    const unsigned va = m * KVT + 3072 + hd * 1536 + (4 * h + q4) * 48 + (blk == 0 ? (p4 & 1) * 16 + (p4 >> 1) * 8 : (p4 ? 40 : 32));
    auto pv = [&](auto I, int s, v4u pf) { O = mfma32(trpair(L, va + I * KVT + s * 768, va + I * KVT + s * 768 + 384), pf, O); };
#pragma unroll
    for (int s = 0; s < 2; ++s) {
      v4u p0, p8;
#pragma unroll
      for (int e = 0; e < 4; ++e) {
        const int r = 8 * s + 2 * e, key = (r & 3) + 8 * (r >> 2) + 4 * h;
        const float a = __builtin_amdgcn_exp2f(Sm[r] - mx), c = __builtin_amdgcn_exp2f(Sm[r + 1] - mx);
        const float a8 = key <= r32 ? a : 0.f, c8_ = key + 1 <= r32 ? c : 0.f;
        p8[e] = pk2(a8, c8_); p0[e] = pk2(a - a8, c - c8_);
      }
      pv(IC<0>{}, s, p0); pv(IC<8>{}, s, p8);
    }
    sfor<7>([&](auto I) {
      const f32x16 S = qk(IC<I + 1>{}, negm);
#pragma unroll
      for (int s = 0; s < 2; ++s) {
        v4u pf;
#pragma unroll
        for (int e = 0; e < 4; ++e) pf[e] = pk2(__builtin_amdgcn_exp2f(S[8 * s + 2 * e]), __builtin_amdgcn_exp2f(S[8 * s + 2 * e + 1]));
        pv(IC<I + 1>{}, s, pf);
      }
    });
    const float inv = __builtin_amdgcn_rcpf(swap32_hi(O[8]));
    const int tok = 128 * p + 32 * m + r32;
    const unsigned ob = WS_OF + (tok >> 4) * 20480 + (tok & 15) * 16;
    const int c = 5 * j + 2 * hd + h, c2 = 5 * j + 4;
    STAMP(11);
    st16(v4u{pk2(O[0] * inv, O[1] * inv), pk2(O[2] * inv, O[3] * inv), pk2(O[4] * inv, O[5] * inv), pk2(O[6] * inv, O[7] * inv)}, rws, ob + (c >> 2) * 1024 + (c & 3) * 256);
    if (h == 0) st8(v2u{pk2(O[8] * inv, O[9] * inv), pk2(O[10] * inv, O[11] * inv)}, rws, ob + (c2 >> 2) * 1024 + (c2 & 3) * 256 + hd * 8);
  }
  waitv<0>();
  bar();
  const int tt = 8 * p + 4 * rh + m;
  unsigned x2k[6], cbv;
  {
    v2u xr[3];
    const unsigned xo = (16 * tt + i16) * 1280 + (80 * c8 + 4 * g4) * 2;
    if (tid == 0) __hip_atomic_fetch_add((GAS unsigned*)(P.ws + CT_C2 + (p << 7)), 1u, RLX_AGENT);
    STAMP(12);
    ld8p(xr[0], xo, rx, kh * 64); ld8p(xr[1], xo, rx, kh * 64 + 32); ld8p(xr[2], xo, rx, 128);
    sfor<5>([&](auto R) { dma<true>(LB + kh * 20480 + m * 1024 + R * 4096, l16, rws, w3 + (8 + R) * 4096); });
    if (w == 0) poll(caddr, cbase, 16u, lane == 3);
    bar();
    STAMP(13);
    v4u af[10], wf[50];
    const unsigned ab = WS_OF + (tt * 20 + kh) * 1024;
    sfor<10>([&](auto I) { ld16(af[I], l16, rws, ab + I * 2048); });
    asm volatile("global_load_dword %0, %1, off sc1" : "=v"(cbv) : "v"(caddr) : "memory");
    f32x4 acc[5] = {};
    constexpr int D = 6;
    unsigned wa = L_W3A + kh * 32768 + l16; tie(wa);
    const unsigned wb = kh * 20480 + l16;
    auto rdw = [&](auto N) { if constexpr (N < 32) wf[N] = lds16(L, wa + N * 1024); else wf[N] = lds16(L, wb + (N - 32) * 1024); };
    sfor<10>([&](auto E) {
      constexpr int e = E;
      waitv<10 - e>();
      tie(af[e]);
      if constexpr (e == 0) { rbar(); sfor<D>(rdw); }
      sfor<5>([&](auto K) {
        constexpr int n = 5 * e + K;
        if constexpr (n + D < 50) rdw(IC<n + D>{});
        acc[K] = mfma16(wf[n], af[e], acc[K]); tie(acc[K]);
        SB();
      });
    });
    STAMP(14);
    const unsigned xs = L_X3 + m * 5120;
    if (kh == 0) { *(LAS f32x4*)(L + xs + l16) = acc[2]; *(LAS f32x4*)(L + xs + 1024 + l16) = acc[3]; }
    else { *(LAS f32x4*)(L + xs + 2048 + l16) = acc[0]; *(LAS f32x4*)(L + xs + 3072 + l16) = acc[1]; *(LAS f32x4*)(L + xs + 4096 + l16) = acc[4]; }
    bar();
    tie(xr[0]); tie(xr[1]); tie(xr[2]);
    f32x4 r0, r1, r2 = {};
    if (kh == 0) { r0 = acc[0] + ldsf4(L, xs + 2048 + l16); r1 = acc[1] + ldsf4(L, xs + 3072 + l16); r2 = acc[4] + ldsf4(L, xs + 4096 + l16); }
    else { r0 = acc[2] + ldsf4(L, xs + l16); r1 = acc[3] + ldsf4(L, xs + 1024 + l16); }
    const f32x4 ga0 = ldsf4(L, L_MODV + 5120 + (32 * kh + 4 * g4) * 4), ga1 = ldsf4(L, L_MODV + 5120 + (32 * kh + 16 + 4 * g4) * 4), ga2 = ldsf4(L, L_MODV + 5120 + (64 + 4 * g4) * 4);
    x2k[0] = pk2(bflo(xr[0].x) + ga0[0] * r0[0], bfhi(xr[0].x) + ga0[1] * r0[1]); x2k[1] = pk2(bflo(xr[0].y) + ga0[2] * r0[2], bfhi(xr[0].y) + ga0[3] * r0[3]);
    x2k[2] = pk2(bflo(xr[1].x) + ga1[0] * r1[0], bfhi(xr[1].x) + ga1[1] * r1[1]); x2k[3] = pk2(bflo(xr[1].y) + ga1[2] * r1[2], bfhi(xr[1].y) + ga1[3] * r1[3]);
    x2k[4] = pk2(bflo(xr[2].x) + ga2[0] * r2[0], bfhi(xr[2].x) + ga2[1] * r2[1]); x2k[5] = pk2(bflo(xr[2].y) + ga2[2] * r2[2], bfhi(xr[2].y) + ga2[3] * r2[3]);
    if (kh) { x2k[4] = 0; x2k[5] = 0; }
    const unsigned xb = WS_X2F + ((tt >> 1) * 40 + 5 * c8) * 1024 + ((tt & 1) * 16 + i16) * 16;
    st16(v4u{x2k[0], x2k[1], x2k[2], x2k[3]}, rws, xb + (2 * kh + (g4 >> 1)) * 1024 + (g4 & 1) * 512);
    if (kh == 0) st8(v2u{x2k[4], x2k[5]}, rws, xb + 4096 + (g4 >> 1) * 512 + (g4 & 1) * 8);
    float s1 = 0.f, s2 = 0.f;
#pragma unroll
    for (int e = 0; e < 6; ++e) { s1 = dot2(x2k[e], 0x3f803f80u, s1); s2 = dot2(x2k[e], x2k[e], s2); }
    s1 = swap32_add(swap16_add(s1)); s2 = swap32_add(swap16_add(s2));
    if (g4 == 0) st8(v2u{__builtin_bit_cast(unsigned, s1), __builtin_bit_cast(unsigned, s2)}, rws, WS_ST + ((tt >> 1) * 8 + c8) * 512 + ((tt & 1) * 16 + i16) * 16 + kh * 8);
  }
  waitv<0>();
  tie(cbv);
  if (w == 0 && !all64(lane < 8 || lane >= 16 || cbv - cbase >= 28u)) poll(caddr, cbase, 28u, lane >= 8 && lane < 16);
  bar();
  const unsigned w5 = WS_WT5 + c8 * 245760 + kh * 122880 + m * 1024;
  {
    const unsigned w4 = WS_WT4 + j * 245760 + kh * 122880 + m * 1024;
    if (tid == 0) __hip_atomic_fetch_add((GAS unsigned*)(P.ws + CT_C3 + (p << 7)), 1u, RLX_AGENT);
    STAMP(15);
    sfor<S4::R0>([&](auto R) { dma<true>(ringD + R * 4096, l16, rws, w4 + R * 4096); });
    if (w == 0) poll(caddr, cbase, 16u, lane == 4);
    bar();
    STAMP(16);
    v4u af[20], stv[4], wf[120]; unsigned uv[8];
    const unsigned ab = WS_X2F + ((4 * p + m) * 40 + 20 * kh) * 1024;
    sfor<S4::A0>([&](auto I) { ld16(af[I], l16, rws, ab + I * 1024); });
    f32x16 acc[6] = {};
    const unsigned wbK = ringL + kh * 3072, wbS = ringL + 3072 - kh * 3072;
    constexpr int D = 4;
    auto rdw = [&](auto N) { wf[N] = lds16(L, ((N % 6) < 3 ? wbK : wbS) + (((N / 6) * 6) % 60 + N % 3) * 1024); };
    const int ti = tid < 384 ? tid : 0;
    const unsigned uo = ((ti / 192) * 3072 + j * 192 + ti % 192) * 4;
    sfor<10>([&](auto E) {
      constexpr int e = E;
      waitv<sch_wait<S4>(e)>();
      tie(af[2 * e]); tie(af[2 * e + 1]);
      rbar();
      if constexpr (e == 0) sfor<D>(rdw);
      sfor<12>([&](auto K) {
        constexpr int n = 12 * e + K, nA = S4::nA(e), nW = S4::nW(e), nX = S4::nX(e), v = nA + nW + nX > 6 ? K : (K & 1) ? 99 : K / 2;
        if constexpr (n + D < 120) rdw(IC<n + D>{});
        acc[n % 6] = mfma32(wf[n], af[n / 6], acc[n % 6]); tie(acc[n % 6]);
        SB();
        if constexpr (v < nA) { constexpr int a = sch_A<S4>(e) + v; ld16(af[a], l16, rws, ab + a * 1024); }
        else if constexpr (v < nA + nW) { constexpr int r = sch_W<S4>(e) + v - nA; dma<true>(ringD + (r % 15) * 4096, l16, rws, w4 + r * 4096); }
        else if constexpr (v < nA + nW + nX) {
          constexpr int x = sch_X<S4>(e) + v - nA - nW;
          if constexpr (x < 4) ld16(stv[x], r32 * 16 + h * 2048 + x * 512, rws, WS_ST + (4 * p + m) * 4096);
          else if constexpr (x < 12) ld4(uv[x - 4], uo, rws, WS_UV + (x - 4) * 24576);
          else dma<true>(ringD + (x - 12) * 4096, l16, rws, w5 + (x - 12) * 4096);
        }
        SB();
      });
    });
    STAMP(17);
    waitv<12>();
    sfor<4>([&](auto I) { tie(stv[I]); });
    sfor<8>([&](auto I) { tie(uv[I]); });
    {
      float s = 0.f;
#pragma unroll
      for (int i = 0; i < 8; ++i) s += __builtin_bit_cast(float, uv[i]);
      if (tid < 384) *(LAS float*)(L + L_UVS + tid * 4) = s;
    }
    float s1 = 0.f, s2 = 0.f;
#pragma unroll
    for (int i = 0; i < 4; ++i) { f32x4 v = __builtin_bit_cast(f32x4, stv[i]); s1 += v[0] + v[2]; s2 += v[1] + v[3]; }
    s1 = swap32_add(s1); s2 = swap32_add(s2);
    const float mean = s1 * (1.f / 640.f);
    const float rs2 = __builtin_amdgcn_rsqf(fmaxf(s2 * (1.f / 640.f) - mean * mean, 0.f) + 1e-5f), nm2 = -mean * rs2;
    const int tok = 128 * p + 32 * m + r32;
    const unsigned ao = WS_ACT + ((tok >> 4) * 48 + 3 * j) * 1024 + h * 256 + (tok & 15) * 16;
    sfor<3>([&](auto T) {
      constexpr int t = T;
      const f32x16 sd = acc[3 + t];
      const unsigned wr = L_BSRC + ((t & 1) ? (w ^ 4) : w) * 4096 + l16, rd = L_BSRC + ((t & 1) ? w : (w ^ 4)) * 4096 + l16;
#pragma unroll
      for (int qd = 0; qd < 4; ++qd) *(LAS f32x4*)(L + wr + qd * 1024) = f32x4{sd[4 * qd], sd[4 * qd + 1], sd[4 * qd + 2], sd[4 * qd + 3]};
      bar();
      f32x16 kp = acc[t];
      const int tg = 3 * kh + t;
      float a[8];
#pragma unroll
      for (int qd = 0; qd < 4; ++qd) {
        const f32x4 o = ldsf4(L, rd + qd * 1024);
        const f32x4 uu = ldsf4(L, L_UVS + (tg * 32 + 8 * qd + 4 * h) * 4), vv = ldsf4(L, L_UVS + 768 + (tg * 32 + 8 * qd + 4 * h) * 4);
#pragma unroll
        for (int e = 0; e < 4; ++e) kp[4 * qd + e] = fmaf(kp[4 * qd + e] + o[e], rs2, fmaf(nm2, uu[e], vv[e]));
      }
#pragma unroll
      for (int e = 0; e < 8; ++e) { const float gv = kp[e]; a[e] = gv * __builtin_amdgcn_rcpf(1.f + __builtin_amdgcn_exp2f(-1.44269504f * gv)) * kp[8 + e]; }
      st16(v4u{pk2(a[0], a[1]), pk2(a[2], a[3]), pk2(a[4], a[5]), pk2(a[6], a[7])}, rws, ao + (tg >> 1) * 1024 + (tg & 1) * 512);
    });
  }
  waitv<0>();
  bar();
  {
    if (tid == 0) __hip_atomic_fetch_add((GAS unsigned*)(P.ws + CT_C4 + (p << 7)), 1u, RLX_AGENT);
    STAMP(18);
    sfor<3>([&](auto R) { dma<true>(ringD + (12 + R) * 4096, l16, rws, w5 + (12 + R) * 4096); });
    if (w == 0) poll(caddr, cbase, 16u, lane == 5);
    bar();
    STAMP(19);
    v4u af[24], wf[120];
    const unsigned ab = WS_ACT + (tt * 48 + kh) * 1024;
    sfor<S5::A0>([&](auto I) { ld16(af[I], l16, rws, ab + I * 2048); });
    f32x4 acc[5] = {};
    constexpr int D = 6;
    auto rdw = [&](auto N) { wf[N] = lds16(L, ringL + (N % 60) * 1024); };
    sfor<12>([&](auto E) {
      constexpr int e = E;
      waitv<sch_wait<S5>(e)>();
      tie(af[2 * e]); tie(af[2 * e + 1]);
      rbar();
      if constexpr (e == 0) sfor<D>(rdw);
      sfor<10>([&](auto K) {
        constexpr int n = 10 * e + K, v = K / 2, nA = S5::nA(e), nW = S5::nW(e);
        if constexpr (n + D < 120) rdw(IC<n + D>{});
        acc[n % 5] = mfma16(wf[n], af[n / 5], acc[n % 5]); tie(acc[n % 5]);
        SB();
        if constexpr (!(K & 1)) {
          if constexpr (v < nA) { constexpr int a = sch_A<S5>(e) + v; ld16(af[a], l16, rws, ab + a * 2048); }
          else if constexpr (v < nA + nW) { constexpr int r = sch_W<S5>(e) + v - nA; dma<true>(ringD + (r % 15) * 4096, l16, rws, w5 + r * 4096); }
          SB();
        }
      });
    });
    STAMP(20);
    const unsigned xs = L_BSRC + m * 5120;
    if (kh == 0) { *(LAS f32x4*)(L + xs + l16) = acc[2]; *(LAS f32x4*)(L + xs + 1024 + l16) = acc[3]; }
    else { *(LAS f32x4*)(L + xs + 2048 + l16) = acc[0]; *(LAS f32x4*)(L + xs + 3072 + l16) = acc[1]; *(LAS f32x4*)(L + xs + 4096 + l16) = acc[4]; }
    bar();
    f32x4 r0, r1, r2 = {};
    if (kh == 0) { r0 = acc[0] + ldsf4(L, xs + 2048 + l16); r1 = acc[1] + ldsf4(L, xs + 3072 + l16); r2 = acc[4] + ldsf4(L, xs + 4096 + l16); }
    else { r0 = acc[2] + ldsf4(L, xs + l16); r1 = acc[3] + ldsf4(L, xs + 1024 + l16); }
    const f32x4 g0 = ldsf4(L, L_MODV + 5440 + (32 * kh + 4 * g4) * 4), g1 = ldsf4(L, L_MODV + 5440 + (32 * kh + 16 + 4 * g4) * 4), g2 = ldsf4(L, L_MODV + 5440 + (64 + 4 * g4) * 4);
    GAS char* o = (GAS char*)P.out + (size_t)(16 * tt + i16) * 1280 + (80 * c8 + 4 * g4) * 2;
    *(GAS v2u*)(o + kh * 64) = v2u{pk2(bflo(x2k[0]) + g0[0] * r0[0], bfhi(x2k[0]) + g0[1] * r0[1]), pk2(bflo(x2k[1]) + g0[2] * r0[2], bfhi(x2k[1]) + g0[3] * r0[3])};
    *(GAS v2u*)(o + kh * 64 + 32) = v2u{pk2(bflo(x2k[2]) + g1[0] * r1[0], bfhi(x2k[2]) + g1[1] * r1[1]), pk2(bflo(x2k[3]) + g1[2] * r1[2], bfhi(x2k[3]) + g1[3] * r1[3])};
    if (kh == 0) *(GAS v2u*)(o + 128) = v2u{pk2(bflo(x2k[4]) + g2[0] * r2[0], bfhi(x2k[4]) + g2[1] * r2[1]), pk2(bflo(x2k[5]) + g2[2] * r2[2], bfhi(x2k[5]) + g2[3] * r2[3])};
    if (b == 0 && tid == 0) __hip_atomic_store((GAS unsigned*)(P.ws + CT_E), cbase + 1u, RLX_AGENT);
    STAMP(21);
  }
}

#if defined(__HIP_DEVICE_COMPILE__)
#pragma clang attribute pop
#endif
extern "C" void kernel_launch(void* const* d_in, const int* in_sizes, int n_in, void* d_out, int out_size, void* d_ws, size_t ws_size,
                              hipStream_t stream) {
  Params P;
  P.x = (const u16*)d_in[0]; P.wqkv = (const u16*)d_in[1]; P.wo = (const u16*)d_in[2];
  P.wg = (const u16*)d_in[4]; P.wu = (const u16*)d_in[5]; P.wd = (const u16*)d_in[6];
  P.c = (const u16*)d_in[7]; P.wmod = (const u16*)d_in[8];
  P.out = (u16*)d_out; P.ws = (char*)d_ws;
  fused_block<<<256, 512, 0, stream>>>(P);
}
```

```cpp
#include <hip/hip_runtime.h>
#include <hip/hip_bf16.h>
#include <utility>

typedef unsigned short u16;
typedef unsigned v4u __attribute__((ext_vector_type(4)));
typedef unsigned v2u __attribute__((ext_vector_type(2)));
typedef short v4s __attribute__((ext_vector_type(4)));
typedef float f32x2 __attribute__((ext_vector_type(2)));
typedef float f32x4 __attribute__((ext_vector_type(4)));
typedef float f32x16 __attribute__((ext_vector_type(16)));
typedef __bf16 bf16x2 __attribute__((ext_vector_type(2)));
typedef __bf16 bf16x8 __attribute__((ext_vector_type(8)));
typedef __amdgpu_buffer_rsrc_t rsrc_t;
#define LAS __attribute__((address_space(3)))
#define GAS __attribute__((address_space(1)))
#define DI __device__ __forceinline__
#define RLX_AGENT __ATOMIC_RELAXED, __HIP_MEMORY_SCOPE_AGENT

#define STAMPS 0

constexpr int WS_CTRL = 0;
constexpr int WS_MODP = 65536;
constexpr int WS_UV   = 131072;
constexpr int WS_ST   = 327680;
constexpr int WS_WT1  = 655360;
constexpr int WS_WT3  = WS_WT1 + 16 * 163840;
constexpr int WS_WT4  = WS_WT3 + 8 * 102400;
constexpr int WS_WT5  = WS_WT4 + 16 * 245760;
constexpr int WS_KV   = WS_WT5 + 8 * 245760;
constexpr int WS_OF   = WS_KV + 64 * 16 * 5120;
constexpr int WS_X2F  = WS_OF + 128 * 20 * 1024;
constexpr int WS_ACT  = WS_X2F + 64 * 40 * 1024;
constexpr int WS_XH   = WS_ACT + 128 * 48 * 1024;
constexpr int WS_END  = WS_XH + 64 * 40 * 1024;
constexpr int WS_DBG  = WS_END;
constexpr int CT_E = 0, CT_FLAG = 4096  , CT_C1 = 16384  , CT_C2 = 49152, CT_C3 = CT_C2 + 2048,
              CT_C4 = CT_C3 + 2048, CT_CB = CT_C4 + 2048  , CT_CW = CT_CB + 1024;
constexpr int L_WQ = 147456, L_RED = 157696, L_ST1 = 158720;
constexpr int L_BSRC = 122880;
constexpr int L_MODV = 155648;
constexpr int L_CNT = 162816;
constexpr int L_UVS = 155648;
constexpr int KVT = 5632;
constexpr int L_Q = 67584, L_QHI = 75776, L_X1 = 81920;
constexpr unsigned SPIN_CAP = 1u << 20;

#if defined(__HIP_DEVICE_COMPILE__)
#pragma clang attribute push (__attribute__((target("no-packed-fp32-ops"))), apply_to = function)
#endif
struct Params { const u16 *x, *wqkv, *wo, *wg, *wu, *wd, *c, *wmod; u16* out; char* ws; };

DI f32x16 mfma32(v4u a, v4u b, f32x16 c) { return __builtin_amdgcn_mfma_f32_32x32x16_bf16(__builtin_bit_cast(bf16x8, a), __builtin_bit_cast(bf16x8, b), c, 0, 0, 0); }
DI f32x4 mfma16(v4u a, v4u b, f32x4 c) { return __builtin_amdgcn_mfma_f32_16x16x32_bf16(__builtin_bit_cast(bf16x8, a), __builtin_bit_cast(bf16x8, b), c, 0, 0, 0); }
DI unsigned pk2(float a, float b) { f32x2 v = {a, b}; return __builtin_bit_cast(unsigned, __builtin_convertvector(v, bf16x2)); }
DI float bflo(unsigned x) { return __builtin_bit_cast(float, x << 16); }
DI float bfhi(unsigned x) { return __builtin_bit_cast(float, x & 0xffff0000u); }
DI float bfr(float a) { return bflo(pk2(a, a)); }
DI f32x2 swp32(float v) {
  unsigned a = __builtin_bit_cast(unsigned, v), c = a; asm("" : "+v"(c));
  auto r = __builtin_amdgcn_permlane32_swap(a, c, false, false);
  unsigned r0 = r[0], r1 = r[1];
  return f32x2{__builtin_bit_cast(float, r0), __builtin_bit_cast(float, r1)};
}
DI f32x2 swp16(float v) {
  unsigned a = __builtin_bit_cast(unsigned, v), c = a; asm("" : "+v"(c));
  auto r = __builtin_amdgcn_permlane16_swap(a, c, false, false);
  unsigned r0 = r[0], r1 = r[1];
  return f32x2{__builtin_bit_cast(float, r0), __builtin_bit_cast(float, r1)};
}
DI float swap32_add(float v) { f32x2 r = swp32(v); return r.x + r.y; }
DI float swap16_add(float v) { f32x2 r = swp16(v); return r.x + r.y; }
DI float swap32_max(float v) { f32x2 r = swp32(v); return fmaxf(r.x, r.y); }
DI float swap32_hi(float v) { return swp32(v).y; }
DI bool all64(bool c) { return __builtin_amdgcn_ballot_w64(!c) == 0ull; }
DI float ror8_add(float v) { return v + __builtin_bit_cast(float, __builtin_amdgcn_update_dpp(0, __builtin_bit_cast(int, v), 0x128, 0xf, 0xf, true)); }

template <bool SC1> DI void dma(unsigned lds_addr, unsigned voff, rsrc_t rs, unsigned soff) {
  if (SC1) asm volatile("s_mov_b32 m0, %0\n\ts_nop 0\n\tbuffer_load_dwordx4 %1, %2, %3 offen sc1 lds" ::"s"(lds_addr), "v"(voff), "s"(rs), "s"(soff) : "memory");
  else     asm volatile("s_mov_b32 m0, %0\n\ts_nop 0\n\tbuffer_load_dwordx4 %1, %2, %3 offen lds" ::"s"(lds_addr), "v"(voff), "s"(rs), "s"(soff) : "memory");
}
DI void ld16(v4u& d, unsigned voff, rsrc_t rs, unsigned soff) { asm volatile("buffer_load_dwordx4 %0, %1, %2, %3 offen sc1" : "=v"(d) : "v"(voff), "s"(rs), "s"(soff) : "memory"); }
DI void ld16p(v4u& d, unsigned voff, rsrc_t rs, unsigned soff) { asm volatile("buffer_load_dwordx4 %0, %1, %2, %3 offen" : "=v"(d) : "v"(voff), "s"(rs), "s"(soff) : "memory"); }
DI void ld8p(v2u& d, unsigned voff, rsrc_t rs, unsigned soff) { asm volatile("buffer_load_dwordx2 %0, %1, %2, %3 offen" : "=v"(d) : "v"(voff), "s"(rs), "s"(soff) : "memory"); }
DI void ld4(unsigned& d, unsigned voff, rsrc_t rs, unsigned soff) { asm volatile("buffer_load_dword %0, %1, %2, %3 offen sc1" : "=v"(d) : "v"(voff), "s"(rs), "s"(soff) : "memory"); }
template <int N> DI void waitv() { asm volatile("s_waitcnt vmcnt(%0)" ::"n"(N) : "memory"); }
template <class T> DI void tie(T& a) { asm volatile("" : "+v"(a)); }
DI void rbar() { asm volatile("s_barrier" ::: "memory"); }
DI void bar() { asm volatile("s_waitcnt lgkmcnt(0)\n\ts_barrier" ::: "memory"); }
DI void st16(v4u v, rsrc_t rs, unsigned off) { __builtin_amdgcn_raw_buffer_store_b128(v, rs, off, 0, 16); }
DI void st8(v2u v, rsrc_t rs, unsigned off) { __builtin_amdgcn_raw_buffer_store_b64(v, rs, off, 0, 16); }
DI v4u trpair(LAS char* L, unsigned a0, unsigned a1) {
  v4s lo = __builtin_amdgcn_ds_read_tr16_b64_v4i16((LAS v4s*)(L + a0));
  v4s hi = __builtin_amdgcn_ds_read_tr16_b64_v4i16((LAS v4s*)(L + a1));
  v2u l = __builtin_bit_cast(v2u, lo), h = __builtin_bit_cast(v2u, hi);
  return v4u{l.x, l.y, h.x, h.y};
}
DI v4u lds16(LAS char* L, unsigned a) { return *(LAS v4u*)(L + a); }
DI f32x4 ldsf4(LAS char* L, unsigned a) { return *(LAS f32x4*)(L + a); }

template <int... I, class F> DI void sfor_impl(std::integer_sequence<int, I...>, F&& f) { (f(std::integral_constant<int, I>{}), ...); }
template <int N, class F> DI void sfor(F&& f) { sfor_impl(std::make_integer_sequence<int, N>{}, f); }
#define SB() __builtin_amdgcn_sched_barrier(0)

template <int N> using IC = std::integral_constant<int, N>;
DI float dot2(unsigned a, unsigned b, float c) { return __builtin_amdgcn_fdot2_f32_bf16(__builtin_bit_cast(bf16x2, a), __builtin_bit_cast(bf16x2, b), c, false); }
DI f32x2 xswp32(float a, float c) { auto r = __builtin_amdgcn_permlane32_swap(__builtin_bit_cast(unsigned, a), __builtin_bit_cast(unsigned, c), false, false); unsigned r0 = r[0], r1 = r[1]; return f32x2{__builtin_bit_cast(float, r0), __builtin_bit_cast(float, r1)}; }
DI f32x2 xswp16(float a, float c) { auto r = __builtin_amdgcn_permlane16_swap(__builtin_bit_cast(unsigned, a), __builtin_bit_cast(unsigned, c), false, false); unsigned r0 = r[0], r1 = r[1]; return f32x2{__builtin_bit_cast(float, r0), __builtin_bit_cast(float, r1)}; }

template <class S> constexpr int sch_tot(int e) { int t = S::A0; for (int i = 0; i < e; ++i) t += S::nA(i) + S::nW(i) + S::nX(i); return t; }
template <class S> constexpr int sch_W(int e) { int t = S::R0; for (int i = 0; i < e; ++i) t += S::nW(i); return t; }
template <class S> constexpr int sch_A(int e) { int t = S::A0; for (int i = 0; i < e; ++i) t += S::nA(i); return t; }
template <class S> constexpr int sch_posA(int a) { if (a < S::A0) return a; for (int i = 0;; ++i) if (a < sch_A<S>(i + 1)) return sch_tot<S>(i) + (a - sch_A<S>(i)); }
template <class S> constexpr int sch_posW(int r) { if (r < S::R0) return r - S::R0; for (int i = 0;; ++i) if (r < sch_W<S>(i + 1)) return sch_tot<S>(i) + S::nA(i) + (r - sch_W<S>(i)); }
template <class S> constexpr int sch_wait(int e) {
  int nd = (S::PS * (e + 1) + S::LOOK + 3) / 4; if (nd > S::TOT) nd = S::TOT;
  int pw = sch_posW<S>(nd - 1), na = S::APS * (e + 1) - 1 + S::ALOOK, pa = sch_posA<S>(na < S::NA ? na : S::NA - 1);
  return sch_tot<S>(e) - 1 - (pw > pa ? pw : pa);
}
constexpr int cmin(int a, int b) { return a < b ? a : b; }
template <int PS_, int R0_, int RATE, int TOT_> struct Ring {
  static constexpr int upto(int e) { int v = R0_; for (int i = 0; i <= e; ++i) v = cmin(cmin(TOT_, (PS_ * i + 60) / 4), v + RATE); return v; }
  static constexpr int n(int e) { return upto(e) - (e ? upto(e - 1) : R0_); } };
struct S1 { static constexpr int PS = 8, LOOK = 4, R0 = 6, TOT = 20, A0 = 6, APS = 2, ALOOK = 1, NA = 20;
  static constexpr int nA(int e) { return e < 7 ? 2 : 0; } static constexpr int nW(int e) { return Ring<PS, R0, 2, TOT>::n(e); } static constexpr int nX(int e) { return e == 7 || e == 8 ? 2 : 0; } };
struct S4 { static constexpr int PS = 12, LOOK = 4, R0 = 10, TOT = 30, A0 = 6, APS = 2, ALOOK = 0, NA = 20;
  static constexpr int nA(int e) { return e < 7 ? 2 : 0; } static constexpr int nW(int e) { return Ring<PS, R0, 4, TOT>::n(e); } static constexpr int nX(int e) { return e == 6 ? 4 : e == 7 ? 6 : e == 8 ? 2 : 0; } };
struct S5 { static constexpr int PS = 10, LOOK = 6, R0 = 12, TOT = 30, A0 = 10, APS = 2, ALOOK = 0, NA = 24;
  static constexpr int nA(int e) { return e < 7 ? 2 : 0; } static constexpr int nW(int e) { return Ring<PS, R0, 3, TOT>::n(e); } static constexpr int nX(int) { return 0; } };

#define STAMP(i)

DI void poll(const GAS unsigned* addr, unsigned base, unsigned want, bool active) {
  for (unsigned s = 0; s < SPIN_CAP; ++s) {
    unsigned v = __hip_atomic_load(addr, RLX_AGENT);
    if (all64(!active || (v - base) >= want)) break;
  }
  __builtin_amdgcn_fence(__ATOMIC_ACQUIRE, "wavefront");
}
DI unsigned lds_arrive(LAS char* L, int off, int lane) {
  unsigned o = 0;
  asm volatile("s_waitcnt lgkmcnt(0)" ::: "memory");
  if (lane == 0) o = __hip_atomic_fetch_add((LAS unsigned*)(L + off), 1u, __ATOMIC_RELAXED, __HIP_MEMORY_SCOPE_WORKGROUP);
  o = __builtin_amdgcn_readfirstlane(o);
  asm volatile("" ::: "memory");
  return o;
}
DI void lds_wait(LAS char* L, int off, unsigned want) {
  for (unsigned s = 0; s < SPIN_CAP && *(volatile LAS unsigned*)(L + off) < want; ++s) __builtin_amdgcn_s_sleep(1);
  asm volatile("" ::: "memory");
}

__global__ __launch_bounds__(512, 2) void fused_block(Params P) {
  __shared__ __attribute__((aligned(16))) char lds_raw[163840];
  LAS char* L = (LAS char*)lds_raw;
  const unsigned LB = (unsigned)(size_t)L;
  const int tid = threadIdx.x, lane = tid & 63;
  const int w = __builtin_amdgcn_readfirstlane(tid >> 6);
  const int b = blockIdx.x;
  const int p = 4 * ((b & 7) >> 1) + ((b >> 3) & 3), j = 8 * (b & 1) + (b >> 5);
  const int m = w & 3, kh = w >> 2, h = lane >> 5, r32 = lane & 31;
  const int g4 = lane >> 4, i16 = lane & 15, q4 = (lane & 15) >> 2, p4 = lane & 3, blk = (lane >> 4) & 1;
  const rsrc_t rws = __builtin_amdgcn_make_buffer_rsrc((void*)P.ws, 0, WS_END, 0x00020000);
  const rsrc_t rx = __builtin_amdgcn_make_buffer_rsrc((void*)P.x, 0, 2048 * 1280, 0x00020000);
  const unsigned l16 = lane * 16;
  const unsigned ringL = kh * 61440 + l16, ringD = LB + kh * 61440 + m * 1024;
  STAMP(0);

  const GAS unsigned* caddr;
  {
    int off = CT_E;
    if (w == 0) {
      if (lane == 1) off = CT_C1 + (((p >= 1 ? p - 1 : 0) * 16 + j) << 7);
      if (lane == 2) off = CT_C1 + (((p >= 2 ? p - 2 : 0) * 16 + j) << 7);
      if (lane == 3) off = CT_C2 + (p << 7);
      if (lane == 4) off = CT_C3 + (p << 7);
      if (lane == 5) off = CT_C4 + (p << 7);
      if (lane == 6) off = CT_CW;
      if (lane >= 8 && lane < 16) off = CT_CB + ((lane - 8) << 7);
    }
    caddr = (const GAS unsigned*)(P.ws + off);
  }
  unsigned cbase = 0;
  if (w < 4) {
    const int bj = b < 240 ? b : 0;
    const int cc = bj % 60, kq = bj / 60, s = bj / 6, q = bj % 6;
    v4u wm[5]; u16 cv[5]; v4u wq[3];
    const GAS char* pm = (const GAS char*)P.wmod + (size_t)(160 * kq + (tid >> 3)) * 7680 + cc * 128 + (tid & 7) * 16;
    const GAS u16* pc = (const GAS u16*)P.c + 160 * kq + (tid >> 3);
#pragma unroll
    for (int i = 0; i < 5; ++i) { wm[i] = *(const GAS v4u*)(pm + i * 245760); cv[i] = pc[32 * i]; }
    const int lr = (lane * 205) >> 11, lc = lane - 10 * lr;
    const GAS char* pq = (const GAS char*)P.wqkv + (size_t)(16 * s + (lr < 6 ? lr : 5)) * 3840 + q * 640 + w * 160 + lc * 16;
    wq[0] = *(const GAS v4u*)pq; wq[1] = *(const GAS v4u*)(pq + 6 * 3840); wq[2] = *(const GAS v4u*)(pq + (lr < 4 ? 12 : 10) * 3840);
    cbase = __hip_atomic_load(caddr, RLX_AGENT);
    if (tid < 2) *(LAS v4u*)(L + L_CNT + tid * 16) = v4u{0u, 0u, 0u, 0u};
    bar();
    if (b < 240) {
      float acc[8] = {0, 0, 0, 0, 0, 0, 0, 0};
#pragma unroll
      for (int i = 0; i < 5; ++i) {
        float cf = bflo(cv[i]);
        float sv = bfr(cf * __builtin_amdgcn_rcpf(1.f + __builtin_amdgcn_exp2f(-1.44269504f * cf)));
#pragma unroll
        for (int e = 0; e < 4; ++e) { acc[2 * e] += sv * bflo(wm[i][e]); acc[2 * e + 1] += sv * bfhi(wm[i][e]); }
      }
      float u[4], v[2];
#pragma unroll
      for (int e = 0; e < 4; ++e) { f32x2 r = xswp32(acc[e], acc[e + 4]); u[e] = r.x + r.y; }
#pragma unroll
      for (int e = 0; e < 2; ++e) { f32x2 r = xswp16(u[e], u[e + 2]); v[e] = ror8_add(r.x + r.y); }
      if (!(lane & 8)) *(LAS f32x2*)(L + L_RED + w * 256 + (lane & 7) * 32 + g4 * 8) = f32x2{v[0], v[1]};
      const unsigned wql = L_WQ + w * 2560;
      if (lr < 6) { *(LAS v4u*)(L + wql + l16) = wq[0]; *(LAS v4u*)(L + wql + 960 + l16) = wq[1]; }
      if (lr < 4) *(LAS v4u*)(L + wql + 1920 + l16) = wq[2];
      if (lds_arrive(L, L_CNT, lane) == 3 && lane < 16) {
        f32x4 t = (ldsf4(L, L_RED + l16) + ldsf4(L, L_RED + 256 + l16)) + (ldsf4(L, L_RED + 512 + l16) + ldsf4(L, L_RED + 768 + l16));
        st16(__builtin_bit_cast(v4u, t), rws, WS_MODP + (kq * 3840 + cc * 64 + lane * 4) * 4);
      }
      const int part = q >> 1, jb = 8 * (q & 1) + 2 * w;
      const int tmain = part == 0 ? 3 : part - 1;
      const unsigned dst0 = WS_WT1 + jb * 163840 + (s >= 20 ? 81920 + (s - 20) * 4096 : s * 4096);
      const unsigned rowa = wql + (8 * h + q4) * 160;
      const unsigned a = rowa + (20 * blk + 4 * p4) * 2, a2 = rowa + (40 * blk + 16 + 20 * (p4 & 1)) * 2;
      v4u f0 = trpair(L, a, a + 640), f1 = trpair(L, a + 80, a + 720), f2 = trpair(L, a2, a2 + 640);
      st16(f0, rws, dst0 + tmain * 1024 + l16);
      st16(f1, rws, dst0 + 163840 + tmain * 1024 + l16);
      if (i16 >= 8) f2 = v4u{0, 0, 0, 0};
      const int r = (part == 0 ? 16 : part == 1 ? 0 : 8) + i16;
      if (part == 0 || i16 < 8) st16(f2, rws, dst0 + blk * 163840 + 2048 + (h * 32 + r) * 16);
    }
  } else if (w == 4) {
    const int r8 = lane & 7, cg = lane >> 3, tok = 8 * b + r8;
    const GAS char* px = (const GAS char*)P.x + (size_t)tok * 1280 + cg * 16;
    v4u xv[10];
#pragma unroll
    for (int i = 0; i < 10; ++i) xv[i] = *(const GAS v4u*)(px + i * 128);
    cbase = __hip_atomic_load(caddr, RLX_AGENT);
    bar();
    float s1 = 0.f, s2 = 0.f, s3 = 0.f, s4 = 0.f;
#pragma unroll
    for (int i = 0; i < 10; ++i) {
      s1 = dot2(xv[i][0], 0x3f803f80u, s1); s2 = dot2(xv[i][0], xv[i][0], s2); s3 = dot2(xv[i][1], 0x3f803f80u, s3); s4 = dot2(xv[i][1], xv[i][1], s4);
      s1 = dot2(xv[i][2], 0x3f803f80u, s1); s2 = dot2(xv[i][2], xv[i][2], s2); s3 = dot2(xv[i][3], 0x3f803f80u, s3); s4 = dot2(xv[i][3], xv[i][3], s4);
    }
    s1 = swap32_add(swap16_add(ror8_add(s1 + s3))); s2 = swap32_add(swap16_add(ror8_add(s2 + s4)));
    const float mean = s1 * (1.f / 640.f);
    const float rstd = __builtin_amdgcn_rsqf(fmaxf(s2 * (1.f / 640.f) - mean * mean, 0.f) + 1e-5f), nm = -mean * rstd;
    const unsigned xo = WS_XH + (tok >> 5) * 40960 + cg * 512 + (tok & 31) * 16;
#pragma unroll
    for (int i = 0; i < 10; ++i) {
      v4u o;
#pragma unroll
      for (int e = 0; e < 4; ++e) o[e] = pk2(fmaf(bflo(xv[i][e]), rstd, nm), fmaf(bfhi(xv[i][e]), rstd, nm));
      st16(o, rws, xo + i * 4096);
    }
  } else bar();
  if (w <= 4) {
    const unsigned tag = __builtin_amdgcn_readfirstlane(cbase) + 1u;
    waitv<0>();
    if (lds_arrive(L, L_CNT + 4, lane) == 4 && lane < 8) __hip_atomic_store((GAS unsigned*)(P.ws + CT_FLAG + lane * 1024 + b * 4), tag, RLX_AGENT);
    STAMP(1);
    if (w == 4) {
      for (unsigned s = 0; s < SPIN_CAP; ++s) {
        v4u f; ld16(f, l16, rws, CT_FLAG + (b & 7) * 1024); waitv<0>(); tie(f);
        if (all64(f.x == tag && f.y == tag && f.z == tag && f.w == tag)) break;
        __builtin_amdgcn_s_sleep(2);
      }
      __builtin_amdgcn_fence(__ATOMIC_ACQUIRE, "wavefront");
    }
  }
  bar();
  STAMP(4);

  const int c8 = 4 * (b & 1) + ((b >> 5) & 3), rh = b >> 7;
  {
    int col = 4 * tid;
    if (tid >= 320) col = 1280 + 80 * c8 + 4 * (tid - 320);
    if (tid >= 340) col = 3200 + 80 * c8 + 4 * (tid - 340);
    if (tid >= 360) col = 2560 + 80 * ((b >> 3) & 7) + 4 * (tid - 360);
    if (tid >= 380) col = 1920 + 80 * ((b >> 3) & 7) + 4 * (tid - 380);
    if (tid >= 400) col = 0;
    v4u mp[4], xf[20];
    sfor<4>([&](auto I) { ld16(mp[I], col * 4, rws, WS_MODP + I * 15360); });
    const unsigned w1 = WS_WT1 + j * 163840 + kh * 81920 + m * 1024;
    const unsigned ab = WS_XH + ((4 * p + m) * 40 + 20 * kh) * 1024;
    sfor<S1::R0>([&](auto R) { dma<true>(ringD + R * 4096, l16, rws, w1 + R * 4096); });
    sfor<S1::A0>([&](auto I) { ld16(xf[I], l16, rws, ab + I * 1024); });
    waitv<S1::R0 + S1::A0>();
    sfor<4>([&](auto I) { tie(mp[I]); });
    {
      f32x4 v = (__builtin_bit_cast(f32x4, mp[0]) + __builtin_bit_cast(f32x4, mp[1])) + (__builtin_bit_cast(f32x4, mp[2]) + __builtin_bit_cast(f32x4, mp[3]));
      const bool isc = (tid >= 160 && tid < 320) || (tid >= 360 && tid < 380);
#pragma unroll
      for (int e = 0; e < 4; ++e) { float t = bfr(v[e]); v[e] = isc ? bfr(1.f + t) : t; }
      if (tid < 400) *(LAS f32x4*)(L + L_MODV + (tid < 160 ? tid * 32 + 16 : tid < 320 ? (tid - 160) * 32 : tid * 16)) = v;
    }
    bar();
    const char* src; int nch, cpr, rstride;
    if (b < 128) { src = (const char*)((b >> 6) ? P.wu : P.wg) + (size_t)(80 * ((b >> 3) & 7)) * 3072 + (b & 7) * 384; cpr = 24; nch = 1920; rstride = 3072; }
    else if (b < 224) { src = (const char*)P.wd + (size_t)(32 * ((b - 128) >> 1)) * 1280 + (b & 1) * 640; cpr = 40; nch = 1280; rstride = 1280; }
    else { src = (const char*)P.wo + (size_t)(160 * ((b - 224) >> 3)) * 1280 + (b & 7) * 160; cpr = 10; nch = 1600; rstride = 1280; }
    const rsrc_t rb = __builtin_amdgcn_make_buffer_rsrc((void*)src, 0, 0x00400000, 0x00020000);
    unsigned bo[4];
#pragma unroll
    for (int i = 0; i < 4; ++i) { int c = (i * 8 + w) * 64 + lane; if (c >= nch) c = 0; bo[i] = (c / cpr) * rstride + (c % cpr) * 16; }
    STAMP(5);
    f32x16 acc[4] = {};
    const unsigned wbK = ringL + kh * 1024, wbS = ringL + 1024 - kh * 1024;
    unsigned cb = L_MODV + (80 * kh + 2 * h) * 32; tie(cb);
    constexpr int D = 4;
    v4u wf[80], hf[20]; f32x4 ca[40], cs[40];
    auto rdw = [&](auto N) { wf[N] = lds16(L, ((N % 4) < 2 ? wbK : wbS) + (((N / 4) * 4) % 60 + (N & 1) * 2) * 1024); };
    auto rdc = [&](auto R) { ca[R] = ldsf4(L, cb + (R / 2) * 128 + (R & 1) * 32); cs[R] = ldsf4(L, cb + (R / 2) * 128 + (R & 1) * 32 + 16); };
    auto aff = [&](auto I, auto E) {
      constexpr int e = E, r = 2 * I + (e >> 1), o = 2 * (e & 1);
      unsigned v = pk2(fmaf(bflo(xf[I][e]), ca[r][o], cs[r][o]), fmaf(bfhi(xf[I][e]), ca[r][o + 1], cs[r][o + 1]));
      tie(v); hf[I][e] = v;
    };
    sfor<4>(rdc);
    sfor<10>([&](auto E) {
      constexpr int e = E;
      waitv<sch_wait<S1>(e)>();
      if constexpr (e == 0) tie(xf[0]);
      tie(xf[2 * e + 1]); if constexpr (e < 9) tie(xf[2 * e + 2]);
      rbar();
      if constexpr (e == 0) { sfor<D>(rdw); sfor<4>([&](auto E2) { aff(IC<0>{}, E2); }); }
      sfor<8>([&](auto K) {
        constexpr int n = 8 * e + K, i = n / 4, t = n % 4, v = K / 2;
        if constexpr (n + D < 80) rdw(IC<n + D>{});
        if constexpr ((t & 1) && i + 2 < 20) rdc(IC<2 * (i + 2) + (t >> 1)>{});
        acc[t] = mfma32(wf[n], hf[i], acc[t]); tie(acc[t]);
        SB();
        if constexpr (i + 1 < 20) aff(IC<i + 1>{}, IC<t>{});
        if constexpr (K & 1) {
          constexpr int nA = S1::nA(e), nW = S1::nW(e);
          if constexpr (v < nA) { constexpr int a = sch_A<S1>(e) + v; ld16(xf[a], l16, rws, ab + a * 1024); }
          else if constexpr (v < nA + nW) { constexpr int r = sch_W<S1>(e) + v - nA; dma<true>(ringD + (r % 15) * 4096, l16, rws, w1 + r * 4096); }
          else if constexpr (v < nA + nW + S1::nX(e)) { constexpr int q = 2 * (e - 7) + v - nA - nW; dma<false>(LB + L_BSRC + (q * 8 + w) * 1024, bo[q], rb, 0); }
        }
        SB();
      });
    });
    rbar();
    STAMP(6);
    f32x16 ka = acc[0], kb = acc[1]; const f32x16 sa = acc[2], sb = acc[3];
#pragma unroll
    for (int qd = 0; qd < 4; ++qd) *(LAS f32x4*)(L + L_X1 + w * 4096 + qd * 1024 + l16) = f32x4{sa[4 * qd], sa[4 * qd + 1], sa[4 * qd + 2], sa[4 * qd + 3]};
    bar();
#pragma unroll
    for (int qd = 0; qd < 4; ++qd) {
      f32x4 u = ldsf4(L, L_X1 + (w ^ 4) * 4096 + qd * 1024 + l16);
#pragma unroll
      for (int e = 0; e < 4; ++e) ka[4 * qd + e] += u[e];
    }
#pragma unroll
    for (int qd = 0; qd < 4; ++qd) *(LAS f32x4*)(L + L_X1 + (w ^ 4) * 4096 + qd * 1024 + l16) = f32x4{sb[4 * qd], sb[4 * qd + 1], sb[4 * qd + 2], sb[4 * qd + 3]};
    const unsigned kvg = WS_KV + ((4 * p + m) * 16 + j) * 5120, kvl = (8 + m) * KVT;
    {
      v4u c0 = {pk2(ka[0], ka[1]), pk2(ka[2], ka[3]), pk2(ka[4], ka[5]), pk2(ka[6], ka[7])};
      v4u c1 = {pk2(ka[8], ka[9]), pk2(ka[10], ka[11]), pk2(ka[12], ka[13]), pk2(ka[14], ka[15])};
      const unsigned go = kh ? 3072 + r32 * 32 + h * 16 : l16, lo = kh ? 2560 + r32 * 48 + h * 16 : l16, ls = kh ? 1536 : 1024;
      st16(c0, rws, kvg + go); st16(c1, rws, kvg + go + 1024);
      *(LAS v4u*)(L + kvl + lo) = c0; *(LAS v4u*)(L + kvl + lo + ls) = c1;
    }
    bar();
#pragma unroll
    for (int qd = 0; qd < 4; ++qd) {
      f32x4 u = ldsf4(L, L_X1 + w * 4096 + qd * 1024 + l16);
#pragma unroll
      for (int e = 0; e < 4; ++e) kb[4 * qd + e] += u[e];
    }
    const float qs = 0.22360679775f * 1.44269504089f;
    if (kh == 0) {
      v4u c2 = {pk2(kb[0], kb[1]), pk2(kb[2], kb[3]), pk2(kb[4], kb[5]), pk2(kb[6], kb[7])};
      st16(c2, rws, kvg + 2048 + l16);
      *(LAS v2u*)(L + kvl + 2048 + lane * 8) = v2u{c2.x, c2.y};
      *(LAS v2u*)(L + kvl + 2560 + h * 1536 + r32 * 48 + 32) = v2u{c2.z, c2.w};
      *(LAS v2u*)(L + L_QHI + m * 512 + lane * 8) = v2u{pk2(kb[8] * qs, kb[9] * qs), pk2(kb[10] * qs, kb[11] * qs)};
    } else {
      *(LAS v4u*)(L + L_Q + m * 2048 + l16) = v4u{pk2(kb[0] * qs, kb[1] * qs), pk2(kb[2] * qs, kb[3] * qs), pk2(kb[4] * qs, kb[5] * qs), pk2(kb[6] * qs, kb[7] * qs)};
      *(LAS v4u*)(L + L_Q + m * 2048 + 1024 + l16) = v4u{pk2(kb[8] * qs, kb[9] * qs), pk2(kb[10] * qs, kb[11] * qs), pk2(kb[12] * qs, kb[13] * qs), pk2(kb[14] * qs, kb[15] * qs)};
    }
    waitv<0>();
    bar();
    if (tid == 0) __hip_atomic_fetch_add((GAS unsigned*)(P.ws + CT_C1 + ((p * 16 + j) << 7)), 1u, RLX_AGENT);
    STAMP(7);
  }

  *(LAS v2u*)(L + w * KVT + 2560 + h * 1536 + r32 * 48 + 40) = v2u{0x3f80u, 0u};
  if (w < 4) *(LAS v2u*)(L + (8 + w) * KVT + 2560 + h * 1536 + r32 * 48 + 40) = v2u{0x3f80u, 0u};
  if (b < 128) {
    if (w < 6) {
      const int mat = b >> 6, kp = (b >> 3) & 7, cg = b & 7;
      const int tl = 2 * w + blk;
      const int jd = 2 * cg + tl / 6, t = tl % 6;
      float us = 0.f, vs = 0.f;
#pragma unroll
      for (int sg = 0; sg < 5; ++sg) {
        const int k0 = sg < 4 ? 32 * (sg >> 1) + 4 * (2 * (sg & 1) + h) : 64 + 8 * h;
        const int k1 = sg < 4 ? k0 + 16 : k0 + 4;
        const unsigned ca = L_BSRC + q4 * 384 + (16 * tl + 4 * p4) * 2;
        v4u f = trpair(L, ca + k0 * 384, ca + k1 * 384);
        f32x4 a0 = ldsf4(L, L_MODV + 5760 + k0 * 4), a1 = ldsf4(L, L_MODV + 5760 + k1 * 4);
        f32x4 b0 = ldsf4(L, L_MODV + 6080 + k0 * 4), b1 = ldsf4(L, L_MODV + 6080 + k1 * 4);
        v4u o;
        o[0] = pk2(bflo(f[0]) * a0[0], bfhi(f[0]) * a0[1]); o[1] = pk2(bflo(f[1]) * a0[2], bfhi(f[1]) * a0[3]);
        o[2] = pk2(bflo(f[2]) * a1[0], bfhi(f[2]) * a1[1]); o[3] = pk2(bflo(f[3]) * a1[2], bfhi(f[3]) * a1[3]);
        vs += bflo(f[0]) * b0[0] + bfhi(f[0]) * b0[1] + bflo(f[1]) * b0[2] + bfhi(f[1]) * b0[3] + bflo(f[2]) * b1[0] + bfhi(f[2]) * b1[1] + bflo(f[3]) * b1[2] + bfhi(f[3]) * b1[3];
        us = dot2(o[0], 0x3f803f80u, us); us = dot2(o[1], 0x3f803f80u, us); us = dot2(o[2], 0x3f803f80u, us); us = dot2(o[3], 0x3f803f80u, us);
        const int s = 5 * kp + sg;
        st16(o, rws, WS_WT4 + jd * 245760 + (s / 20) * 122880 + ((s % 20) * 6 + t) * 1024 + (h * 32 + mat * 16 + i16) * 16);
      }
      us = swap32_add(us); vs = swap32_add(vs);
      const int row = (jd * 6 + t) * 32 + mat * 16 + i16;
      __builtin_amdgcn_raw_buffer_store_b32(__builtin_bit_cast(unsigned, h ? vs : us), rws, WS_UV + ((kp * 2 + h) * 3072 + row) * 4, 0, 16);
    }
  } else if (b < 224) {
    const int st = (b - 128) >> 1, ch = b & 1;
    for (int tt = w; tt < 20; tt += 8) {
      const unsigned a = L_BSRC + (16 * (g4 >> 1) + 4 * (g4 & 1) + q4) * 640 + (16 * tt + 4 * p4) * 2;
      v4u f = trpair(L, a, a + 8 * 640);
      st16(f, rws, WS_WT5 + (4 * ch + tt / 5) * 245760 + (st & 1) * 122880 + ((st >> 1) * 5 + tt % 5) * 1024 + l16);
    }
  } else {
    const int kr = (b - 224) >> 3, cw = b & 7;
    for (int u = w; u < 25; u += 8) {
      const int sl = u / 5, t = u % 5, c = 4 * sl + g4, jl = c / 5, ii = c % 5;
      const int r0 = ii < 4 ? 40 * jl + 20 * (ii >> 1) + 4 * (ii & 1) : 40 * jl + 16;
      const int r1 = ii < 4 ? r0 + 8 : r0 + 20;
      const unsigned ca = L_BSRC + q4 * 160 + (16 * t + 4 * p4) * 2;
      v4u f = trpair(L, ca + r0 * 160, ca + r1 * 160);
      const int s = 5 * kr + sl;
      st16(f, rws, WS_WT3 + cw * 102400 + (s & 1) * 51200 + ((s >> 1) * 5 + t) * 1024 + l16);
    }
  }
  STAMP(8);
  if (w == 0) poll(caddr, cbase, 1u, (lane == 1 && p >= 1) || (lane == 2 && p >= 2));
  bar();
  STAMP(9);
  unsigned cbv;
  {
    v4u kv[5];
    const int gt = 4 * p - 8 + w;
    const unsigned ko = gt >= 0 ? WS_KV + (gt * 16 + j) * 5120 : 0x7ff00000u;
    sfor<5>([&](auto I) { ld16(kv[I], l16, rws, ko + I * 1024); });
    waitv<0>();
    sfor<5>([&](auto I) { tie(kv[I]); });
    {
      const unsigned tb = w * KVT, vo = 2560 + (lane >> 1) * 48 + (lane & 1) * 16;
      *(LAS v4u*)(L + tb + l16) = kv[0]; *(LAS v4u*)(L + tb + 1024 + l16) = kv[1];
      *(LAS v2u*)(L + tb + 2048 + lane * 8) = v2u{kv[2].x, kv[2].y};
      *(LAS v2u*)(L + tb + 2560 + h * 1536 + r32 * 48 + 32) = v2u{kv[2].z, kv[2].w};
      *(LAS v4u*)(L + tb + vo) = kv[3]; *(LAS v4u*)(L + tb + vo + 1536) = kv[4];
    }
    bar();
    if (tid == 0) __hip_atomic_fetch_add((GAS unsigned*)(P.ws + (b < 224 ? CT_CB + ((b & 7) << 7) : CT_CW)), 1u, RLX_AGENT);
    STAMP(10);
    const int hd = kh;
    const v4u q0 = lds16(L, L_Q + m * 2048 + hd * 1024 + l16);
    v2u qh = *(LAS v2u*)(L + L_QHI + m * 512 + lane * 8);
    if (h != hd) qh = v2u{0, 0};
    const v4u q1 = {qh.x, qh.y, 0, 0};
    f32x16 S[9];
    const int nskip = 8 - 4 * p - m;
    const f32x16 zero = {};
    const unsigned ka = m * KVT + hd * 1024 + l16, kb = m * KVT + 2048 + lane * 8;
    sfor<9>([&](auto I) {
      const v2u k1 = *(LAS v2u*)(L + kb + I * KVT);
      S[I] = mfma32(lds16(L, ka + I * KVT), q0, zero);
      S[I] = mfma32(v4u{k1.x, k1.y, 0, 0}, q1, S[I]);
    });
#pragma unroll
    for (int r = 0; r < 16; ++r) {
      const bool le = (r & 3) + 8 * (r >> 2) + 4 * h <= r32;
      S[0][r] = le ? -3.0e38f : S[0][r];
      S[8][r] = le ? S[8][r] : -3.0e38f;
    }
    float mx = -3.0e38f;
    sfor<9>([&](auto I) {
      float t = fmaxf(S[I][0], S[I][1]);
#pragma unroll
      for (int r = 2; r < 16; r += 2) t = fmaxf(fmaxf(S[I][r], S[I][r + 1]), t);
      if constexpr (I < 8) t = I >= nskip ? t : -3.0e38f;
      mx = fmaxf(mx, t);
    });
    mx = swap32_max(mx);
    f32x16 O = {};
    const unsigned va = m * KVT + 2560 + hd * 1536 + (4 * h + q4) * 48 + (blk == 0 ? (p4 & 1) * 16 + (p4 >> 1) * 8 : (p4 ? 40 : 32));
    sfor<9>([&](auto I) {
      float mi = mx;
      if constexpr (I < 8) mi = I >= nskip ? mx : 3.0e38f;
      if constexpr (I == 6) asm volatile("global_load_dword %0, %1, off sc1" : "=v"(cbv) : "v"(caddr) : "memory");
#pragma unroll
      for (int s = 0; s < 2; ++s) {
        v4u pf;
#pragma unroll
        for (int e = 0; e < 4; ++e) pf[e] = pk2(__builtin_amdgcn_exp2f(S[I][8 * s + 2 * e] - mi), __builtin_amdgcn_exp2f(S[I][8 * s + 2 * e + 1] - mi));
        O = mfma32(trpair(L, va + I * KVT + s * 768, va + I * KVT + s * 768 + 384), pf, O);
      }
    });
    const float inv = __builtin_amdgcn_rcpf(swap32_hi(O[8]));
    const int tok = 128 * p + 32 * m + r32;
    const unsigned ob = WS_OF + (tok >> 4) * 20480 + (tok & 15) * 16;
    const int c = 5 * j + 2 * hd + h, c2 = 5 * j + 4;
    STAMP(11);
    st16(v4u{pk2(O[0] * inv, O[1] * inv), pk2(O[2] * inv, O[3] * inv), pk2(O[4] * inv, O[5] * inv), pk2(O[6] * inv, O[7] * inv)}, rws, ob + (c >> 2) * 1024 + (c & 3) * 256);
    if (h == 0) st8(v2u{pk2(O[8] * inv, O[9] * inv), pk2(O[10] * inv, O[11] * inv)}, rws, ob + (c2 >> 2) * 1024 + (c2 & 3) * 256 + hd * 8);
  }
  waitv<0>();
  tie(cbv);
  if (w == 0 && !all64(lane != 6 || cbv - cbase >= 32u)) poll(caddr, cbase, 32u, lane == 6);
  bar();
  const int tt = 8 * p + 4 * rh + m;
  unsigned x2k[6];
  {
    const unsigned w3 = WS_WT3 + c8 * 102400 + kh * 51200 + m * 1024;
    v2u xr[3];
    const unsigned xo = (16 * tt + i16) * 1280 + (80 * c8 + 4 * g4) * 2;
    if (tid == 0) __hip_atomic_fetch_add((GAS unsigned*)(P.ws + CT_C2 + (p << 7)), 1u, RLX_AGENT);
    STAMP(12);
    ld8p(xr[0], xo, rx, kh * 64); ld8p(xr[1], xo, rx, kh * 64 + 32); ld8p(xr[2], xo, rx, 128);
    sfor<13>([&](auto R) { dma<true>(ringD + R * 4096, l16, rws, w3 + R * 4096); });
    if (w == 0) poll(caddr, cbase, 16u, lane == 3);
    bar();
    STAMP(13);
    v4u af[10], wf[50];
    const unsigned ab = WS_OF + (tt * 20 + kh) * 1024;
    sfor<10>([&](auto I) { ld16(af[I], l16, rws, ab + I * 2048); });
    asm volatile("global_load_dword %0, %1, off sc1" : "=v"(cbv) : "v"(caddr) : "memory");
    f32x4 acc[5] = {};
    constexpr int D = 6;
    auto rdw = [&](auto N) { wf[N] = lds16(L, ringL + N * 1024); };
    sfor<10>([&](auto E) {
      constexpr int e = E;
      waitv<10 - e>();
      tie(af[e]);
      if constexpr (e == 0) { rbar(); sfor<D>(rdw); }
      sfor<5>([&](auto K) {
        constexpr int n = 5 * e + K;
        if constexpr (n + D < 50) rdw(IC<n + D>{});
        acc[K] = mfma16(wf[n], af[e], acc[K]); tie(acc[K]);
        SB();
      });
    });
    STAMP(14);
    const unsigned xs = L_BSRC + m * 5120;
    if (kh == 0) { *(LAS f32x4*)(L + xs + l16) = acc[2]; *(LAS f32x4*)(L + xs + 1024 + l16) = acc[3]; }
    else { *(LAS f32x4*)(L + xs + 2048 + l16) = acc[0]; *(LAS f32x4*)(L + xs + 3072 + l16) = acc[1]; *(LAS f32x4*)(L + xs + 4096 + l16) = acc[4]; }
    bar();
    tie(xr[0]); tie(xr[1]); tie(xr[2]);
    f32x4 r0, r1, r2 = {};
    if (kh == 0) { r0 = acc[0] + ldsf4(L, xs + 2048 + l16); r1 = acc[1] + ldsf4(L, xs + 3072 + l16); r2 = acc[4] + ldsf4(L, xs + 4096 + l16); }
    else { r0 = acc[2] + ldsf4(L, xs + l16); r1 = acc[3] + ldsf4(L, xs + 1024 + l16); }
    const f32x4 ga0 = ldsf4(L, L_MODV + 5120 + (32 * kh + 4 * g4) * 4), ga1 = ldsf4(L, L_MODV + 5120 + (32 * kh + 16 + 4 * g4) * 4), ga2 = ldsf4(L, L_MODV + 5120 + (64 + 4 * g4) * 4);
    x2k[0] = pk2(bflo(xr[0].x) + ga0[0] * r0[0], bfhi(xr[0].x) + ga0[1] * r0[1]); x2k[1] = pk2(bflo(xr[0].y) + ga0[2] * r0[2], bfhi(xr[0].y) + ga0[3] * r0[3]);
    x2k[2] = pk2(bflo(xr[1].x) + ga1[0] * r1[0], bfhi(xr[1].x) + ga1[1] * r1[1]); x2k[3] = pk2(bflo(xr[1].y) + ga1[2] * r1[2], bfhi(xr[1].y) + ga1[3] * r1[3]);
    x2k[4] = pk2(bflo(xr[2].x) + ga2[0] * r2[0], bfhi(xr[2].x) + ga2[1] * r2[1]); x2k[5] = pk2(bflo(xr[2].y) + ga2[2] * r2[2], bfhi(xr[2].y) + ga2[3] * r2[3]);
    if (kh) { x2k[4] = 0; x2k[5] = 0; }
    const unsigned xb = WS_X2F + ((tt >> 1) * 40 + 5 * c8) * 1024 + ((tt & 1) * 16 + i16) * 16;
    st16(v4u{x2k[0], x2k[1], x2k[2], x2k[3]}, rws, xb + (2 * kh + (g4 >> 1)) * 1024 + (g4 & 1) * 512);
    if (kh == 0) st8(v2u{x2k[4], x2k[5]}, rws, xb + 4096 + (g4 >> 1) * 512 + (g4 & 1) * 8);
    float s1 = 0.f, s2 = 0.f;
#pragma unroll
    for (int e = 0; e < 6; ++e) { s1 = dot2(x2k[e], 0x3f803f80u, s1); s2 = dot2(x2k[e], x2k[e], s2); }
    s1 = swap32_add(swap16_add(s1)); s2 = swap32_add(swap16_add(s2));
    if (g4 == 0) st8(v2u{__builtin_bit_cast(unsigned, s1), __builtin_bit_cast(unsigned, s2)}, rws, WS_ST + ((tt >> 1) * 8 + c8) * 512 + ((tt & 1) * 16 + i16) * 16 + kh * 8);
  }
  waitv<0>();
  tie(cbv);
  if (w == 0 && !all64(lane < 8 || lane >= 16 || cbv - cbase >= 28u)) poll(caddr, cbase, 28u, lane >= 8 && lane < 16);
  bar();
  {
    const unsigned w4 = WS_WT4 + j * 245760 + kh * 122880 + m * 1024;
    if (tid == 0) __hip_atomic_fetch_add((GAS unsigned*)(P.ws + CT_C3 + (p << 7)), 1u, RLX_AGENT);
    STAMP(15);
    sfor<S4::R0>([&](auto R) { dma<true>(ringD + R * 4096, l16, rws, w4 + R * 4096); });
    if (w == 0) poll(caddr, cbase, 16u, lane == 4);
    bar();
    STAMP(16);
    v4u af[20], stv[4], wf[120]; unsigned uv[8];
    const unsigned ab = WS_X2F + ((4 * p + m) * 40 + 20 * kh) * 1024;
    sfor<S4::A0>([&](auto I) { ld16(af[I], l16, rws, ab + I * 1024); });
    f32x16 acc[6] = {};
    const unsigned wbK = ringL + kh * 3072, wbS = ringL + 3072 - kh * 3072;
    constexpr int D = 4;
    auto rdw = [&](auto N) { wf[N] = lds16(L, ((N % 6) < 3 ? wbK : wbS) + (((N / 6) * 6) % 60 + N % 3) * 1024); };
    const int ti = tid < 384 ? tid : 0;
    const unsigned uo = ((ti / 192) * 3072 + j * 192 + ti % 192) * 4;
    sfor<10>([&](auto E) {
      constexpr int e = E;
      waitv<sch_wait<S4>(e)>();
      tie(af[2 * e]); tie(af[2 * e + 1]);
      rbar();
      if constexpr (e == 0) sfor<D>(rdw);
      sfor<12>([&](auto K) {
        constexpr int n = 12 * e + K, v = K / 2, nA = S4::nA(e), nW = S4::nW(e);
        if constexpr (n + D < 120) rdw(IC<n + D>{});
        acc[n % 6] = mfma32(wf[n], af[n / 6], acc[n % 6]); tie(acc[n % 6]);
        SB();
        if constexpr (!(K & 1)) {
          if constexpr (v < nA) { constexpr int a = sch_A<S4>(e) + v; ld16(af[a], l16, rws, ab + a * 1024); }
          else if constexpr (v < nA + nW) { constexpr int r = sch_W<S4>(e) + v - nA; dma<true>(ringD + (r % 15) * 4096, l16, rws, w4 + r * 4096); }
          else if constexpr (v < nA + nW + S4::nX(e)) {
            constexpr int x = (e - 6) * 4 + v - nA - nW + (e > 6 ? 0 : 0) + (e == 8 ? 2 : 0);
            if constexpr (x < 4) ld16(stv[x], r32 * 16 + h * 2048 + x * 512, rws, WS_ST + (4 * p + m) * 4096);
            else ld4(uv[x - 4], uo, rws, WS_UV + (x - 4) * 24576);
          }
          SB();
        }
      });
    });
    STAMP(17);
    waitv<0>();
    sfor<4>([&](auto I) { tie(stv[I]); });
    sfor<8>([&](auto I) { tie(uv[I]); });
    {
      float s = 0.f;
#pragma unroll
      for (int i = 0; i < 8; ++i) s += __builtin_bit_cast(float, uv[i]);
      if (tid < 384) *(LAS float*)(L + L_UVS + tid * 4) = s;
    }
    float s1 = 0.f, s2 = 0.f;
#pragma unroll
    for (int i = 0; i < 4; ++i) { f32x4 v = __builtin_bit_cast(f32x4, stv[i]); s1 += v[0] + v[2]; s2 += v[1] + v[3]; }
    s1 = swap32_add(s1); s2 = swap32_add(s2);
    const float mean = s1 * (1.f / 640.f);
    const float rs2 = __builtin_amdgcn_rsqf(fmaxf(s2 * (1.f / 640.f) - mean * mean, 0.f) + 1e-5f), nm2 = -mean * rs2;
    const int tok = 128 * p + 32 * m + r32;
    const unsigned ao = WS_ACT + ((tok >> 4) * 48 + 3 * j) * 1024 + h * 256 + (tok & 15) * 16;
    sfor<3>([&](auto T) {
      constexpr int t = T;
      const f32x16 sd = acc[3 + t];
      const unsigned wr = L_BSRC + ((t & 1) ? (w ^ 4) : w) * 4096 + l16, rd = L_BSRC + ((t & 1) ? w : (w ^ 4)) * 4096 + l16;
#pragma unroll
      for (int qd = 0; qd < 4; ++qd) *(LAS f32x4*)(L + wr + qd * 1024) = f32x4{sd[4 * qd], sd[4 * qd + 1], sd[4 * qd + 2], sd[4 * qd + 3]};
      bar();
      f32x16 kp = acc[t];
      const int tg = 3 * kh + t;
      float a[8];
#pragma unroll
      for (int qd = 0; qd < 4; ++qd) {
        const f32x4 o = ldsf4(L, rd + qd * 1024);
        const f32x4 uu = ldsf4(L, L_UVS + (tg * 32 + 8 * qd + 4 * h) * 4), vv = ldsf4(L, L_UVS + 768 + (tg * 32 + 8 * qd + 4 * h) * 4);
#pragma unroll
        for (int e = 0; e < 4; ++e) kp[4 * qd + e] = fmaf(kp[4 * qd + e] + o[e], rs2, fmaf(nm2, uu[e], vv[e]));
      }
#pragma unroll
      for (int e = 0; e < 8; ++e) { const float gv = kp[e]; a[e] = gv * __builtin_amdgcn_rcpf(1.f + __builtin_amdgcn_exp2f(-1.44269504f * gv)) * kp[8 + e]; }
      st16(v4u{pk2(a[0], a[1]), pk2(a[2], a[3]), pk2(a[4], a[5]), pk2(a[6], a[7])}, rws, ao + (tg >> 1) * 1024 + (tg & 1) * 512);
    });
  }
  waitv<0>();
  bar();
  {
    const unsigned w5 = WS_WT5 + c8 * 245760 + kh * 122880 + m * 1024;
    if (tid == 0) __hip_atomic_fetch_add((GAS unsigned*)(P.ws + CT_C4 + (p << 7)), 1u, RLX_AGENT);
    STAMP(18);
    sfor<S5::R0>([&](auto R) { dma<true>(ringD + R * 4096, l16, rws, w5 + R * 4096); });
    if (w == 0) poll(caddr, cbase, 16u, lane == 5);
    bar();
    STAMP(19);
    v4u af[24], wf[120];
    const unsigned ab = WS_ACT + (tt * 48 + kh) * 1024;
    sfor<S5::A0>([&](auto I) { ld16(af[I], l16, rws, ab + I * 2048); });
    f32x4 acc[5] = {};
    constexpr int D = 6;
    auto rdw = [&](auto N) { wf[N] = lds16(L, ringL + (N % 60) * 1024); };
    sfor<12>([&](auto E) {
      constexpr int e = E;
      waitv<sch_wait<S5>(e)>();
      tie(af[2 * e]); tie(af[2 * e + 1]);
      rbar();
      if constexpr (e == 0) sfor<D>(rdw);
      sfor<10>([&](auto K) {
        constexpr int n = 10 * e + K, v = K / 2, nA = S5::nA(e), nW = S5::nW(e);
        if constexpr (n + D < 120) rdw(IC<n + D>{});
        acc[n % 5] = mfma16(wf[n], af[n / 5], acc[n % 5]); tie(acc[n % 5]);
        SB();
        if constexpr (!(K & 1)) {
          if constexpr (v < nA) { constexpr int a = sch_A<S5>(e) + v; ld16(af[a], l16, rws, ab + a * 2048); }
          else if constexpr (v < nA + nW) { constexpr int r = sch_W<S5>(e) + v - nA; dma<true>(ringD + (r % 15) * 4096, l16, rws, w5 + r * 4096); }
          SB();
        }
      });
    });
    STAMP(20);
    const unsigned xs = L_BSRC + m * 5120;
    if (kh == 0) { *(LAS f32x4*)(L + xs + l16) = acc[2]; *(LAS f32x4*)(L + xs + 1024 + l16) = acc[3]; }
    else { *(LAS f32x4*)(L + xs + 2048 + l16) = acc[0]; *(LAS f32x4*)(L + xs + 3072 + l16) = acc[1]; *(LAS f32x4*)(L + xs + 4096 + l16) = acc[4]; }
    bar();
    f32x4 r0, r1, r2 = {};
    if (kh == 0) { r0 = acc[0] + ldsf4(L, xs + 2048 + l16); r1 = acc[1] + ldsf4(L, xs + 3072 + l16); r2 = acc[4] + ldsf4(L, xs + 4096 + l16); }
    else { r0 = acc[2] + ldsf4(L, xs + l16); r1 = acc[3] + ldsf4(L, xs + 1024 + l16); }
    const f32x4 g0 = ldsf4(L, L_MODV + 5440 + (32 * kh + 4 * g4) * 4), g1 = ldsf4(L, L_MODV + 5440 + (32 * kh + 16 + 4 * g4) * 4), g2 = ldsf4(L, L_MODV + 5440 + (64 + 4 * g4) * 4);
    GAS char* o = (GAS char*)P.out + (size_t)(16 * tt + i16) * 1280 + (80 * c8 + 4 * g4) * 2;
    *(GAS v2u*)(o + kh * 64) = v2u{pk2(bflo(x2k[0]) + g0[0] * r0[0], bfhi(x2k[0]) + g0[1] * r0[1]), pk2(bflo(x2k[1]) + g0[2] * r0[2], bfhi(x2k[1]) + g0[3] * r0[3])};
    *(GAS v2u*)(o + kh * 64 + 32) = v2u{pk2(bflo(x2k[2]) + g1[0] * r1[0], bfhi(x2k[2]) + g1[1] * r1[1]), pk2(bflo(x2k[3]) + g1[2] * r1[2], bfhi(x2k[3]) + g1[3] * r1[3])};
    if (kh == 0) *(GAS v2u*)(o + 128) = v2u{pk2(bflo(x2k[4]) + g2[0] * r2[0], bfhi(x2k[4]) + g2[1] * r2[1]), pk2(bflo(x2k[5]) + g2[2] * r2[2], bfhi(x2k[5]) + g2[3] * r2[3])};
    if (b == 0 && tid == 0) __hip_atomic_store((GAS unsigned*)(P.ws + CT_E), cbase + 1u, RLX_AGENT);
    STAMP(21);
  }
}

#if defined(__HIP_DEVICE_COMPILE__)
#pragma clang attribute pop
#endif
extern "C" void kernel_launch(void* const* d_in, const int* in_sizes, int n_in, void* d_out, int out_size, void* d_ws, size_t ws_size,
                              hipStream_t stream) {
  Params P;
  P.x = (const u16*)d_in[0]; P.wqkv = (const u16*)d_in[1]; P.wo = (const u16*)d_in[2];
  P.wg = (const u16*)d_in[4]; P.wu = (const u16*)d_in[5]; P.wd = (const u16*)d_in[6];
  P.c = (const u16*)d_in[7]; P.wmod = (const u16*)d_in[8];
  P.out = (u16*)d_out; P.ws = (char*)d_ws;
  fused_block<<<256, 512, 0, stream>>>(P);
}
```

```cpp
#include <hip/hip_runtime.h>
#include <hip/hip_bf16.h>
#include <utility>

typedef unsigned short u16;
typedef unsigned v4u __attribute__((ext_vector_type(4)));
typedef unsigned v2u __attribute__((ext_vector_type(2)));
typedef short v4s __attribute__((ext_vector_type(4)));
typedef float f32x2 __attribute__((ext_vector_type(2)));
typedef float f32x4 __attribute__((ext_vector_type(4)));
typedef float f32x16 __attribute__((ext_vector_type(16)));
typedef __bf16 bf16x2 __attribute__((ext_vector_type(2)));
typedef __bf16 bf16x8 __attribute__((ext_vector_type(8)));
typedef __amdgpu_buffer_rsrc_t rsrc_t;
#define LAS __attribute__((address_space(3)))
#define GAS __attribute__((address_space(1)))
#define DI __device__ __forceinline__
#define RLX_AGENT __ATOMIC_RELAXED, __HIP_MEMORY_SCOPE_AGENT

#define STAMPS 0

constexpr int WS_CTRL = 0;
constexpr int WS_MODP = 65536;
constexpr int WS_UV   = 131072;
constexpr int WS_ST   = 327680;
constexpr int WS_WT1  = 655360;
constexpr int WS_WT3  = WS_WT1 + 16 * 163840;
constexpr int WS_WT4  = WS_WT3 + 8 * 102400;
constexpr int WS_WT5  = WS_WT4 + 16 * 245760;
constexpr int WS_KV   = WS_WT5 + 8 * 245760;
constexpr int WS_OF   = WS_KV + 64 * 16 * 5120;
constexpr int WS_X2F  = WS_OF + 128 * 20 * 1024;
constexpr int WS_ACT  = WS_X2F + 64 * 40 * 1024;
constexpr int WS_XH   = WS_ACT + 128 * 48 * 1024;
constexpr int WS_END  = WS_XH + 64 * 40 * 1024;
constexpr int WS_DBG  = WS_END;
constexpr int CT_E = 0, CT_FLAG = 4096  , CT_C1 = 16384  , CT_C2 = 49152, CT_C3 = CT_C2 + 2048,
              CT_C4 = CT_C3 + 2048, CT_CB = CT_C4 + 2048  , CT_CW = CT_CB + 1024, CT_CM = CT_CW + 128;
constexpr int L_WQ = 0, L_RED = 10240;
constexpr int L_BSRC = 122880;
constexpr int L_MODV = 155648;
constexpr int L_CNT = 162816;
constexpr int L_UVS = 155648;
constexpr int KVT = 6144;
constexpr int L_Q = 73728, L_QHI = 81920, L_X1 = 86016, L_W3A = 86016;
constexpr unsigned SPIN_CAP = 1u << 20;

#if defined(__HIP_DEVICE_COMPILE__)
#pragma clang attribute push (__attribute__((target("no-packed-fp32-ops"))), apply_to = function)
#endif
struct Params { const u16 *x, *wqkv, *wo, *wg, *wu, *wd, *c, *wmod; u16* out; char* ws; };

DI f32x16 mfma32(v4u a, v4u b, f32x16 c) { return __builtin_amdgcn_mfma_f32_32x32x16_bf16(__builtin_bit_cast(bf16x8, a), __builtin_bit_cast(bf16x8, b), c, 0, 0, 0); }
DI f32x4 mfma16(v4u a, v4u b, f32x4 c) { return __builtin_amdgcn_mfma_f32_16x16x32_bf16(__builtin_bit_cast(bf16x8, a), __builtin_bit_cast(bf16x8, b), c, 0, 0, 0); }
DI unsigned pk2(float a, float b) { f32x2 v = {a, b}; return __builtin_bit_cast(unsigned, __builtin_convertvector(v, bf16x2)); }
DI float bflo(unsigned x) { return __builtin_bit_cast(float, x << 16); }
DI float bfhi(unsigned x) { return __builtin_bit_cast(float, x & 0xffff0000u); }
DI float bfr(float a) { return bflo(pk2(a, a)); }
DI f32x2 swp32(float v) {
  unsigned a = __builtin_bit_cast(unsigned, v), c = a; asm("" : "+v"(c));
  auto r = __builtin_amdgcn_permlane32_swap(a, c, false, false);
  unsigned r0 = r[0], r1 = r[1];
  return f32x2{__builtin_bit_cast(float, r0), __builtin_bit_cast(float, r1)};
}
DI f32x2 swp16(float v) {
  unsigned a = __builtin_bit_cast(unsigned, v), c = a; asm("" : "+v"(c));
  auto r = __builtin_amdgcn_permlane16_swap(a, c, false, false);
  unsigned r0 = r[0], r1 = r[1];
  return f32x2{__builtin_bit_cast(float, r0), __builtin_bit_cast(float, r1)};
}
DI float swap32_add(float v) { f32x2 r = swp32(v); return r.x + r.y; }
DI float swap16_add(float v) { f32x2 r = swp16(v); return r.x + r.y; }
DI float swap32_max(float v) { f32x2 r = swp32(v); return fmaxf(r.x, r.y); }
DI float swap32_hi(float v) { return swp32(v).y; }
DI bool all64(bool c) { return __builtin_amdgcn_ballot_w64(!c) == 0ull; }
DI float ror8_add(float v) { return v + __builtin_bit_cast(float, __builtin_amdgcn_update_dpp(0, __builtin_bit_cast(int, v), 0x128, 0xf, 0xf, true)); }

template <bool SC1> DI void dma(unsigned lds_addr, unsigned voff, rsrc_t rs, unsigned soff) {
  if (SC1) asm volatile("s_mov_b32 m0, %0\n\ts_nop 0\n\tbuffer_load_dwordx4 %1, %2, %3 offen sc1 lds" ::"s"(lds_addr), "v"(voff), "s"(rs), "s"(soff) : "memory");
  else     asm volatile("s_mov_b32 m0, %0\n\ts_nop 0\n\tbuffer_load_dwordx4 %1, %2, %3 offen lds" ::"s"(lds_addr), "v"(voff), "s"(rs), "s"(soff) : "memory");
}
DI void ld16(v4u& d, unsigned voff, rsrc_t rs, unsigned soff) { asm volatile("buffer_load_dwordx4 %0, %1, %2, %3 offen sc1" : "=v"(d) : "v"(voff), "s"(rs), "s"(soff) : "memory"); }
DI void ld16p(v4u& d, unsigned voff, rsrc_t rs, unsigned soff) { asm volatile("buffer_load_dwordx4 %0, %1, %2, %3 offen" : "=v"(d) : "v"(voff), "s"(rs), "s"(soff) : "memory"); }
DI void ld8p(v2u& d, unsigned voff, rsrc_t rs, unsigned soff) { asm volatile("buffer_load_dwordx2 %0, %1, %2, %3 offen" : "=v"(d) : "v"(voff), "s"(rs), "s"(soff) : "memory"); }
DI void ld2p(unsigned& d, unsigned voff, rsrc_t rs, unsigned soff) { asm volatile("buffer_load_ushort %0, %1, %2, %3 offen" : "=v"(d) : "v"(voff), "s"(rs), "s"(soff) : "memory"); }
DI void ld4(unsigned& d, unsigned voff, rsrc_t rs, unsigned soff) { asm volatile("buffer_load_dword %0, %1, %2, %3 offen sc1" : "=v"(d) : "v"(voff), "s"(rs), "s"(soff) : "memory"); }
template <int N> DI void waitv() { asm volatile("s_waitcnt vmcnt(%0)" ::"n"(N) : "memory"); }
template <class T> DI void tie(T& a) { asm volatile("" : "+v"(a)); }
DI void rbar() { asm volatile("s_barrier" ::: "memory"); }
DI void bar() { asm volatile("s_waitcnt lgkmcnt(0)\n\ts_barrier" ::: "memory"); }
DI void st16(v4u v, rsrc_t rs, unsigned off) { __builtin_amdgcn_raw_buffer_store_b128(v, rs, off, 0, 16); }
DI void st8(v2u v, rsrc_t rs, unsigned off) { __builtin_amdgcn_raw_buffer_store_b64(v, rs, off, 0, 16); }
DI v4u trpair(LAS char* L, unsigned a0, unsigned a1) {
  v4s lo = __builtin_amdgcn_ds_read_tr16_b64_v4i16((LAS v4s*)(L + a0));
  v4s hi = __builtin_amdgcn_ds_read_tr16_b64_v4i16((LAS v4s*)(L + a1));
  v2u l = __builtin_bit_cast(v2u, lo), h = __builtin_bit_cast(v2u, hi);
  return v4u{l.x, l.y, h.x, h.y};
}
DI v4u lds16(LAS char* L, unsigned a) { return *(LAS v4u*)(L + a); }
DI f32x4 ldsf4(LAS char* L, unsigned a) { return *(LAS f32x4*)(L + a); }

template <int... I, class F> DI void sfor_impl(std::integer_sequence<int, I...>, F&& f) { (f(std::integral_constant<int, I>{}), ...); }
template <int N, class F> DI void sfor(F&& f) { sfor_impl(std::make_integer_sequence<int, N>{}, f); }
#define SB() __builtin_amdgcn_sched_barrier(0)

template <int N> using IC = std::integral_constant<int, N>;
DI float dot2(unsigned a, unsigned b, float c) { return __builtin_amdgcn_fdot2_f32_bf16(__builtin_bit_cast(bf16x2, a), __builtin_bit_cast(bf16x2, b), c, false); }
DI f32x2 xswp32(float a, float c) { auto r = __builtin_amdgcn_permlane32_swap(__builtin_bit_cast(unsigned, a), __builtin_bit_cast(unsigned, c), false, false); unsigned r0 = r[0], r1 = r[1]; return f32x2{__builtin_bit_cast(float, r0), __builtin_bit_cast(float, r1)}; }
DI f32x2 xswp16(float a, float c) { auto r = __builtin_amdgcn_permlane16_swap(__builtin_bit_cast(unsigned, a), __builtin_bit_cast(unsigned, c), false, false); unsigned r0 = r[0], r1 = r[1]; return f32x2{__builtin_bit_cast(float, r0), __builtin_bit_cast(float, r1)}; }

template <class S> constexpr int sch_tot(int e) { int t = S::A0; for (int i = 0; i < e; ++i) t += S::nA(i) + S::nW(i) + S::nX(i); return t; }
template <class S> constexpr int sch_W(int e) { int t = S::R0; for (int i = 0; i < e; ++i) t += S::nW(i); return t; }
template <class S> constexpr int sch_A(int e) { int t = S::A0; for (int i = 0; i < e; ++i) t += S::nA(i); return t; }
template <class S> constexpr int sch_posA(int a) { if (a < S::A0) return a; for (int i = 0;; ++i) if (a < sch_A<S>(i + 1)) return sch_tot<S>(i) + (a - sch_A<S>(i)); }
template <class S> constexpr int sch_posW(int r) { if (r < S::R0) return r - S::R0; for (int i = 0;; ++i) if (r < sch_W<S>(i + 1)) return sch_tot<S>(i) + S::nA(i) + (r - sch_W<S>(i)); }
template <class S> constexpr int sch_wait(int e) {
  int nd = (S::PS * (e + 1) + S::LOOK + 3) / 4; if (nd > S::TOT) nd = S::TOT;
  int pw = sch_posW<S>(nd - 1), na = S::APS * (e + 1) - 1 + S::ALOOK, pa = sch_posA<S>(na < S::NA ? na : S::NA - 1);
  return sch_tot<S>(e) - 1 - (pw > pa ? pw : pa);
}
constexpr int cmin(int a, int b) { return a < b ? a : b; }
template <int PS_, int R0_, int RATE, int TOT_> struct Ring {
  static constexpr int upto(int e) { int v = R0_; for (int i = 0; i <= e; ++i) v = cmin(cmin(TOT_, (PS_ * i + 60) / 4), v + RATE); return v; }
  static constexpr int n(int e) { return upto(e) - (e ? upto(e - 1) : R0_); } };
struct S1 { static constexpr int PS = 8, LOOK = 4, R0 = 6, TOT = 20, A0 = 6, APS = 2, ALOOK = 1, NA = 20;
  static constexpr int nA(int e) { return e < 3 ? 4 : e == 3 ? 2 : 0; } static constexpr int nW(int e) { return e < 3 ? 4 : e == 3 ? 2 : 0; }
  static constexpr int nX(int e) { return e == 4 ? 6 : 0; } };
struct S4 { static constexpr int PS = 12, LOOK = 4, R0 = 10, TOT = 30, A0 = 6, APS = 2, ALOOK = 0, NA = 20;
  static constexpr int nA(int e) { return e < 7 ? 2 : 0; } static constexpr int nW(int e) { return Ring<PS, R0, 4, TOT>::n(e); }
  static constexpr int nX(int e) { return e == 4 ? 4 : e == 5 ? 6 : e == 6 ? 5 : e > 6 ? 3 : 0; } };
struct S5 { static constexpr int PS = 10, LOOK = 6, R0 = 15, TOT = 30, A0 = 10, APS = 2, ALOOK = 0, NA = 24;
  static constexpr int nA(int e) { return e < 7 ? 2 : 0; } static constexpr int nW(int e) { return Ring<PS, R0, 3, TOT>::n(e); } static constexpr int nX(int) { return 0; } };
template <class S> constexpr int sch_X(int e) { int t = 0; for (int i = 0; i < e; ++i) t += S::nX(i); return t; }

#define STAMP(i)

DI void poll(const GAS unsigned* addr, unsigned base, unsigned want, bool active) {
  for (unsigned s = 0; s < SPIN_CAP; ++s) {
    unsigned v = __hip_atomic_load(addr, RLX_AGENT);
    if (all64(!active || (v - base) >= want)) break;
  }
  __builtin_amdgcn_fence(__ATOMIC_ACQUIRE, "wavefront");
}
DI unsigned lds_arrive(LAS char* L, int off, int lane) {
  unsigned o = 0;
  asm volatile("s_waitcnt lgkmcnt(0)" ::: "memory");
  if (lane == 0) o = __hip_atomic_fetch_add((LAS unsigned*)(L + off), 1u, __ATOMIC_RELAXED, __HIP_MEMORY_SCOPE_WORKGROUP);
  o = __builtin_amdgcn_readfirstlane(o);
  asm volatile("" ::: "memory");
  return o;
}
template <int N> DI void mod_rows(const v4u* wm, const unsigned* cv, LAS char* red, int lane) {
  float acc[8] = {0, 0, 0, 0, 0, 0, 0, 0};
#pragma unroll
  for (int i = 0; i < N; ++i) {
    float cf = bflo(cv[i]);
    float sv = bfr(cf * __builtin_amdgcn_rcpf(1.f + __builtin_amdgcn_exp2f(-1.44269504f * cf)));
#pragma unroll
    for (int e = 0; e < 4; ++e) { acc[2 * e] += sv * bflo(wm[i][e]); acc[2 * e + 1] += sv * bfhi(wm[i][e]); }
  }
  float u[4], v[2];
#pragma unroll
  for (int e = 0; e < 4; ++e) { f32x2 r = xswp32(acc[e], acc[e + 4]); u[e] = r.x + r.y; }
#pragma unroll
  for (int e = 0; e < 2; ++e) { f32x2 r = xswp16(u[e], u[e + 2]); v[e] = ror8_add(r.x + r.y); }
  if (!(lane & 8)) *(LAS f32x2*)(red + (lane & 7) * 32 + (lane >> 4) * 8) = f32x2{v[0], v[1]};
}
DI void lds_wait(LAS char* L, int off, unsigned want) {
  for (unsigned s = 0; s < SPIN_CAP && *(volatile LAS unsigned*)(L + off) < want; ++s) __builtin_amdgcn_s_sleep(1);
  asm volatile("" ::: "memory");
}

__global__ __launch_bounds__(512, 2) void fused_block(Params P) {
  __shared__ __attribute__((aligned(16))) char lds_raw[163840];
  LAS char* L = (LAS char*)lds_raw;
  const unsigned LB = (unsigned)(size_t)L;
  const int tid = threadIdx.x, lane = tid & 63;
  const int w = __builtin_amdgcn_readfirstlane(tid >> 6);
  const int b = blockIdx.x;
  const int p = 4 * ((b & 7) >> 1) + ((b >> 3) & 3), j = 8 * (b & 1) + (b >> 5);
  const int m = w & 3, kh = w >> 2, h = lane >> 5, r32 = lane & 31;
  const int g4 = lane >> 4, i16 = lane & 15, q4 = (lane & 15) >> 2, p4 = lane & 3, blk = (lane >> 4) & 1;
  const rsrc_t rws = __builtin_amdgcn_make_buffer_rsrc((void*)P.ws, 0, WS_END, 0x00020000);
  const rsrc_t rx = __builtin_amdgcn_make_buffer_rsrc((void*)P.x, 0, 2048 * 1280, 0x00020000);
  const unsigned l16 = lane * 16;
  const unsigned ringL = kh * 61440 + l16, ringD = LB + kh * 61440 + m * 1024;
  STAMP(0);
  unsigned long long pc0; asm volatile("s_getpc_b64 %0" : "=s"(pc0));
  unsigned cbase = 0;
  const GAS unsigned* caddr = (const GAS unsigned*)P.ws;
  const int cc = b < 80 ? b % 20 : 20 + (b - 80) % 40, kq = b < 80 ? b / 20 : ((b - 80) / 40) & 3;
  const char* wm0 = (const char*)P.wmod + (size_t)(160 * kq) * 7680 + cc * 128;
  if (w <= 4) {
    const int bj = b < 240 ? b : 0, s = bj / 6, q = bj % 6;
    const int lr = (lane * 205) >> 11, lc = lane - 10 * lr;
    const bool lo = w < 4;
    v4u t[10]; unsigned cv[5];
    {
      const char* px = (const char*)P.x + (size_t)b * 10240;
      const rsrc_t r1 = __builtin_amdgcn_make_buffer_rsrc((void*)(lo ? wm0 : px), 0, lo && b >= 80 ? 0 : 0x200000, 0x00020000);
      const rsrc_t r2 = __builtin_amdgcn_make_buffer_rsrc((void*)(lo ? (const char*)P.wqkv + (size_t)(16 * s) * 3840 + q * 640 + w * 160 : px), 0, 0x10000, 0x00020000);
      const rsrc_t r3 = __builtin_amdgcn_make_buffer_rsrc((void*)px, 0, lo ? 0 : 10240, 0x00020000);
      const rsrc_t rc = __builtin_amdgcn_make_buffer_rsrc((void*)(P.c + 160 * kq), 0, lo && b < 80 ? 320 : 0, 0x00020000);
      const unsigned vx = (lane & 7) * 1280 + (lane >> 3) * 16;
      const unsigned v1 = lo ? (tid >> 3) * 7680 + (tid & 7) * 16 : vx, st1 = lo ? 245760 : 128;
      const unsigned v2 = lo ? (lr < 6 ? lr : 5) * 3840 + lc * 16 : vx + 640, st2 = lo ? 23040 : 128;
      sfor<5>([&](auto I) { ld16p(t[I], v1, r1, I * st1); });
      ld16p(t[5], v2, r2, 0); ld16p(t[6], v2, r2, st2); ld16p(t[7], lo && lr >= 4 ? v2 - 7680 : v2, r2, 2 * st2);
      ld16p(t[8], vx, r3, 1024); ld16p(t[9], vx, r3, 1152);
      sfor<5>([&](auto I) { ld2p(cv[I], (tid >> 3) * 2, rc, I * 64); });
    }
    if (w == 0) {
      int off = CT_E;
      if (lane == 1) off = CT_C1 + (((p >= 1 ? p - 1 : 0) * 16 + j) << 7);
      if (lane == 2) off = CT_C1 + (((p >= 2 ? p - 2 : 0) * 16 + j) << 7);
      if (lane == 3) off = CT_C2 + (p << 7);
      if (lane == 4) off = CT_C3 + (p << 7);
      if (lane == 5) off = CT_C4 + (p << 7);
      if (lane == 6) off = CT_CW;
      if (lane == 7) off = CT_CM;
      if (lane >= 8 && lane < 16) off = CT_CB + ((lane - 8) << 7);
      caddr = (const GAS unsigned*)(P.ws + off);
    }
    asm volatile("global_load_dword %0, %1, off sc1" : "=v"(cbase) : "v"(caddr) : "memory");
    if (tid < 2) *(LAS v4u*)(L + L_CNT + tid * 16) = v4u{0u, 0u, 0u, 0u};
    bar();
    waitv<0>();
    sfor<10>([&](auto I) { tie(t[I]); });
    sfor<5>([&](auto I) { tie(cv[I]); });
    tie(cbase);
    if (lo) {
      const unsigned wql = L_WQ + w * 2560;
      if (lr < 6) { *(LAS v4u*)(L + wql + l16) = t[5]; *(LAS v4u*)(L + wql + 960 + l16) = t[6]; }
      if (lr < 4) *(LAS v4u*)(L + wql + 1920 + l16) = t[7];
      if (b < 80) {
        mod_rows<5>(t, cv, L + L_RED + w * 256, lane);
        if (lds_arrive(L, L_CNT, lane) == 3 && lane < 16) {
          f32x4 u = (ldsf4(L, L_RED + l16) + ldsf4(L, L_RED + 256 + l16)) + (ldsf4(L, L_RED + 512 + l16) + ldsf4(L, L_RED + 768 + l16));
          st16(__builtin_bit_cast(v4u, u), rws, WS_MODP + (kq * 3840 + cc * 64 + lane * 4) * 4);
        }
      }
      if (b < 240) {
        asm volatile("s_waitcnt lgkmcnt(0)" ::: "memory");
        const int part = q >> 1, jb = 8 * (q & 1) + 2 * w;
        const int tmain = part == 0 ? 3 : part - 1;
        const unsigned dst0 = WS_WT1 + jb * 163840 + (s >= 20 ? 81920 + (s - 20) * 4096 : s * 4096);
        const unsigned rowa = wql + (8 * h + q4) * 160;
        const unsigned a = rowa + (20 * blk + 4 * p4) * 2, a2 = rowa + (40 * blk + 16 + 20 * (p4 & 1)) * 2;
        v4u f0 = trpair(L, a, a + 640), f1 = trpair(L, a + 80, a + 720), f2 = trpair(L, a2, a2 + 640);
        st16(f0, rws, dst0 + tmain * 1024 + l16);
        st16(f1, rws, dst0 + 163840 + tmain * 1024 + l16);
        if (i16 >= 8) f2 = v4u{0, 0, 0, 0};
        const int r = (part == 0 ? 16 : part == 1 ? 0 : 8) + i16;
        if (part == 0 || i16 < 8) st16(f2, rws, dst0 + blk * 163840 + 2048 + (h * 32 + r) * 16);
      }
    } else {
      float s1 = 0.f, s2 = 0.f, s3 = 0.f, s4 = 0.f;
#pragma unroll
      for (int i = 0; i < 10; ++i) {
        s1 = dot2(t[i][0], 0x3f803f80u, s1); s2 = dot2(t[i][0], t[i][0], s2); s3 = dot2(t[i][1], 0x3f803f80u, s3); s4 = dot2(t[i][1], t[i][1], s4);
        s1 = dot2(t[i][2], 0x3f803f80u, s1); s2 = dot2(t[i][2], t[i][2], s2); s3 = dot2(t[i][3], 0x3f803f80u, s3); s4 = dot2(t[i][3], t[i][3], s4);
      }
      s1 = swap32_add(swap16_add(ror8_add(s1 + s3))); s2 = swap32_add(swap16_add(ror8_add(s2 + s4)));
      const float mean = s1 * (1.f / 640.f);
      const float rstd = __builtin_amdgcn_rsqf(fmaxf(s2 * (1.f / 640.f) - mean * mean, 0.f) + 1e-5f), nm = -mean * rstd;
      const int tok = 8 * b + (lane & 7);
      const unsigned xo = WS_XH + (tok >> 5) * 40960 + (lane >> 3) * 512 + (tok & 31) * 16;
#pragma unroll
      for (int i = 0; i < 10; ++i) {
        v4u o;
#pragma unroll
        for (int e = 0; e < 4; ++e) o[e] = pk2(fmaf(bflo(t[i][e]), rstd, nm), fmaf(bfhi(t[i][e]), rstd, nm));
        st16(o, rws, xo + i * 4096);
      }
    }
    waitv<0>();
    if (lds_arrive(L, L_CNT + 4, lane) == 4 && lane < 8) __hip_atomic_store((GAS unsigned*)(P.ws + CT_FLAG + lane * 1024 + b * 4), __builtin_amdgcn_readfirstlane(cbase) + 1u, RLX_AGENT);
    STAMP(1);
  } else {
    const int v = w - 5;
    bar();
    unsigned d = 0; v4u d4 = {};
    if (w == 7) {
      const GAS char* t = lane < 16 ? (const GAS char*)P.ws + lane * 2000000 : lane < 18 ? (const GAS char*)P.out + (lane - 16) * 2000000 : (const GAS char*)P.ws;
      asm volatile("global_load_dword %0, %1, off" : "=v"(d) : "v"(t) : "memory");
      const GAS char* cp = (const GAS char*)(pc0 & ~255ull) + (b >> 3) * 1024 + l16;
      asm volatile("global_load_dwordx4 %0, %1, off" : "=v"(d4) : "v"(cp) : "memory");
    }
    if (b >= 224) {
      const rsrc_t rb = __builtin_amdgcn_make_buffer_rsrc((void*)((const char*)P.wo + (size_t)(160 * ((b - 224) >> 3)) * 1280 + (b & 7) * 160), 0, 0x00100000, 0x00020000);
      int row = (v * 64 + lane) / 10, cl = (v * 64 + lane) % 10;
      for (int i = v; i < 25; i += 3) {
        dma<false>(LB + L_BSRC + i * 1024, row * 1280 + cl * 16, rb, 0);
        row += 19; cl += 2; if (cl >= 10) { cl -= 10; ++row; }
      }
    }
    waitv<0>();
    tie(d); tie(d4);
    if (b >= 224) {
      lds_arrive(L, L_CNT + 8, lane);
      lds_wait(L, L_CNT + 8, 3u);
      const int kr = (b - 224) >> 3, cw = b & 7;
      for (int u = v; u < 25; u += 3) {
        const int sl = u / 5, t = u % 5, c = 4 * sl + g4, jl = c / 5, ii = c % 5;
        const int r0 = ii < 4 ? 40 * jl + 20 * (ii >> 1) + 4 * (ii & 1) : 40 * jl + 16;
        const int r1 = ii < 4 ? r0 + 8 : r0 + 20;
        const unsigned ca = L_BSRC + q4 * 160 + (16 * t + 4 * p4) * 2;
        v4u f = trpair(L, ca + r0 * 160, ca + r1 * 160);
        const int s = 5 * kr + sl;
        st16(f, rws, WS_WT3 + cw * 102400 + (s & 1) * 51200 + ((s >> 1) * 5 + t) * 1024 + l16);
      }
      waitv<0>();
    }
  }
  const int c8 = 4 * (b & 1) + ((b >> 5) & 3), rh = b >> 7;
  int col = tid < 320 ? 16 * tid : 0;
  unsigned mdst = L_MODV + (tid < 160 ? tid * 32 + 16 : (tid - 160) * 32);
  const rsrc_t rb = __builtin_amdgcn_make_buffer_rsrc((void*)(b < 128 ? (const char*)((b >> 6) ? P.wu : P.wg) + (size_t)(80 * ((b >> 3) & 7)) * 3072 + (b & 7) * 384
                                                                       : (const char*)P.wd + (size_t)(32 * (((b - 128) >> 1) % 48)) * 1280 + (b & 1) * 640), 0, 0x00400000, 0x00020000);
  unsigned bo[4];
#pragma unroll
  for (int i = 0; i < 4; ++i) {
    int c = (i * 8 + w) * 64 + lane; if (c >= (b < 128 ? 1920 : b < 224 ? 1280 : 0)) c = 0;
    bo[i] = b < 128 ? (c / 24) * 3072 + (c % 24) * 16 : (c / 40) * 1280 + (c % 40) * 16; tie(bo[i]);
  }
  tie(col); tie(mdst);
  if (w == 4) {
    const unsigned tag = __builtin_amdgcn_readfirstlane(cbase) + 1u;
    for (unsigned s = 0; s < SPIN_CAP; ++s) {
      v4u f; ld16(f, l16, rws, CT_FLAG + (b & 7) * 1024); waitv<0>(); tie(f);
      if (all64(f.x == tag && f.y == tag && f.z == tag && f.w == tag)) break;
      __builtin_amdgcn_s_sleep(1);
    }
    __builtin_amdgcn_fence(__ATOMIC_ACQUIRE, "wavefront");
  }
  bar();
  STAMP(4);

  auto mod_put = [&](v4u* mp, bool isc, bool on, unsigned dst) {
    sfor<4>([&](auto I) { tie(mp[I]); });
    f32x4 v = (__builtin_bit_cast(f32x4, mp[0]) + __builtin_bit_cast(f32x4, mp[1])) + (__builtin_bit_cast(f32x4, mp[2]) + __builtin_bit_cast(f32x4, mp[3]));
#pragma unroll
    for (int e = 0; e < 4; ++e) { float t = bfr(v[e]); v[e] = isc ? bfr(1.f + t) : t; }
    if (on) *(LAS f32x4*)(L + dst) = v;
  };
  const bool mod2 = b >= 80 && b < 240;
  v4u m2w[3]; unsigned m2c[3];
  {
    v4u mp[4], xf[20];
    sfor<4>([&](auto I) { ld16(mp[I], col, rws, WS_MODP + I * 15360); });
    const unsigned w1 = WS_WT1 + j * 163840 + kh * 81920 + m * 1024;
    const unsigned ab = WS_XH + ((4 * p + m) * 40 + 20 * kh) * 1024;
    sfor<3>([&](auto R) { dma<true>(ringD + R * 4096, l16, rws, w1 + R * 4096); });
    sfor<3>([&](auto I) { ld16(xf[I], l16, rws, ab + I * 1024); });
    if (tid == 320 && b >= 224) __hip_atomic_fetch_add((GAS unsigned*)(P.ws + CT_CW), 1u, RLX_AGENT);
    waitv<6>();
    mod_put(mp, tid >= 160, tid < 320, mdst);
    sfor<3>([&](auto R) { dma<true>(ringD + (3 + R) * 4096, l16, rws, w1 + (3 + R) * 4096); });
    sfor<3>([&](auto I) { ld16(xf[3 + I], l16, rws, ab + (3 + I) * 1024); });
    bar();
    const rsrc_t rm = __builtin_amdgcn_make_buffer_rsrc((void*)wm0, 0, mod2 ? 160 * 7680 : 0, 0x00020000);
    const rsrc_t rc = __builtin_amdgcn_make_buffer_rsrc((void*)(P.c + 160 * kq), 0, mod2 ? 320 : 0, 0x00020000);
    STAMP(5);
    f32x16 acc[4] = {};
    const unsigned wbK = ringL + kh * 1024, wbS = ringL + 1024 - kh * 1024;
    unsigned cb = L_MODV + (80 * kh + 2 * h) * 32; tie(cb);
    constexpr int D = 4;
    v4u wf[80], hf[20]; f32x4 ca[40], cs[40];
    auto rdw = [&](auto N) { wf[N] = lds16(L, ((N % 4) < 2 ? wbK : wbS) + (((N / 4) * 4) % 60 + (N & 1) * 2) * 1024); };
    auto rdc = [&](auto R) { ca[R] = ldsf4(L, cb + (R / 2) * 128 + (R & 1) * 32); cs[R] = ldsf4(L, cb + (R / 2) * 128 + (R & 1) * 32 + 16); };
    auto aff = [&](auto I, auto E) {
      constexpr int e = E, r = 2 * I + (e >> 1), o = 2 * (e & 1);
      unsigned v = pk2(fmaf(bflo(xf[I][e]), ca[r][o], cs[r][o]), fmaf(bfhi(xf[I][e]), ca[r][o + 1], cs[r][o + 1]));
      tie(v); hf[I][e] = v;
    };
    sfor<4>(rdc);
    sfor<10>([&](auto E) {
      constexpr int e = E;
      waitv<(e == 0 ? 6 : sch_wait<S1>(e))>();
      if constexpr (e == 0) tie(xf[0]);
      tie(xf[2 * e + 1]); if constexpr (e < 9) tie(xf[2 * e + 2]);
      rbar();
      if constexpr (e == 0) { sfor<D>(rdw); sfor<4>([&](auto E2) { aff(IC<0>{}, E2); }); }
      sfor<8>([&](auto K) {
        constexpr int n = 8 * e + K, i = n / 4, t = n % 4;
        if constexpr (n + D < 80) rdw(IC<n + D>{});
        if constexpr ((t & 1) && i + 2 < 20) rdc(IC<2 * (i + 2) + (t >> 1)>{});
        acc[t] = mfma32(wf[n], hf[i], acc[t]); tie(acc[t]);
        SB();
        if constexpr (i + 1 < 20) aff(IC<i + 1>{}, IC<t>{});
        {
          constexpr int nA = S1::nA(e), nW = S1::nW(e), v = nA + nW + S1::nX(e) > 4 ? K : (K & 1) ? K / 2 : 99;
          if constexpr (v < nA) { constexpr int a = sch_A<S1>(e) + v; ld16(xf[a], l16, rws, ab + a * 1024); }
          else if constexpr (v < nA + nW) { constexpr int r = sch_W<S1>(e) + v - nA; dma<true>(ringD + (r % 15) * 4096, l16, rws, w1 + r * 4096); }
          else if constexpr (e == 4 && v < 3) ld16p(m2w[v], (tid >> 3) * 7680 + (tid & 7) * 16, rm, v * 491520);
          else if constexpr (e == 4 && v < 6) ld2p(m2c[v - 3], (tid >> 3) * 2, rc, (v - 3) * 128);
        }
        SB();
      });
    });
    rbar();
    STAMP(6);
    f32x16 ka = acc[0], kb = acc[1]; const f32x16 sa = acc[2], sb = acc[3];
#pragma unroll
    for (int qd = 0; qd < 4; ++qd) *(LAS f32x4*)(L + L_X1 + w * 4096 + qd * 1024 + l16) = f32x4{sa[4 * qd], sa[4 * qd + 1], sa[4 * qd + 2], sa[4 * qd + 3]};
    bar();
#pragma unroll
    for (int qd = 0; qd < 4; ++qd) {
      f32x4 u = ldsf4(L, L_X1 + (w ^ 4) * 4096 + qd * 1024 + l16);
#pragma unroll
      for (int e = 0; e < 4; ++e) ka[4 * qd + e] += u[e];
    }
#pragma unroll
    for (int qd = 0; qd < 4; ++qd) *(LAS f32x4*)(L + L_X1 + (w ^ 4) * 4096 + qd * 1024 + l16) = f32x4{sb[4 * qd], sb[4 * qd + 1], sb[4 * qd + 2], sb[4 * qd + 3]};
    const unsigned kvg = WS_KV + ((4 * p + m) * 16 + j) * 5120, kvl = (8 + m) * KVT;
    {
      v4u c0 = {pk2(ka[0], ka[1]), pk2(ka[2], ka[3]), pk2(ka[4], ka[5]), pk2(ka[6], ka[7])};
      v4u c1 = {pk2(ka[8], ka[9]), pk2(ka[10], ka[11]), pk2(ka[12], ka[13]), pk2(ka[14], ka[15])};
      const unsigned go = kh ? 3072 + r32 * 32 + h * 16 : l16, lo = kh ? 3072 + r32 * 48 + h * 16 : l16, ls = kh ? 1536 : 1024;
      st16(c0, rws, kvg + go); st16(c1, rws, kvg + go + 1024);
      *(LAS v4u*)(L + kvl + lo) = c0; *(LAS v4u*)(L + kvl + lo + ls) = c1;
    }
    bar();
#pragma unroll
    for (int qd = 0; qd < 4; ++qd) {
      f32x4 u = ldsf4(L, L_X1 + w * 4096 + qd * 1024 + l16);
#pragma unroll
      for (int e = 0; e < 4; ++e) kb[4 * qd + e] += u[e];
    }
    const float qs = 0.22360679775f * 1.44269504089f;
    if (kh == 0) {
      v4u c2 = {pk2(kb[0], kb[1]), pk2(kb[2], kb[3]), pk2(kb[4], kb[5]), pk2(kb[6], kb[7])};
      st16(c2, rws, kvg + 2048 + l16);
      *(LAS v4u*)(L + kvl + 2048 + l16) = v4u{c2.x, c2.y, 0u, 0u};
      *(LAS v4u*)(L + kvl + 3072 + h * 1536 + r32 * 48 + 32) = v4u{c2.z, c2.w, 0x3f80u, 0u};
      *(LAS v2u*)(L + L_QHI + m * 512 + lane * 8) = v2u{pk2(kb[8] * qs, kb[9] * qs), pk2(kb[10] * qs, kb[11] * qs)};
    } else {
      *(LAS v4u*)(L + L_Q + m * 2048 + l16) = v4u{pk2(kb[0] * qs, kb[1] * qs), pk2(kb[2] * qs, kb[3] * qs), pk2(kb[4] * qs, kb[5] * qs), pk2(kb[6] * qs, kb[7] * qs)};
      *(LAS v4u*)(L + L_Q + m * 2048 + 1024 + l16) = v4u{pk2(kb[8] * qs, kb[9] * qs), pk2(kb[10] * qs, kb[11] * qs), pk2(kb[12] * qs, kb[13] * qs), pk2(kb[14] * qs, kb[15] * qs)};
    }
    waitv<0>();
    bar();
    if (tid == 0) __hip_atomic_fetch_add((GAS unsigned*)(P.ws + CT_C1 + ((p * 16 + j) << 7)), 1u, RLX_AGENT);
    STAMP(7);
  }

  if (mod2) {
    sfor<3>([&](auto I) { tie(m2w[I]); tie(m2c[I]); });
    mod_rows<3>(m2w, m2c, L + L_BSRC + w * 256, lane);
  }
  STAMP(8);
  if (w == 0) poll(caddr, cbase, lane == 6 ? 32u : 1u, (lane == 1 && p >= 1) || (lane == 2 && p >= 2) || lane == 6);
  bar();
  STAMP(9);
  const unsigned w3 = WS_WT3 + c8 * 102400 + kh * 51200 + m * 1024;
  unsigned cbv;
  {
    v4u kv[5];
    const int gt = 4 * p - 8 + w;
    const unsigned ko = gt >= 0 ? WS_KV + (gt * 16 + j) * 5120 : 0x7ff00000u;
    sfor<5>([&](auto I) { ld16(kv[I], l16, rws, ko + I * 1024); });
    if (mod2 && w == 1 && lane < 16) {
      f32x4 u = ldsf4(L, L_BSRC + l16);
#pragma unroll
      for (int i = 1; i < 8; ++i) u += ldsf4(L, L_BSRC + i * 256 + l16);
      st16(__builtin_bit_cast(v4u, u), rws, WS_MODP + (kq * 3840 + cc * 64 + lane * 4) * 4);
    }
    waitv<0>();
    sfor<5>([&](auto I) { tie(kv[I]); });
    {
      const unsigned tb = w * KVT, vo = 3072 + (lane >> 1) * 48 + (lane & 1) * 16;
      *(LAS v4u*)(L + tb + l16) = kv[0]; *(LAS v4u*)(L + tb + 1024 + l16) = kv[1];
      *(LAS v4u*)(L + tb + 2048 + l16) = v4u{kv[2].x, kv[2].y, gt >= 0 ? 0u : 0xfe96u, 0u};
      *(LAS v4u*)(L + tb + 3072 + h * 1536 + r32 * 48 + 32) = v4u{kv[2].z, kv[2].w, 0x3f80u, 0u};
      *(LAS v4u*)(L + tb + vo) = kv[3]; *(LAS v4u*)(L + tb + vo + 1536) = kv[4];
    }
    bar();
    if (tid == 0 && mod2) __hip_atomic_fetch_add((GAS unsigned*)(P.ws + CT_CM), 1u, RLX_AGENT);
    STAMP(10);
    const int hd = kh;
    const v4u q0 = lds16(L, L_Q + m * 2048 + hd * 1024 + l16);
    v2u qh = *(LAS v2u*)(L + L_QHI + m * 512 + lane * 8);
    if (h != hd) qh = v2u{0, 0};
    const v4u q1 = {qh.x, qh.y, 0x3f80u, 0};
    const f32x16 zero = {};
    const unsigned ka = m * KVT + hd * 1024 + l16, kb = m * KVT + 2048 + l16;
    auto qk = [&](auto I, f32x16 c) { return mfma32(lds16(L, kb + I * KVT), q1, mfma32(lds16(L, ka + I * KVT), q0, c)); };
    auto vmax = [](float a, float c) { return __builtin_elementwise_maximum(a, c); };
    auto tmax = [&](const f32x16& v) { float t = vmax(v[0], v[1]);
#pragma unroll
      for (int r = 2; r < 16; r += 2) t = vmax(vmax(v[r], v[r + 1]), t);
      return t; };
    f32x16 Sm;
    {
      const f32x16 s0 = qk(IC<0>{}, zero), s8 = qk(IC<8>{}, zero);
#pragma unroll
      for (int r = 0; r < 16; ++r) Sm[r] = (r & 3) + 8 * (r >> 2) + 4 * h <= r32 ? s8[r] : s0[r];
    }
    float mx = tmax(Sm);
    sfor<7>([&](auto I) {
      mx = vmax(mx, tmax(qk(IC<I + 1>{}, zero)));
      if constexpr (I < 4) dma<false>(LB + L_BSRC + (I * 8 + w) * 1024, bo[I], rb, 0);
      else dma<true>(LB + L_W3A + kh * 16384 + m * 1024 + (I - 4) * 4096, l16, rws, w3 + (I - 4) * 4096);
    });
    dma<true>(LB + L_W3A + kh * 16384 + m * 1024 + 3 * 4096, l16, rws, w3 + 3 * 4096);
    { f32x2 t = swp32(mx); mx = vmax(t.x, t.y); }
    f32x16 negm;
#pragma unroll
    for (int r = 0; r < 16; ++r) negm[r] = -mx;
    tie(negm);
    f32x16 O = {};
    const unsigned va = m * KVT + 3072 + hd * 1536 + (4 * h + q4) * 48 + (blk == 0 ? (p4 & 1) * 16 + (p4 >> 1) * 8 : (p4 ? 40 : 32));
    auto pv = [&](auto I, int s, v4u pf) { O = mfma32(trpair(L, va + I * KVT + s * 768, va + I * KVT + s * 768 + 384), pf, O); };
#pragma unroll
    for (int s = 0; s < 2; ++s) {
      v4u p0, p8;
#pragma unroll
      for (int e = 0; e < 4; ++e) {
        const int r = 8 * s + 2 * e, key = (r & 3) + 8 * (r >> 2) + 4 * h;
        const float a = __builtin_amdgcn_exp2f(Sm[r] - mx), c = __builtin_amdgcn_exp2f(Sm[r + 1] - mx);
        const float a8 = key <= r32 ? a : 0.f, c8_ = key + 1 <= r32 ? c : 0.f;
        p8[e] = pk2(a8, c8_); p0[e] = pk2(a - a8, c - c8_);
      }
      pv(IC<0>{}, s, p0); pv(IC<8>{}, s, p8);
    }
    sfor<7>([&](auto I) {
      const f32x16 S = qk(IC<I + 1>{}, negm);
      if constexpr (I == 4) asm volatile("global_load_dword %0, %1, off sc1" : "=v"(cbv) : "v"(caddr) : "memory");
#pragma unroll
      for (int s = 0; s < 2; ++s) {
        v4u pf;
#pragma unroll
        for (int e = 0; e < 4; ++e) pf[e] = pk2(__builtin_amdgcn_exp2f(S[8 * s + 2 * e]), __builtin_amdgcn_exp2f(S[8 * s + 2 * e + 1]));
        pv(IC<I + 1>{}, s, pf);
      }
    });
    const float inv = __builtin_amdgcn_rcpf(swap32_hi(O[8]));
    const int tok = 128 * p + 32 * m + r32;
    const unsigned ob = WS_OF + (tok >> 4) * 20480 + (tok & 15) * 16;
    const int c = 5 * j + 2 * hd + h, c2 = 5 * j + 4;
    STAMP(11);
    st16(v4u{pk2(O[0] * inv, O[1] * inv), pk2(O[2] * inv, O[3] * inv), pk2(O[4] * inv, O[5] * inv), pk2(O[6] * inv, O[7] * inv)}, rws, ob + (c >> 2) * 1024 + (c & 3) * 256);
    if (h == 0) st8(v2u{pk2(O[8] * inv, O[9] * inv), pk2(O[10] * inv, O[11] * inv)}, rws, ob + (c2 >> 2) * 1024 + (c2 & 3) * 256 + hd * 8);
  }
  waitv<0>();
  tie(cbv);
  if (w == 0 && !all64(lane != 7 || cbv - cbase >= 160u)) poll(caddr, cbase, 160u, lane == 7);
  bar();
  const int tt = 8 * p + 4 * rh + m;
  unsigned x2k[6];
  {
    v2u xr[3];
    const unsigned xo = (16 * tt + i16) * 1280 + (80 * c8 + 4 * g4) * 2;
    if (tid == 0) __hip_atomic_fetch_add((GAS unsigned*)(P.ws + CT_C2 + (p << 7)), 1u, RLX_AGENT);
    STAMP(12);
    {
      const int l20 = lane % 20, lq = lane / 20;
      const unsigned col2 = ((w == 6 ? 1920 + 80 * ((b >> 3) & 7) : lq == 0 ? 1280 + 80 * c8 : lq == 1 ? 3200 + 80 * c8 : 2560 + 80 * ((b >> 3) & 7)) + 4 * l20) * 4;
      v4u mp[4];
      sfor<4>([&](auto I) { ld16(mp[I], col2, rws, WS_MODP + I * 15360); });
      ld8p(xr[0], xo, rx, kh * 64); ld8p(xr[1], xo, rx, kh * 64 + 32); ld8p(xr[2], xo, rx, 128);
      sfor<9>([&](auto R) { dma<true>(LB + kh * 36864 + m * 1024 + R * 4096, l16, rws, w3 + (4 + R) * 4096); });
      waitv<12>();
      mod_put(mp, w == 5 && lq == 2, (w == 5 && lane < 60) || (w == 6 && lane < 20), L_MODV + 5120 + (w - 5) * 960 + l16);
      bar();
    }
  if (b < 128) {
    if (w < 6) {
      const int mat = b >> 6, kp = (b >> 3) & 7, cg = b & 7;
      const int tl = 2 * w + blk;
      const int jd = 2 * cg + tl / 6, t = tl % 6;
      float us = 0.f, vs = 0.f;
#pragma unroll
      for (int sg = 0; sg < 5; ++sg) {
        const int k0 = sg < 4 ? 32 * (sg >> 1) + 4 * (2 * (sg & 1) + h) : 64 + 8 * h;
        const int k1 = sg < 4 ? k0 + 16 : k0 + 4;
        const unsigned ca = L_BSRC + q4 * 384 + (16 * tl + 4 * p4) * 2;
        v4u f = trpair(L, ca + k0 * 384, ca + k1 * 384);
        f32x4 a0 = ldsf4(L, L_MODV + 5760 + k0 * 4), a1 = ldsf4(L, L_MODV + 5760 + k1 * 4);
        f32x4 b0 = ldsf4(L, L_MODV + 6080 + k0 * 4), b1 = ldsf4(L, L_MODV + 6080 + k1 * 4);
        v4u o;
        o[0] = pk2(bflo(f[0]) * a0[0], bfhi(f[0]) * a0[1]); o[1] = pk2(bflo(f[1]) * a0[2], bfhi(f[1]) * a0[3]);
        o[2] = pk2(bflo(f[2]) * a1[0], bfhi(f[2]) * a1[1]); o[3] = pk2(bflo(f[3]) * a1[2], bfhi(f[3]) * a1[3]);
        vs += bflo(f[0]) * b0[0] + bfhi(f[0]) * b0[1] + bflo(f[1]) * b0[2] + bfhi(f[1]) * b0[3] + bflo(f[2]) * b1[0] + bfhi(f[2]) * b1[1] + bflo(f[3]) * b1[2] + bfhi(f[3]) * b1[3];
        us = dot2(o[0], 0x3f803f80u, us); us = dot2(o[1], 0x3f803f80u, us); us = dot2(o[2], 0x3f803f80u, us); us = dot2(o[3], 0x3f803f80u, us);
        const int s = 5 * kp + sg;
        st16(o, rws, WS_WT4 + jd * 245760 + (s / 20) * 122880 + ((s % 20) * 6 + t) * 1024 + (h * 32 + mat * 16 + i16) * 16);
      }
      us = swap32_add(us); vs = swap32_add(vs);
      const int row = (jd * 6 + t) * 32 + mat * 16 + i16;
      __builtin_amdgcn_raw_buffer_store_b32(__builtin_bit_cast(unsigned, h ? vs : us), rws, WS_UV + ((kp * 2 + h) * 3072 + row) * 4, 0, 16);
    }
  }
  if (b >= 128 && b < 224) {
    const int st = (b - 128) >> 1, ch = b & 1;
    for (int tt = w; tt < 20; tt += 8) {
      const unsigned a = L_BSRC + (16 * (g4 >> 1) + 4 * (g4 & 1) + q4) * 640 + (16 * tt + 4 * p4) * 2;
      v4u f = trpair(L, a, a + 8 * 640);
      st16(f, rws, WS_WT5 + (4 * ch + tt / 5) * 245760 + (st & 1) * 122880 + ((st >> 1) * 5 + tt % 5) * 1024 + l16);
    }
  }
    if (w == 0) poll(caddr, cbase, 16u, lane == 3);
    bar();
    STAMP(13);
    v4u af[10], wf[50];
    const unsigned ab = WS_OF + (tt * 20 + kh) * 1024;
    sfor<10>([&](auto I) { ld16(af[I], l16, rws, ab + I * 2048); });
    f32x4 acc[5] = {};
    constexpr int D = 6;
    unsigned wa = L_W3A + kh * 16384 + l16; tie(wa);
    const unsigned wb = kh * 36864 + l16;
    auto rdw = [&](auto N) { if constexpr (N < 16) wf[N] = lds16(L, wa + N * 1024); else wf[N] = lds16(L, wb + (N - 16) * 1024); };
    sfor<10>([&](auto E) {
      constexpr int e = E;
      waitv<9 - e>();
      tie(af[e]);
      if constexpr (e == 0) {
        rbar();
        if (tid == 64 && b < 224) __hip_atomic_fetch_add((GAS unsigned*)(P.ws + CT_CB + ((b & 7) << 7)), 1u, RLX_AGENT);
        sfor<D>(rdw);
      }
      sfor<5>([&](auto K) {
        constexpr int n = 5 * e + K;
        if constexpr (n + D < 50) rdw(IC<n + D>{});
        acc[K] = mfma16(wf[n], af[e], acc[K]); tie(acc[K]);
        SB();
      });
    });
    STAMP(14);
    asm volatile("global_load_dword %0, %1, off sc1" : "=v"(cbv) : "v"(caddr) : "memory");
    const unsigned xs = L_BSRC + m * 5120;
    if (kh == 0) { *(LAS f32x4*)(L + xs + l16) = acc[2]; *(LAS f32x4*)(L + xs + 1024 + l16) = acc[3]; }
    else { *(LAS f32x4*)(L + xs + 2048 + l16) = acc[0]; *(LAS f32x4*)(L + xs + 3072 + l16) = acc[1]; *(LAS f32x4*)(L + xs + 4096 + l16) = acc[4]; }
    bar();
    tie(xr[0]); tie(xr[1]); tie(xr[2]);
    f32x4 r0, r1, r2 = {};
    if (kh == 0) { r0 = acc[0] + ldsf4(L, xs + 2048 + l16); r1 = acc[1] + ldsf4(L, xs + 3072 + l16); r2 = acc[4] + ldsf4(L, xs + 4096 + l16); }
    else { r0 = acc[2] + ldsf4(L, xs + l16); r1 = acc[3] + ldsf4(L, xs + 1024 + l16); }
    const f32x4 ga0 = ldsf4(L, L_MODV + 5120 + (32 * kh + 4 * g4) * 4), ga1 = ldsf4(L, L_MODV + 5120 + (32 * kh + 16 + 4 * g4) * 4), ga2 = ldsf4(L, L_MODV + 5120 + (64 + 4 * g4) * 4);
    x2k[0] = pk2(bflo(xr[0].x) + ga0[0] * r0[0], bfhi(xr[0].x) + ga0[1] * r0[1]); x2k[1] = pk2(bflo(xr[0].y) + ga0[2] * r0[2], bfhi(xr[0].y) + ga0[3] * r0[3]);
    x2k[2] = pk2(bflo(xr[1].x) + ga1[0] * r1[0], bfhi(xr[1].x) + ga1[1] * r1[1]); x2k[3] = pk2(bflo(xr[1].y) + ga1[2] * r1[2], bfhi(xr[1].y) + ga1[3] * r1[3]);
    x2k[4] = pk2(bflo(xr[2].x) + ga2[0] * r2[0], bfhi(xr[2].x) + ga2[1] * r2[1]); x2k[5] = pk2(bflo(xr[2].y) + ga2[2] * r2[2], bfhi(xr[2].y) + ga2[3] * r2[3]);
    if (kh) { x2k[4] = 0; x2k[5] = 0; }
    const unsigned xb = WS_X2F + ((tt >> 1) * 40 + 5 * c8) * 1024 + ((tt & 1) * 16 + i16) * 16;
    st16(v4u{x2k[0], x2k[1], x2k[2], x2k[3]}, rws, xb + (2 * kh + (g4 >> 1)) * 1024 + (g4 & 1) * 512);
    if (kh == 0) st8(v2u{x2k[4], x2k[5]}, rws, xb + 4096 + (g4 >> 1) * 512 + (g4 & 1) * 8);
    float s1 = 0.f, s2 = 0.f;
#pragma unroll
    for (int e = 0; e < 6; ++e) { s1 = dot2(x2k[e], 0x3f803f80u, s1); s2 = dot2(x2k[e], x2k[e], s2); }
    s1 = swap32_add(swap16_add(s1)); s2 = swap32_add(swap16_add(s2));
    if (g4 == 0) st8(v2u{__builtin_bit_cast(unsigned, s1), __builtin_bit_cast(unsigned, s2)}, rws, WS_ST + ((tt >> 1) * 8 + c8) * 512 + ((tt & 1) * 16 + i16) * 16 + kh * 8);
  }
  waitv<0>();
  tie(cbv);
  if (w == 0 && !all64(lane < 8 || lane >= 16 || cbv - cbase >= 28u)) poll(caddr, cbase, 28u, lane >= 8 && lane < 16);
  bar();
  const unsigned w5 = WS_WT5 + c8 * 245760 + kh * 122880 + m * 1024;
  {
    const unsigned w4 = WS_WT4 + j * 245760 + kh * 122880 + m * 1024;
    if (tid == 0) __hip_atomic_fetch_add((GAS unsigned*)(P.ws + CT_C3 + (p << 7)), 1u, RLX_AGENT);
    STAMP(15);
    sfor<S4::R0>([&](auto R) { dma<true>(ringD + R * 4096, l16, rws, w4 + R * 4096); });
    if (w == 0) poll(caddr, cbase, 16u, lane == 4);
    bar();
    STAMP(16);
    v4u af[20], stv[4], wf[120]; unsigned uv[8];
    const unsigned ab = WS_X2F + ((4 * p + m) * 40 + 20 * kh) * 1024;
    sfor<S4::A0>([&](auto I) { ld16(af[I], l16, rws, ab + I * 1024); });
    f32x16 acc[6] = {};
    const unsigned wbK = ringL + kh * 3072, wbS = ringL + 3072 - kh * 3072;
    constexpr int D = 4;
    auto rdw = [&](auto N) { wf[N] = lds16(L, ((N % 6) < 3 ? wbK : wbS) + (((N / 6) * 6) % 60 + N % 3) * 1024); };
    const int ti = tid < 384 ? tid : 0;
    const unsigned uo = ((ti / 192) * 3072 + j * 192 + ti % 192) * 4;
    sfor<10>([&](auto E) {
      constexpr int e = E;
      waitv<sch_wait<S4>(e)>();
      tie(af[2 * e]); tie(af[2 * e + 1]);
      rbar();
      if constexpr (e == 0) sfor<D>(rdw);
      sfor<12>([&](auto K) {
        constexpr int n = 12 * e + K, nA = S4::nA(e), nW = S4::nW(e), nX = S4::nX(e), v = nA + nW + nX > 6 ? K : (K & 1) ? 99 : K / 2;
        if constexpr (n + D < 120) rdw(IC<n + D>{});
        acc[n % 6] = mfma32(wf[n], af[n / 6], acc[n % 6]); tie(acc[n % 6]);
        SB();
        if constexpr (v < nA) { constexpr int a = sch_A<S4>(e) + v; ld16(af[a], l16, rws, ab + a * 1024); }
        else if constexpr (v < nA + nW) { constexpr int r = sch_W<S4>(e) + v - nA; dma<true>(ringD + (r % 15) * 4096, l16, rws, w4 + r * 4096); }
        else if constexpr (v < nA + nW + nX) {
          constexpr int x = sch_X<S4>(e) + v - nA - nW;
          if constexpr (x < 4) ld16(stv[x], r32 * 16 + h * 2048 + x * 512, rws, WS_ST + (4 * p + m) * 4096);
          else if constexpr (x < 12) ld4(uv[x - 4], uo, rws, WS_UV + (x - 4) * 24576);
          else dma<true>(ringD + (x - 12) * 4096, l16, rws, w5 + (x - 12) * 4096);
        }
        SB();
      });
    });
    STAMP(17);
    waitv<12>();
    sfor<4>([&](auto I) { tie(stv[I]); });
    sfor<8>([&](auto I) { tie(uv[I]); });
    {
      float s = 0.f;
#pragma unroll
      for (int i = 0; i < 8; ++i) s += __builtin_bit_cast(float, uv[i]);
      if (tid < 384) *(LAS float*)(L + L_UVS + tid * 4) = s;
    }
    float s1 = 0.f, s2 = 0.f;
#pragma unroll
    for (int i = 0; i < 4; ++i) { f32x4 v = __builtin_bit_cast(f32x4, stv[i]); s1 += v[0] + v[2]; s2 += v[1] + v[3]; }
    s1 = swap32_add(s1); s2 = swap32_add(s2);
    const float mean = s1 * (1.f / 640.f);
    const float rs2 = __builtin_amdgcn_rsqf(fmaxf(s2 * (1.f / 640.f) - mean * mean, 0.f) + 1e-5f), nm2 = -mean * rs2;
    const int tok = 128 * p + 32 * m + r32;
    const unsigned ao = WS_ACT + ((tok >> 4) * 48 + 3 * j) * 1024 + h * 256 + (tok & 15) * 16;
    sfor<3>([&](auto T) {
      constexpr int t = T;
      const f32x16 sd = acc[3 + t];
      const unsigned wr = L_BSRC + ((t & 1) ? (w ^ 4) : w) * 4096 + l16, rd = L_BSRC + ((t & 1) ? w : (w ^ 4)) * 4096 + l16;
#pragma unroll
      for (int qd = 0; qd < 4; ++qd) *(LAS f32x4*)(L + wr + qd * 1024) = f32x4{sd[4 * qd], sd[4 * qd + 1], sd[4 * qd + 2], sd[4 * qd + 3]};
      bar();
      f32x16 kp = acc[t];
      const int tg = 3 * kh + t;
      float a[8];
#pragma unroll
      for (int qd = 0; qd < 4; ++qd) {
        const f32x4 o = ldsf4(L, rd + qd * 1024);
        const f32x4 uu = ldsf4(L, L_UVS + (tg * 32 + 8 * qd + 4 * h) * 4), vv = ldsf4(L, L_UVS + 768 + (tg * 32 + 8 * qd + 4 * h) * 4);
#pragma unroll
        for (int e = 0; e < 4; ++e) kp[4 * qd + e] = fmaf(kp[4 * qd + e] + o[e], rs2, fmaf(nm2, uu[e], vv[e]));
      }
#pragma unroll
      for (int e = 0; e < 8; ++e) { const float gv = kp[e]; a[e] = gv * __builtin_amdgcn_rcpf(1.f + __builtin_amdgcn_exp2f(-1.44269504f * gv)) * kp[8 + e]; }
      st16(v4u{pk2(a[0], a[1]), pk2(a[2], a[3]), pk2(a[4], a[5]), pk2(a[6], a[7])}, rws, ao + (tg >> 1) * 1024 + (tg & 1) * 512);
    });
  }
  waitv<0>();
  bar();
  {
    if (tid == 0) __hip_atomic_fetch_add((GAS unsigned*)(P.ws + CT_C4 + (p << 7)), 1u, RLX_AGENT);
    STAMP(18);
    sfor<3>([&](auto R) { dma<true>(ringD + (12 + R) * 4096, l16, rws, w5 + (12 + R) * 4096); });
    if (w == 0) poll(caddr, cbase, 16u, lane == 5);
    bar();
    STAMP(19);
    v4u af[24], wf[120];
    const unsigned ab = WS_ACT + (tt * 48 + kh) * 1024;
    sfor<S5::A0>([&](auto I) { ld16(af[I], l16, rws, ab + I * 2048); });
    f32x4 acc[5] = {};
    constexpr int D = 6;
    auto rdw = [&](auto N) { wf[N] = lds16(L, ringL + (N % 60) * 1024); };
    sfor<12>([&](auto E) {
      constexpr int e = E;
      waitv<sch_wait<S5>(e)>();
      tie(af[2 * e]); tie(af[2 * e + 1]);
      rbar();
      if constexpr (e == 0) sfor<D>(rdw);
      sfor<10>([&](auto K) {
        constexpr int n = 10 * e + K, v = K / 2, nA = S5::nA(e), nW = S5::nW(e);
        if constexpr (n + D < 120) rdw(IC<n + D>{});
        acc[n % 5] = mfma16(wf[n], af[n / 5], acc[n % 5]); tie(acc[n % 5]);
        SB();
        if constexpr (!(K & 1)) {
          if constexpr (v < nA) { constexpr int a = sch_A<S5>(e) + v; ld16(af[a], l16, rws, ab + a * 2048); }
          else if constexpr (v < nA + nW) { constexpr int r = sch_W<S5>(e) + v - nA; dma<true>(ringD + (r % 15) * 4096, l16, rws, w5 + r * 4096); }
          SB();
        }
      });
    });
    STAMP(20);
    const unsigned xs = L_BSRC + m * 5120;
    if (kh == 0) { *(LAS f32x4*)(L + xs + l16) = acc[2]; *(LAS f32x4*)(L + xs + 1024 + l16) = acc[3]; }
    else { *(LAS f32x4*)(L + xs + 2048 + l16) = acc[0]; *(LAS f32x4*)(L + xs + 3072 + l16) = acc[1]; *(LAS f32x4*)(L + xs + 4096 + l16) = acc[4]; }
    bar();
    f32x4 r0, r1, r2 = {};
    if (kh == 0) { r0 = acc[0] + ldsf4(L, xs + 2048 + l16); r1 = acc[1] + ldsf4(L, xs + 3072 + l16); r2 = acc[4] + ldsf4(L, xs + 4096 + l16); }
    else { r0 = acc[2] + ldsf4(L, xs + l16); r1 = acc[3] + ldsf4(L, xs + 1024 + l16); }
    const f32x4 g0 = ldsf4(L, L_MODV + 5440 + (32 * kh + 4 * g4) * 4), g1 = ldsf4(L, L_MODV + 5440 + (32 * kh + 16 + 4 * g4) * 4), g2 = ldsf4(L, L_MODV + 5440 + (64 + 4 * g4) * 4);
    GAS char* o = (GAS char*)P.out + (size_t)(16 * tt + i16) * 1280 + (80 * c8 + 4 * g4) * 2;
    *(GAS v2u*)(o + kh * 64) = v2u{pk2(bflo(x2k[0]) + g0[0] * r0[0], bfhi(x2k[0]) + g0[1] * r0[1]), pk2(bflo(x2k[1]) + g0[2] * r0[2], bfhi(x2k[1]) + g0[3] * r0[3])};
    *(GAS v2u*)(o + kh * 64 + 32) = v2u{pk2(bflo(x2k[2]) + g1[0] * r1[0], bfhi(x2k[2]) + g1[1] * r1[1]), pk2(bflo(x2k[3]) + g1[2] * r1[2], bfhi(x2k[3]) + g1[3] * r1[3])};
    if (kh == 0) *(GAS v2u*)(o + 128) = v2u{pk2(bflo(x2k[4]) + g2[0] * r2[0], bfhi(x2k[4]) + g2[1] * r2[1]), pk2(bflo(x2k[5]) + g2[2] * r2[2], bfhi(x2k[5]) + g2[3] * r2[3])};
    if (b == 0 && tid == 0) __hip_atomic_store((GAS unsigned*)(P.ws + CT_E), cbase + 1u, RLX_AGENT);
    STAMP(21);
  }
}

#if defined(__HIP_DEVICE_COMPILE__)
#pragma clang attribute pop
#endif
extern "C" void kernel_launch(void* const* d_in, const int* in_sizes, int n_in, void* d_out, int out_size, void* d_ws, size_t ws_size,
                              hipStream_t stream) {
  Params P;
  P.x = (const u16*)d_in[0]; P.wqkv = (const u16*)d_in[1]; P.wo = (const u16*)d_in[2];
  P.wg = (const u16*)d_in[4]; P.wu = (const u16*)d_in[5]; P.wd = (const u16*)d_in[6];
  P.c = (const u16*)d_in[7]; P.wmod = (const u16*)d_in[8];
  P.out = (u16*)d_out; P.ws = (char*)d_ws;
  fused_block<<<256, 512, 0, stream>>>(P);
}
```

```cpp
#include <hip/hip_runtime.h>
#include <hip/hip_bf16.h>
#include <utility>

typedef unsigned short u16;
typedef unsigned v4u __attribute__((ext_vector_type(4)));
typedef unsigned v2u __attribute__((ext_vector_type(2)));
typedef short v4s __attribute__((ext_vector_type(4)));
typedef float f32x2 __attribute__((ext_vector_type(2)));
typedef float f32x4 __attribute__((ext_vector_type(4)));
typedef float f32x16 __attribute__((ext_vector_type(16)));
typedef __bf16 bf16x2 __attribute__((ext_vector_type(2)));
typedef __bf16 bf16x8 __attribute__((ext_vector_type(8)));
typedef _Float16 f16x2 __attribute__((ext_vector_type(2)));
typedef __amdgpu_buffer_rsrc_t rsrc_t;
#define LAS __attribute__((address_space(3)))
#define GAS __attribute__((address_space(1)))
#define DI __device__ __forceinline__
#define RLX_AGENT __ATOMIC_RELAXED, __HIP_MEMORY_SCOPE_AGENT

#define STAMPS 0

constexpr int WS_CTRL = 0;
constexpr int WS_MODP = 65536;
constexpr int WS_UV   = 131072;
constexpr int WS_ST   = 327680;
constexpr int WS_WT1  = 655360;
constexpr int WS_WT3  = WS_WT1 + 16 * 163840;
constexpr int WS_WT4  = WS_WT3 + 8 * 106496;
constexpr int WS_WT5  = WS_WT4 + 16 * 245760;
constexpr int WS_KV   = WS_WT5 + 8 * 245760;
constexpr int WS_OF   = WS_KV + 64 * 16 * 5120;
constexpr int WS_X2F  = WS_OF + 128 * 20 * 1024;
constexpr int WS_ACT  = WS_X2F + 64 * 40 * 1024;
constexpr int WS_XH   = WS_ACT + 128 * 48 * 1024;
constexpr int WS_END  = WS_XH + 64 * 40 * 1024;
constexpr int WS_DBG  = WS_END;
constexpr int CT_E = 0, CT_FLAG = 4096  , CT_C1 = 16384  , CT_C2 = 49152, CT_C3 = CT_C2 + 2048,
              CT_C4 = CT_C3 + 2048  , CT_CB = CT_C4 + 6144  , CT_CW = CT_CB + 1024, CT_CM = CT_CW + 128;
constexpr int L_WQ = 0, L_RED = 10240;
constexpr int L_BSRC = 122880;
constexpr int L_MODV = 155648;
constexpr int L_CNT = 162816;
constexpr int L_UVS = 155648;
constexpr int KVT = 6144;
constexpr int L_Q = 73728, L_QHI = 81920, L_RED2 = 112640  , L_W3A = 86016;
constexpr unsigned SPIN_CAP = 1u << 20;

#if defined(__HIP_DEVICE_COMPILE__)
#pragma clang attribute push (__attribute__((target("no-packed-fp32-ops"))), apply_to = function)
#endif
struct Params { const u16 *x, *wqkv, *wo, *wg, *wu, *wd, *c, *wmod; u16* out; char* ws; };

DI f32x16 mfma32(v4u a, v4u b, f32x16 c) { return __builtin_amdgcn_mfma_f32_32x32x16_bf16(__builtin_bit_cast(bf16x8, a), __builtin_bit_cast(bf16x8, b), c, 0, 0, 0); }
DI f32x4 mfma16(v4u a, v4u b, f32x4 c) { return __builtin_amdgcn_mfma_f32_16x16x32_bf16(__builtin_bit_cast(bf16x8, a), __builtin_bit_cast(bf16x8, b), c, 0, 0, 0); }
DI unsigned pk2(float a, float b) { f32x2 v = {a, b}; return __builtin_bit_cast(unsigned, __builtin_convertvector(v, bf16x2)); }
DI float bflo(unsigned x) { return __builtin_bit_cast(float, x << 16); }
DI float bfhi(unsigned x) { return __builtin_bit_cast(float, x & 0xffff0000u); }
DI float bfr(float a) { return bflo(pk2(a, a)); }
DI f32x2 swp32(float v) {
  unsigned a = __builtin_bit_cast(unsigned, v), c = a; asm("" : "+v"(c));
  auto r = __builtin_amdgcn_permlane32_swap(a, c, false, false);
  unsigned r0 = r[0], r1 = r[1];
  return f32x2{__builtin_bit_cast(float, r0), __builtin_bit_cast(float, r1)};
}
DI f32x2 swp16(float v) {
  unsigned a = __builtin_bit_cast(unsigned, v), c = a; asm("" : "+v"(c));
  auto r = __builtin_amdgcn_permlane16_swap(a, c, false, false);
  unsigned r0 = r[0], r1 = r[1];
  return f32x2{__builtin_bit_cast(float, r0), __builtin_bit_cast(float, r1)};
}
DI float swap32_add(float v) { f32x2 r = swp32(v); return r.x + r.y; }
DI float swap16_add(float v) { f32x2 r = swp16(v); return r.x + r.y; }
DI float swap32_max(float v) { f32x2 r = swp32(v); return fmaxf(r.x, r.y); }
DI float swap32_hi(float v) { return swp32(v).y; }
DI bool all64(bool c) { return __builtin_amdgcn_ballot_w64(!c) == 0ull; }
DI float ror8_add(float v) { return v + __builtin_bit_cast(float, __builtin_amdgcn_update_dpp(0, __builtin_bit_cast(int, v), 0x128, 0xf, 0xf, true)); }

template <bool SC1> DI void dma(unsigned lds_addr, unsigned voff, rsrc_t rs, unsigned soff) {
  if (SC1) asm volatile("s_mov_b32 m0, %0\n\ts_nop 0\n\tbuffer_load_dwordx4 %1, %2, %3 offen sc1 lds" ::"s"(lds_addr), "v"(voff), "s"(rs), "s"(soff) : "memory");
  else     asm volatile("s_mov_b32 m0, %0\n\ts_nop 0\n\tbuffer_load_dwordx4 %1, %2, %3 offen lds" ::"s"(lds_addr), "v"(voff), "s"(rs), "s"(soff) : "memory");
}
template <int N, int LO, int GO> DI void dman(unsigned lb, unsigned voff, rsrc_t rs, unsigned gb) {
  unsigned t;
#define DMA_HEAD "s_add_u32 m0, %1, %5\n\ts_add_u32 %0, %4, %6\n\tbuffer_load_dwordx4 %2, %3, %0 offen sc1 lds"
#define DMA_MORE(o) "\n\tbuffer_load_dwordx4 %2, %3, %0 offen offset:" #o " sc1 lds"
#define DMA_ARGS : "=&s"(t) : "s"(lb), "v"(voff), "s"(rs), "s"(gb), "n"(LO), "n"(GO) : "memory", "scc"
  if constexpr (N == 1) asm volatile(DMA_HEAD DMA_ARGS);
  if constexpr (N == 2) asm volatile(DMA_HEAD DMA_MORE(1024) DMA_ARGS);
  if constexpr (N == 3) asm volatile(DMA_HEAD DMA_MORE(1024) DMA_MORE(2048) DMA_ARGS);
  if constexpr (N == 4) asm volatile(DMA_HEAD DMA_MORE(1024) DMA_MORE(2048) DMA_MORE(3072) DMA_ARGS);
}
template <int I> DI void ldf(v4u& d, unsigned voff, rsrc_t rs, unsigned base) {
  asm volatile("buffer_load_dwordx4 %0, %1, %2, %3 offen offset:%4 sc1" : "=v"(d) : "v"(voff), "s"(rs), "s"(base + (I / 4) * 4096), "n"((I % 4) * 1024) : "memory");
}
DI void ld16(v4u& d, unsigned voff, rsrc_t rs, unsigned soff) { asm volatile("buffer_load_dwordx4 %0, %1, %2, %3 offen sc1" : "=v"(d) : "v"(voff), "s"(rs), "s"(soff) : "memory"); }
DI void ld16p(v4u& d, unsigned voff, rsrc_t rs, unsigned soff) { asm volatile("buffer_load_dwordx4 %0, %1, %2, %3 offen" : "=v"(d) : "v"(voff), "s"(rs), "s"(soff) : "memory"); }
DI void ld8p(v2u& d, unsigned voff, rsrc_t rs, unsigned soff) { asm volatile("buffer_load_dwordx2 %0, %1, %2, %3 offen" : "=v"(d) : "v"(voff), "s"(rs), "s"(soff) : "memory"); }
DI void ld2p(unsigned& d, unsigned voff, rsrc_t rs, unsigned soff) { asm volatile("buffer_load_ushort %0, %1, %2, %3 offen" : "=v"(d) : "v"(voff), "s"(rs), "s"(soff) : "memory"); }
DI void ld4(unsigned& d, unsigned voff, rsrc_t rs, unsigned soff) { asm volatile("buffer_load_dword %0, %1, %2, %3 offen sc1" : "=v"(d) : "v"(voff), "s"(rs), "s"(soff) : "memory"); }
template <int N> DI void waitv() { asm volatile("s_waitcnt vmcnt(%0)" ::"n"(N) : "memory"); }
template <class T> DI void tie(T& a) { asm volatile("" : "+v"(a)); }
DI void rbar() { asm volatile("s_barrier" ::: "memory"); }
DI void bar() { asm volatile("s_waitcnt lgkmcnt(0)\n\ts_barrier" ::: "memory"); }
DI void st16(v4u v, rsrc_t rs, unsigned off) { __builtin_amdgcn_raw_buffer_store_b128(v, rs, off, 0, 16); }
DI void st8(v2u v, rsrc_t rs, unsigned off) { __builtin_amdgcn_raw_buffer_store_b64(v, rs, off, 0, 16); }
DI v4u trpair(LAS char* L, unsigned a0, unsigned a1) {
  v4s lo = __builtin_amdgcn_ds_read_tr16_b64_v4i16((LAS v4s*)(L + a0));
  v4s hi = __builtin_amdgcn_ds_read_tr16_b64_v4i16((LAS v4s*)(L + a1));
  v2u l = __builtin_bit_cast(v2u, lo), h = __builtin_bit_cast(v2u, hi);
  return v4u{l.x, l.y, h.x, h.y};
}
DI v4u lds16(LAS char* L, unsigned a) { return *(LAS v4u*)(L + a); }
DI f32x4 ldsf4(LAS char* L, unsigned a) { return *(LAS f32x4*)(L + a); }

template <int... I, class F> DI void sfor_impl(std::integer_sequence<int, I...>, F&& f) { (f(std::integral_constant<int, I>{}), ...); }
template <int N, class F> DI void sfor(F&& f) { sfor_impl(std::make_integer_sequence<int, N>{}, f); }
#define SB() __builtin_amdgcn_sched_barrier(0)

template <int N> using IC = std::integral_constant<int, N>;
DI void affine4(unsigned x0, unsigned x1, f32x4 a, f32x4 s, unsigned& o0, unsigned& o1) {
  float t0, t1;
  const float a0 = a[0], a1 = a[1], a2 = a[2], a3 = a[3], s0 = s[0], s1 = s[1], s2 = s[2], s3 = s[3];
  asm volatile("v_fma_mix_f32 %0, %4, %6, %10 op_sel_hi:[1,0,0]\n\tv_fma_mix_f32 %2, %4, %7, %11 op_sel:[1,0,0] op_sel_hi:[1,0,0]\n\t"
      "v_fma_mix_f32 %1, %5, %8, %12 op_sel_hi:[1,0,0]\n\tv_fma_mix_f32 %3, %5, %9, %13 op_sel:[1,0,0] op_sel_hi:[1,0,0]\n\t"
      "v_cvt_pk_bf16_f32 %0, %0, %2\n\tv_cvt_pk_bf16_f32 %1, %1, %3"
      : "=&v"(o0), "=&v"(o1), "=&v"(t0), "=&v"(t1) : "v"(x0), "v"(x1), "v"(a0), "v"(a1), "v"(a2), "v"(a3), "v"(s0), "v"(s1), "v"(s2), "v"(s3));
}
constexpr int ring_off(int pc) { return (pc & 3) * 15360 + ((pc >> 2) % 15) * 1024; }
DI unsigned strm_off(int pc, int rounds) { return ((pc & 3) * rounds + (pc >> 2)) * 1024; }
DI float dot2(unsigned a, unsigned b, float c) { return __builtin_amdgcn_fdot2_f32_bf16(__builtin_bit_cast(bf16x2, a), __builtin_bit_cast(bf16x2, b), c, false); }
DI f32x2 xswp32(float a, float c) { auto r = __builtin_amdgcn_permlane32_swap(__builtin_bit_cast(unsigned, a), __builtin_bit_cast(unsigned, c), false, false); unsigned r0 = r[0], r1 = r[1]; return f32x2{__builtin_bit_cast(float, r0), __builtin_bit_cast(float, r1)}; }
DI f32x2 xswp16(float a, float c) { auto r = __builtin_amdgcn_permlane16_swap(__builtin_bit_cast(unsigned, a), __builtin_bit_cast(unsigned, c), false, false); unsigned r0 = r[0], r1 = r[1]; return f32x2{__builtin_bit_cast(float, r0), __builtin_bit_cast(float, r1)}; }

enum { OP_A, OP_W, OP_ST, OP_UV, OP_STORE, OP_PEEK, OP_A5, OP_MW, OP_MC };
struct Sched {
  int n, tot; int ty[128], ix[128], cn[128], pos[128], start[27], before[27];
  constexpr void put(int t, int i, int c) { ty[n] = t; ix[n] = i; cn[n] = c; pos[n] = tot; tot += c; ++n; }
  constexpr void puts(int t, int i, int c) { for (int k = 0; k < c; ++k) put(t, i + k, 1); }
  constexpr void putw(int r0, int r1) {
    while (r0 < r1) { int c = r1 - r0; if (c > 4) c = 4; if (r0 % 15 + c > 15) c = 15 - r0 % 15; put(OP_W, r0, c); r0 += c; } }
  constexpr void seg(int q) { start[q] = n; before[q] = tot; }
  constexpr int at(int t, int i) const { for (int k = 0; k < n; ++k) if (ty[k] == t && i >= ix[k] && i < ix[k] + cn[k]) return pos[k] + i - ix[k]; return -1; }
};
constexpr int cmax(int a, int b) { return a > b ? a : b; }
constexpr Sched make_sched() {
  Sched s{}; int wr = 12;
  s.putw(0, 12); s.puts(OP_A, 0, 12);
  for (int q = 0; q < 25; ++q) {
    s.seg(q);
    if (q == 10 || q == 14 || q == 18) s.put(OP_PEEK, (q - 10) / 4, 1);
    if (q == 0) s.puts(OP_A, 12, 8);
    if (q == 2) { s.puts(OP_ST, 0, 4); s.puts(OP_UV, 0, 2); }
    if (q == 11) s.puts(OP_A5, 0, 8);
    if (q == 15 || q == 16) s.puts(OP_A5, 8 + 4 * (q - 15), 4);
    if (q == 19 || q == 20) s.puts(OP_A5, 16 + 4 * (q - 19), 4);
    int lim = (10 * (q <= 12 ? q : q - 1) + 60) / 4; if (lim > 60) lim = 60; if (lim > wr + 4) lim = wr + 4; if (q < 2) lim = wr;
    s.putw(wr, lim); wr = lim;
    if (q == 6 || q == 10 || q == 12) s.put(OP_STORE, q == 6 ? 0 : q == 10 ? 1 : 2, 1);
  }
  s.seg(25);
  return s;
}
constexpr Sched SC = make_sched();
constexpr int sc_wait(int q) {
  int p = -1;
  if (q <= 11) p = SC.at(OP_W, (10 * (q + 1) + (q == 11 ? 0 : q % 4 == 2 ? 10 : 6) + 3) / 4 - 1);
  if (q >= 13) p = cmax(SC.at(OP_W, (10 * q + (q < 24 ? 10 : 0) + 3) / 4 - 1), SC.at(OP_A5, 2 * (q - 13) + 1));
  if (q < 4) p = cmax(p, SC.at(OP_A, 5 * q + 4));
  if (q == 8 || q == 12 || q == 15) p = cmax(p, SC.at(OP_STORE, q == 8 ? 0 : q == 12 ? 1 : 2));
  if (q == 11 || q == 15 || q == 19) p = cmax(p, SC.at(OP_PEEK, (q - 11) / 4));
  return SC.before[q] - 1 - p;
}
constexpr Sched make_sched1() {
  Sched s{}; int a = 5, r = 7;
  s.putw(0, 4); s.puts(OP_A, 0, 3); s.putw(4, 7); s.puts(OP_A, 3, 2);
  for (int q = 0; q < 14; ++q) {
    s.seg(q);
    int na = q < 5 ? 3 : 0, nw = q < 4 ? 2 : q < 9 ? 1 : 0;
    s.puts(OP_A, a, na); a += na; s.putw(r, r + nw); r += nw;
  }
  s.seg(14);
  return s;
}
constexpr Sched S1 = make_sched1();
constexpr int s1_first(int q) { return q <= 10 ? 6 * q : q == 11 ? 64 : q == 12 ? 70 : q == 13 ? 75 : 80; }
constexpr int s1_wait(int q) {
  int pc = s1_first(q + 1) + 6; if (pc > 80 || q == 11) pc = 80;
  int p = S1.at(OP_W, (pc + 3) / 4 - 1);
  if (q < 10) p = cmax(p, S1.at(OP_A, q < 9 ? 2 * q + 2 : 19));
  return S1.before[q] - 1 - p;
}

#define STAMP(i)

DI void poll(const GAS unsigned* addr, unsigned base, unsigned want, bool active) {
#pragma nounroll
  for (unsigned s = 0; s < SPIN_CAP; ++s) {
    unsigned v = __hip_atomic_load(addr, RLX_AGENT);
    if (all64(!active || (v - base) >= want)) break;
    __builtin_amdgcn_s_sleep(2);
  }
  __builtin_amdgcn_fence(__ATOMIC_ACQUIRE, "wavefront");
}
DI unsigned lds_arrive(LAS char* L, int off, int lane) {
  unsigned o = 0;
  asm volatile("s_waitcnt lgkmcnt(0)" ::: "memory");
  if (lane == 0) o = __hip_atomic_fetch_add((LAS unsigned*)(L + off), 1u, __ATOMIC_RELAXED, __HIP_MEMORY_SCOPE_WORKGROUP);
  o = __builtin_amdgcn_readfirstlane(o);
  asm volatile("" ::: "memory");
  return o;
}
template <int N> DI void mod_rows(const v4u* wm, const unsigned* cv, LAS char* red, int lane) {
  float acc[8] = {0, 0, 0, 0, 0, 0, 0, 0};
#pragma unroll
  for (int i = 0; i < N; ++i) {
    float cf = bflo(cv[i]);
    float sv = bfr(cf * __builtin_amdgcn_rcpf(1.f + __builtin_amdgcn_exp2f(-1.44269504f * cf)));
#pragma unroll
    for (int e = 0; e < 4; ++e) { acc[2 * e] += sv * bflo(wm[i][e]); acc[2 * e + 1] += sv * bfhi(wm[i][e]); }
  }
  float u[4], v[2];
#pragma unroll
  for (int e = 0; e < 4; ++e) { f32x2 r = xswp32(acc[e], acc[e + 4]); u[e] = r.x + r.y; }
#pragma unroll
  for (int e = 0; e < 2; ++e) { f32x2 r = xswp16(u[e], u[e + 2]); v[e] = ror8_add(r.x + r.y); }
  if (!(lane & 8)) *(LAS f32x2*)(red + (lane & 7) * 32 + (lane >> 4) * 8) = f32x2{v[0], v[1]};
}
DI void lds_wait(LAS char* L, int off, unsigned want) {
#pragma nounroll
  for (unsigned s = 0; s < SPIN_CAP && *(volatile LAS unsigned*)(L + off) < want; ++s) __builtin_amdgcn_s_sleep(1);
  asm volatile("" ::: "memory");
}

__global__ __launch_bounds__(512, 2) void fused_block(Params P) {
  __shared__ __attribute__((aligned(16))) char lds_raw[163840];
  LAS char* L = (LAS char*)lds_raw;
  const unsigned LB = (unsigned)(size_t)L;
  const int tid = threadIdx.x, lane = tid & 63;
  const int w = __builtin_amdgcn_readfirstlane(tid >> 6);
  const int b = blockIdx.x;
  const int p = 4 * ((b & 7) >> 1) + ((b >> 3) & 3), j = 8 * (b & 1) + (b >> 5);
  const int m = w & 3, kh = w >> 2, h = lane >> 5, r32 = lane & 31;
  const int g4 = lane >> 4, i16 = lane & 15, q4 = (lane & 15) >> 2, p4 = lane & 3, blk = (lane >> 4) & 1;
  const rsrc_t rws = __builtin_amdgcn_make_buffer_rsrc((void*)P.ws, 0, WS_END, 0x00020000);
  const rsrc_t rx = __builtin_amdgcn_make_buffer_rsrc((void*)P.x, 0, 2048 * 1280, 0x00020000);
  const unsigned l16 = lane * 16;
  const unsigned ringL = kh * 61440 + l16, ringD = LB + kh * 61440 + m * 15360;
  STAMP(0);
  unsigned long long pc0; asm volatile("s_getpc_b64 %0" : "=s"(pc0));
  unsigned cbase = 0;
  const GAS unsigned* caddr = (const GAS unsigned*)P.ws;
  const int c40 = b % 40, c20 = (b - 160) % 20;
  const int cc = b < 160 ? (c40 < 20 ? c40 : c40 + 10) : (c20 < 10 ? 20 + c20 : 40 + c20), kq = b < 160 ? b / 40 : ((b - 160) / 20) & 3;
  const char* wm0 = (const char*)P.wmod + (size_t)(160 * kq) * 7680 + cc * 128;
  if (w <= 5) {
    const int bj = b < 240 ? b : 0, s = bj / 6, q = bj % 6;
    const int lr = (lane * 205) >> 11, lc = lane - 10 * lr;
    const bool lo = w < 4;
    v4u t[8]; unsigned cv[5];
    {
      const char* px = (const char*)P.x + (size_t)b * 10240 + (w & 1) * 640;
      const rsrc_t r1 = __builtin_amdgcn_make_buffer_rsrc((void*)(lo ? wm0 : px), 0, lo && b >= 160 ? 0 : 0x200000, 0x00020000);
      const rsrc_t r2 = __builtin_amdgcn_make_buffer_rsrc((void*)(lo ? (const char*)P.wqkv + (size_t)(16 * s) * 3840 + q * 640 + w * 160 : px), 0, lo ? 0x10000 : 0, 0x00020000);
      const rsrc_t rc = __builtin_amdgcn_make_buffer_rsrc((void*)(P.c + 160 * kq), 0, lo && b < 160 ? 320 : 0, 0x00020000);
      const unsigned vx = (lane & 7) * 1280 + (lane >> 3) * 16;
      const unsigned v1 = lo ? (tid >> 3) * 7680 + (tid & 7) * 16 : vx, st1 = lo ? 245760 : 128;
      const unsigned v2 = (lr < 6 ? lr : 5) * 3840 + lc * 16;
      sfor<5>([&](auto I) { ld16p(t[I], v1, r1, I * st1); });
      ld16p(t[5], v2, r2, 0); ld16p(t[6], v2, r2, 23040); ld16p(t[7], lr >= 4 ? v2 - 7680 : v2, r2, 46080);
      sfor<5>([&](auto I) { ld2p(cv[I], (tid >> 3) * 2, rc, I * 64); });
    }
    if (w == 0) {
      int off = CT_E;
      if (lane == 1) off = CT_C1 + (((p >= 1 ? p - 1 : 0) * 16 + j) << 7);
      if (lane == 2) off = CT_C1 + (((p >= 2 ? p - 2 : 0) * 16 + j) << 7);
      if (lane == 3) off = CT_C2 + (p << 7);
      if (lane == 4) off = CT_C3 + (p << 7);
      if (lane == 5) off = CT_C4 + p * 384;
      if (lane == 16 || lane == 17) off = CT_C4 + p * 384 + (lane - 15) * 128;
      if (lane == 6) off = CT_CW;
      if (lane == 7) off = CT_CM;
      if (lane >= 8 && lane < 16) off = CT_CB + ((lane - 8) << 7);
      caddr = (const GAS unsigned*)(P.ws + off);
    }
    asm volatile("global_load_dword %0, %1, off sc1" : "=v"(cbase) : "v"(caddr) : "memory");
    if (tid < 2) *(LAS v4u*)(L + L_CNT + tid * 16) = v4u{0u, 0u, 0u, 0u};
    bar();
    waitv<0>();
    sfor<8>([&](auto I) { tie(t[I]); });
    sfor<5>([&](auto I) { tie(cv[I]); });
    tie(cbase);
    if (tid == 0) *(volatile LAS unsigned*)(L + L_CNT + 12) = 1u;
    if (lo) {
      const unsigned wql = L_WQ + w * 2560;
      if (lr < 6) { *(LAS v4u*)(L + wql + l16) = t[5]; *(LAS v4u*)(L + wql + 960 + l16) = t[6]; }
      if (lr < 4) *(LAS v4u*)(L + wql + 1920 + l16) = t[7];
      if (b < 160) {
        mod_rows<5>(t, cv, L + L_RED + w * 256, lane);
        if (lds_arrive(L, L_CNT, lane) == 3 && lane < 16) {
          f32x4 u = (ldsf4(L, L_RED + l16) + ldsf4(L, L_RED + 256 + l16)) + (ldsf4(L, L_RED + 512 + l16) + ldsf4(L, L_RED + 768 + l16));
          st16(__builtin_bit_cast(v4u, u), rws, WS_MODP + (kq * 3840 + cc * 64 + lane * 4) * 4);
        }
      }
      if (b < 240) {
        asm volatile("s_waitcnt lgkmcnt(0)" ::: "memory");
        const int part = q >> 1, jb = 8 * (q & 1) + 2 * w;
        const int sg = s >= 20 ? s - 20 : s, pk0 = 3 * (sg & ~1);
        const unsigned dst0 = WS_WT1 + jb * 163840 + (s >= 20 ? 81920 : 0);
        const unsigned dmain = dst0 + strm_off(part == 0 ? 60 + sg : pk0 + 2 * (sg & 1) + part - 1, 20), dmix = dst0 + strm_off(pk0 + 4 + (sg & 1), 20);
        const unsigned rowa = wql + (8 * h + q4) * 160;
        const unsigned a = rowa + (20 * blk + 4 * p4) * 2, a2 = rowa + (40 * blk + 16 + 20 * (p4 & 1)) * 2;
        v4u f0 = trpair(L, a, a + 640), f1 = trpair(L, a + 80, a + 720), f2 = trpair(L, a2, a2 + 640);
        st16(f0, rws, dmain + l16);
        st16(f1, rws, dmain + 163840 + l16);
        if (i16 >= 8) f2 = v4u{0, 0, 0, 0};
        const int r = (part == 0 ? 16 : part == 1 ? 0 : 8) + i16;
        if (part == 0 || i16 < 8) st16(f2, rws, dmix + blk * 163840 + (h * 32 + r) * 16);
      }
    } else {
      float s1 = 0.f, s2 = 0.f, s3 = 0.f, s4 = 0.f;
#pragma unroll
      for (int i = 0; i < 5; ++i) {
        s1 = dot2(t[i][0], 0x3f803f80u, s1); s2 = dot2(t[i][0], t[i][0], s2); s3 = dot2(t[i][1], 0x3f803f80u, s3); s4 = dot2(t[i][1], t[i][1], s4);
        s1 = dot2(t[i][2], 0x3f803f80u, s1); s2 = dot2(t[i][2], t[i][2], s2); s3 = dot2(t[i][3], 0x3f803f80u, s3); s4 = dot2(t[i][3], t[i][3], s4);
      }
      s1 = swap32_add(swap16_add(ror8_add(s1 + s3))); s2 = swap32_add(swap16_add(ror8_add(s2 + s4)));
      if (lane < 8) *(LAS f32x2*)(L + L_RED + 1024 + (w & 1) * 64 + lane * 8) = f32x2{s1, s2};
      lds_arrive(L, L_CNT + 8, lane);
      lds_wait(L, L_CNT + 8, 2u);
      const f32x2 o2 = *(LAS f32x2*)(L + L_RED + 1024 + ((w & 1) ^ 1) * 64 + (lane & 7) * 8);
      const float mean = (s1 + o2.x) * (1.f / 640.f);
      const float rstd = __builtin_amdgcn_rsqf(fmaxf((s2 + o2.y) * (1.f / 640.f) - mean * mean, 0.f) + 1e-5f), nm = -mean * rstd;
      const int tok = 8 * b + (lane & 7);
      const unsigned xo = WS_XH + (tok >> 5) * 40960 + (w & 1) * 20480 + (lane >> 3) * 512 + (tok & 31) * 16;
#pragma unroll
      for (int i = 0; i < 5; ++i) {
        v4u o;
#pragma unroll
        for (int e = 0; e < 4; ++e) { f32x2 v = {fmaf(bflo(t[i][e]), rstd, nm), fmaf(bfhi(t[i][e]), rstd, nm)}; o[e] = __builtin_bit_cast(unsigned, __builtin_convertvector(v, f16x2)); }
        st16(o, rws, xo + i * 4096);
      }
    }
    waitv<0>();
    if (lds_arrive(L, L_CNT + 4, lane) == 5 && lane < 8) __hip_atomic_store((GAS unsigned*)(P.ws + CT_FLAG + lane * 1024 + b * 4), __builtin_amdgcn_readfirstlane(cbase) + 1u, RLX_AGENT);
    STAMP(1);
  } else {
    bar();
    if (w == 7) {
      unsigned d; v4u d4;
      const GAS char* t = lane < 16 ? (const GAS char*)P.ws + lane * 2000000 : lane < 18 ? (const GAS char*)P.out + (lane - 16) * 2000000 : (const GAS char*)P.ws;
      const GAS char* cp = (const GAS char*)(pc0 & ~255ull) + (b >> 3) * 1024 + l16;
      asm volatile("global_load_dwordx4 %1, %3, off\n\tglobal_load_dword %0, %2, off\n\ts_waitcnt vmcnt(0)" : "=&v"(d), "=&v"(d4) : "v"(t), "v"(cp) : "memory");
    }
    if (b >= 224) {
      const int v = w - 6, kr = (b - 224) >> 3, cw = b & 7;
      const rsrc_t ro = __builtin_amdgcn_make_buffer_rsrc((void*)((const char*)P.wo + (size_t)(160 * kr) * 1280 + cw * 160), 0, 0x00100000, 0x00020000);
      int row = (v * 64 + lane) / 10, cl = (v * 64 + lane) % 10;
      lds_wait(L, L_CNT + 12, 1u);
      for (int i = v; i < 25; i += 2) {
        dma<false>(LB + L_BSRC + i * 1024, row * 1280 + cl * 16, ro, 0);
        row += 12; cl += 8; if (cl >= 10) { cl -= 10; ++row; }
      }
      waitv<0>();
      lds_arrive(L, L_CNT + 16, lane);
      lds_wait(L, L_CNT + 16, 2u);
      for (int u = v; u < 25; u += 2) {
        const int sl = u / 5, t = u % 5, c = 4 * sl + g4, jl = c / 5, ii = c % 5;
        const int r0 = ii < 4 ? 40 * jl + 20 * (ii >> 1) + 4 * (ii & 1) : 40 * jl + 16;
        const int r1 = ii < 4 ? r0 + 8 : r0 + 20;
        const unsigned ca = L_BSRC + q4 * 160 + (16 * t + 4 * p4) * 2;
        v4u f = trpair(L, ca + r0 * 160, ca + r1 * 160);
        const int s = 5 * kr + sl;
        st16(f, rws, WS_WT3 + cw * 106496 + (s & 1) * 53248 + strm_off((s >> 1) * 5 + t, 13) + l16);
      }
      waitv<0>();
    }
  }
  const int c8 = 4 * (b & 1) + ((b >> 5) & 3), rh = b >> 7;
  int col = tid < 320 ? 16 * tid : tid < 340 ? (2560 + 80 * ((b >> 3) & 7) + 4 * (tid - 320)) * 4 : tid < 360 ? (1920 + 80 * ((b >> 3) & 7) + 4 * (tid - 340)) * 4 : 0;
  unsigned mdst = L_MODV + (tid < 160 ? tid * 32 + 16 : tid < 320 ? (tid - 160) * 32 : 5760 + (tid - 320) * 16);
  const rsrc_t rb = __builtin_amdgcn_make_buffer_rsrc((void*)(b < 128 ? (const char*)((b >> 6) ? P.wu : P.wg) + (size_t)(80 * ((b >> 3) & 7)) * 3072 + (b & 7) * 384
                                                                       : (const char*)P.wd + (size_t)(32 * (((b - 128) >> 1) % 48)) * 1280 + (b & 1) * 640), 0, b < 224 ? 0x00400000 : 0, 0x00020000);
  unsigned bo[4];
#pragma unroll
  for (int i = 0; i < 4; ++i) {
    int c = (i * 8 + w) * 64 + lane; if (c >= (b < 128 ? 1920 : 1280)) c = 0;
    bo[i] = b < 128 ? (c / 24) * 3072 + (c % 24) * 16 : (c / 40) * 1280 + (c % 40) * 16; tie(bo[i]);
  }
  tie(col); tie(mdst);
  if (w == 4) {
    const unsigned tag = __builtin_amdgcn_readfirstlane(cbase) + 1u;
#pragma nounroll
    for (unsigned s = 0; s < SPIN_CAP; ++s) {
      v4u f; ld16(f, l16, rws, CT_FLAG + (b & 7) * 1024); waitv<0>(); tie(f);
      if (all64(f.x == tag && f.y == tag && f.z == tag && f.w == tag)) break;
      __builtin_amdgcn_s_sleep(1);
    }
    __builtin_amdgcn_fence(__ATOMIC_ACQUIRE, "wavefront");
  }
  bar();
  STAMP(4);

  auto mod_put = [&](v4u* mp, bool isc, bool on, unsigned dst) {
    sfor<4>([&](auto I) { tie(mp[I]); });
    f32x4 v = (__builtin_bit_cast(f32x4, mp[0]) + __builtin_bit_cast(f32x4, mp[1])) + (__builtin_bit_cast(f32x4, mp[2]) + __builtin_bit_cast(f32x4, mp[3]));
#pragma unroll
    for (int e = 0; e < 4; ++e) { float t = bfr(v[e]); v[e] = isc ? bfr(1.f + t) : t; }
    if (on) *(LAS f32x4*)(L + dst) = v;
  };
  const bool mod2 = b >= 160 && b < 240;
  v4u m2w[3]; unsigned m2c[3];
  {
    v4u mp[4], xf[20];
    sfor<4>([&](auto I) { ld16(mp[I], col, rws, WS_MODP + I * 15360); });
    const unsigned w1 = WS_WT1 + j * 163840 + kh * 81920 + m * 20480;
    const unsigned ab = WS_XH + ((4 * p + m) * 40 + 20 * kh) * 1024;
    rbar();
    dman<4, 0, 0>(ringD, l16, rws, w1);
    sfor<3>([&](auto I) { ldf<I>(xf[I], l16, rws, ab); });
    if (tid == 384 && b >= 224) __hip_atomic_fetch_add((GAS unsigned*)(P.ws + CT_CW), 1u, RLX_AGENT);
    waitv<7>();
    mod_put(mp, tid >= 160 && tid < 340, tid < 360, mdst);
    dman<3, 4096, 4096>(ringD, l16, rws, w1);
    ldf<3>(xf[3], l16, rws, ab); ldf<4>(xf[4], l16, rws, ab);
    const rsrc_t rm = __builtin_amdgcn_make_buffer_rsrc((void*)wm0, 0, mod2 ? 160 * 7680 : 0, 0x00020000);
    const rsrc_t rc = __builtin_amdgcn_make_buffer_rsrc((void*)(P.c + 160 * kq), 0, mod2 ? 320 : 0, 0x00020000);
    STAMP(5);
    f32x16 acc[4] = {};
    const unsigned wbK = ringL + kh * 15360, wbS = ringL + 15360 - kh * 15360;
    unsigned cb = L_MODV + (80 * kh + 2 * h) * 32; tie(cb);
    unsigned xw = kh * 61440 + m * 15360 + 5120 + l16, xr = (kh ^ 1) * 61440 + m * 15360 + 5120 + l16;
    v4u wf[80], hf[20], c0, c1, c2; f32x4 ca[40], cs[40], eo[6];
    const unsigned kvg = WS_KV + ((4 * p + m) * 16 + j) * 5120, kvl = (8 + m) * KVT;
    auto rdw = [&](auto N) {
      constexpr int n = N < 60 ? 3 * (N / 3) + 2 - N % 3 : (N ^ 1), i = n / 3, pk0 = 3 * (i & ~1);
      if constexpr (n >= 60) wf[n] = lds16(L, ringL + ring_off(n));
      else if constexpr (n % 3 == 1) wf[n] = lds16(L, ringL + ring_off(pk0 + 4 + (i & 1)));
      else wf[n] = lds16(L, (n % 3 == 0 ? wbS : wbK) + ring_off(pk0 + 2 * (i & 1)));
    };
    auto rdc = [&](auto R) { ca[R] = ldsf4(L, cb + (R / 2) * 128 + (R & 1) * 32); cs[R] = ldsf4(L, cb + (R / 2) * 128 + (R & 1) * 32 + 16); };
    auto aff = [&](auto I, auto E) { constexpr int e = 2 * E; unsigned o0, o1; affine4(xf[I][e], xf[I][e + 1], ca[2 * I + E], cs[2 * I + E], o0, o1); hf[I][e] = o0; hf[I][e + 1] = o1; };
    auto quad = [&](const f32x16& a, int q) { return f32x4{a[4 * q], a[4 * q + 1], a[4 * q + 2], a[4 * q + 3]}; };
    sfor<14>([&](auto Q) {
      constexpr int q = Q, n0 = s1_first(q);
      waitv<(q == 13 ? 10 : q == 12 ? 63 : s1_wait(q))>();
      if constexpr (q == 0) tie(xf[0]);
      if constexpr (q < 10) { tie(xf[2 * q + 1]); if constexpr (q < 9) tie(xf[2 * q + 2]); }
      if constexpr (q == 0 || q == 11) bar(); else rbar();
      if constexpr (q == 13) { if (tid == 0) __hip_atomic_fetch_add((GAS unsigned*)(P.ws + CT_C1 + ((p * 16 + j) << 7)), 1u, RLX_AGENT); }
      if constexpr (q == 0) { sfor<6>(rdc); sfor<6>(rdw); aff(IC<0>{}, IC<0>{}); aff(IC<0>{}, IC<1>{}); asm volatile("s_nop 1"); SB(); }
      sfor<s1_first(q + 1) - n0>([&](auto K) {
        constexpr int n = n0 + K, i = n < 60 ? n / 3 : n - 60, t = n < 60 ? n % 3 : 3, k = S1.start[q] + K;
        acc[t] = mfma32(wf[n], hf[i], acc[t]); tie(acc[t]);
        SB();
        if constexpr (t < 2 && i + 1 < 20) aff(IC<i + 1>{}, IC<t>{});
        if constexpr (t < 2 && i + 3 < 20) rdc(IC<2 * (i + 3) + t>{});
        if constexpr (n + 6 < 80) rdw(IC<n + 6>{});
        if constexpr (k < S1.start[q + 1]) {
          constexpr int ty = S1.ty[k], ix = S1.ix[k];
          if constexpr (ty == OP_A) ldf<ix>(xf[ix], l16, rws, ab);
          else if constexpr (ty == OP_W) dman<S1.cn[k], (ix % 15) * 1024, ix * 1024>(ringD, l16, rws, w1);
        }
        if constexpr (n >= 59 && n < 63) *(LAS f32x4*)(L + xw + (n - 59) * 1024) = quad(acc[0], n - 59);
        if constexpr (n == 63) { if (kh) { *(LAS f32x4*)(L + xw + 4096) = quad(acc[1], 0); *(LAS f32x4*)(L + xw + 5120) = quad(acc[1], 1); } }
        if constexpr (n == 64) sfor<6>([&](auto I) { eo[I] = ldsf4(L, xr + I * 1024); });
        if constexpr (n == 65 || n == 66) {
          constexpr int o = 8 * (n - 65);
          v4u c = {pk2(acc[2][o] + eo[o / 4][0], acc[2][o + 1] + eo[o / 4][1]), pk2(acc[2][o + 2] + eo[o / 4][2], acc[2][o + 3] + eo[o / 4][3]),
                   pk2(acc[2][o + 4] + eo[o / 4 + 1][0], acc[2][o + 5] + eo[o / 4 + 1][1]), pk2(acc[2][o + 6] + eo[o / 4 + 1][2], acc[2][o + 7] + eo[o / 4 + 1][3])};
          st16(c, rws, kvg + (kh ? 3072 + r32 * 32 + h * 16 : l16) + (n - 65) * 1024);
          if constexpr (n == 65) c0 = c; else c1 = c;
        }
        if constexpr (n == 67) {
          c2 = v4u{pk2(acc[1][0] + eo[4][0], acc[1][1] + eo[4][1]), pk2(acc[1][2] + eo[4][2], acc[1][3] + eo[4][3]),
                   pk2(acc[1][4] + eo[5][0], acc[1][5] + eo[5][1]), pk2(acc[1][6] + eo[5][2], acc[1][7] + eo[5][3])};
          if (!kh) st16(c2, rws, kvg + 2048 + l16);
        }
        if constexpr (n >= 68 && n < 72) dma<false>(LB + L_BSRC + ((n - 68) * 8 + w) * 1024, bo[n - 68], rb, 0);
        if constexpr (n >= 72 && n < 75) { ld16p(m2w[n - 72], (tid >> 3) * 7680 + (tid & 7) * 16, rm, (n - 72) * 491520); ld2p(m2c[n - 72], (tid >> 3) * 2, rc, (n - 72) * 128); }
        SB();
      });
    });
    STAMP(6);
    const unsigned xq = 5120 + (m >> 1) * 15360 + (m & 1) * 5120 + l16;
    if (!kh) {
#pragma unroll
      for (int qd = 0; qd < 4; ++qd) *(LAS f32x4*)(L + xq + qd * 1024) = quad(acc[3], qd);
      *(LAS f32x4*)(L + xq + 4096) = quad(acc[1], 2);
    }
    bar();
    {
      const unsigned lo = kh ? 3072 + r32 * 48 + h * 16 : l16, ls = kh ? 1536 : 1024;
      *(LAS v4u*)(L + kvl + lo) = c0; *(LAS v4u*)(L + kvl + lo + ls) = c1;
    }
    if (kh == 0) {
      *(LAS v4u*)(L + kvl + 2048 + l16) = v4u{c2.x, c2.y, 0u, 0u};
      *(LAS v4u*)(L + kvl + 3072 + h * 1536 + r32 * 48 + 32) = v4u{c2.z, c2.w, 0x3f80u, 0u};
    } else {
      const float qs = 0.22360679775f * 1.44269504089f;
      float qv[20];
#pragma unroll
      for (int qd = 0; qd < 5; ++qd) {
        const f32x4 u = ldsf4(L, xq + qd * 1024);
#pragma unroll
        for (int e = 0; e < 4; ++e) qv[4 * qd + e] = ((qd < 4 ? acc[3][4 * qd + e] : acc[1][8 + e]) + u[e]) * qs;
      }
      *(LAS v4u*)(L + L_Q + m * 2048 + l16) = v4u{pk2(qv[0], qv[1]), pk2(qv[2], qv[3]), pk2(qv[4], qv[5]), pk2(qv[6], qv[7])};
      *(LAS v4u*)(L + L_Q + m * 2048 + 1024 + l16) = v4u{pk2(qv[8], qv[9]), pk2(qv[10], qv[11]), pk2(qv[12], qv[13]), pk2(qv[14], qv[15])};
      *(LAS v2u*)(L + L_QHI + m * 512 + lane * 8) = v2u{pk2(qv[16], qv[17]), pk2(qv[18], qv[19])};
    }
    STAMP(7);
  }
  if (w == 0) poll(caddr, cbase, lane == 6 ? 32u : 1u, (lane == 1 && p >= 1) || (lane == 2 && p >= 2) || lane == 6);
  waitv<0>();
  if (mod2) {
    sfor<3>([&](auto I) { tie(m2w[I]); tie(m2c[I]); });
    mod_rows<3>(m2w, m2c, L + L_RED2 + w * 256, lane);
  }
  bar();
  STAMP(8);
  v4u kv[5];
  const int gt = 4 * p - 8 + w;
  {
    const unsigned ko = gt >= 0 ? WS_KV + (gt * 16 + j) * 5120 : 0x7ff00000u;
    sfor<5>([&](auto I) { ldf<I>(kv[I], l16, rws, ko); });
  }
  if (mod2 && w == 7 && lane < 16) {
    f32x4 u = ldsf4(L, L_RED2 + l16);
#pragma unroll
    for (int i = 1; i < 8; ++i) u += ldsf4(L, L_RED2 + i * 256 + l16);
    st16(__builtin_bit_cast(v4u, u), rws, WS_MODP + (kq * 3840 + cc * 64 + lane * 4) * 4);
  }
  if (b < 128) {
    if (w < 6) {
      const int mat = b >> 6, kp = (b >> 3) & 7, cg = b & 7;
      const int tl = 2 * w + blk;
      const int jd = 2 * cg + tl / 6, t = tl % 6;
      float us = 0.f, vs = 0.f;
#pragma unroll
      for (int sg = 0; sg < 5; ++sg) {
        const int k0 = sg < 4 ? 32 * (sg >> 1) + 4 * (2 * (sg & 1) + h) : 64 + 8 * h;
        const int k1 = sg < 4 ? k0 + 16 : k0 + 4;
        const unsigned ca = L_BSRC + q4 * 384 + (16 * tl + 4 * p4) * 2;
        v4u f = trpair(L, ca + k0 * 384, ca + k1 * 384);
        f32x4 a0 = ldsf4(L, L_MODV + 5760 + k0 * 4), a1 = ldsf4(L, L_MODV + 5760 + k1 * 4);
        f32x4 b0 = ldsf4(L, L_MODV + 6080 + k0 * 4), b1 = ldsf4(L, L_MODV + 6080 + k1 * 4);
        v4u o;
        o[0] = pk2(bflo(f[0]) * a0[0], bfhi(f[0]) * a0[1]); o[1] = pk2(bflo(f[1]) * a0[2], bfhi(f[1]) * a0[3]);
        o[2] = pk2(bflo(f[2]) * a1[0], bfhi(f[2]) * a1[1]); o[3] = pk2(bflo(f[3]) * a1[2], bfhi(f[3]) * a1[3]);
        vs += bflo(f[0]) * b0[0] + bfhi(f[0]) * b0[1] + bflo(f[1]) * b0[2] + bfhi(f[1]) * b0[3] + bflo(f[2]) * b1[0] + bfhi(f[2]) * b1[1] + bflo(f[3]) * b1[2] + bfhi(f[3]) * b1[3];
        us = dot2(o[0], 0x3f803f80u, us); us = dot2(o[1], 0x3f803f80u, us); us = dot2(o[2], 0x3f803f80u, us); us = dot2(o[3], 0x3f803f80u, us);
        const int s = 5 * kp + sg;
        st16(o, rws, WS_WT4 + jd * 245760 + (s / 20) * 122880 + strm_off((t >> 1) * 40 + (s % 20) * 2 + (t & 1), 30) + (h * 32 + mat * 16 + i16) * 16);
      }
      us = swap32_add(us); vs = swap32_add(vs);
      const int row = (jd * 6 + t) * 32 + mat * 16 + i16;
      __builtin_amdgcn_raw_buffer_store_b32(__builtin_bit_cast(unsigned, h ? vs : us), rws, WS_UV + ((kp * 2 + h) * 3072 + row) * 4, 0, 16);
    }
  }
  if (b >= 128 && b < 224) {
    const int st0 = (b - 128) >> 1, st = 16 * (st0 % 3) + st0 / 3, ch = b & 1;
    for (int tt = w; tt < 20; tt += 8) {
      const unsigned a = L_BSRC + (16 * (g4 >> 1) + 4 * (g4 & 1) + q4) * 640 + (16 * tt + 4 * p4) * 2;
      v4u f = trpair(L, a, a + 8 * 640);
      st16(f, rws, WS_WT5 + (4 * ch + tt / 5) * 245760 + (st & 1) * 122880 + strm_off((st >> 1) * 5 + tt % 5, 30) + l16);
    }
  }
  STAMP(9);
  const unsigned w3 = WS_WT3 + c8 * 106496 + kh * 53248 + m * 13312;
  unsigned cbv;
  {
    waitv<0>();
    sfor<5>([&](auto I) { tie(kv[I]); });
    {
      const unsigned tb = w * KVT, vo = 3072 + (lane >> 1) * 48 + (lane & 1) * 16;
      *(LAS v4u*)(L + tb + l16) = kv[0]; *(LAS v4u*)(L + tb + 1024 + l16) = kv[1];
      *(LAS v4u*)(L + tb + 2048 + l16) = v4u{kv[2].x, kv[2].y, gt >= 0 ? 0u : 0xfe96u, 0u};
      *(LAS v4u*)(L + tb + 3072 + h * 1536 + r32 * 48 + 32) = v4u{kv[2].z, kv[2].w, 0x3f80u, 0u};
      *(LAS v4u*)(L + tb + vo) = kv[3]; *(LAS v4u*)(L + tb + vo + 1536) = kv[4];
    }
    bar();
    if (tid == 0 && mod2) __hip_atomic_fetch_add((GAS unsigned*)(P.ws + CT_CM), 1u, RLX_AGENT);
    if (tid == 64 && b < 224) __hip_atomic_fetch_add((GAS unsigned*)(P.ws + CT_CB + ((b & 7) << 7)), 1u, RLX_AGENT);
    STAMP(10);
    const int hd = kh;
    const v4u q0 = lds16(L, L_Q + m * 2048 + hd * 1024 + l16);
    v2u qh = *(LAS v2u*)(L + L_QHI + m * 512 + lane * 8);
    if (h != hd) qh = v2u{0, 0};
    const v4u q1 = {qh.x, qh.y, 0x3f80u, 0};
    const f32x16 zero = {};
    const unsigned ka = m * KVT + hd * 1024 + l16, kb = m * KVT + 2048 + l16;
    v4u k0[9], k1[9];
    sfor<9>([&](auto I) { constexpr int i = I == 0 ? 0 : I == 1 ? 8 : I - 1; k0[i] = lds16(L, ka + i * KVT); k1[i] = lds16(L, kb + i * KVT); });
    dman<4, L_W3A, 0>(LB + kh * 32768 + m * 8192, l16, rws, w3);
    dman<4, L_W3A + 4096, 4096>(LB + kh * 32768 + m * 8192, l16, rws, w3);
    auto qk = [&](auto I, f32x16 c) { f32x16 r = mfma32(k1[I], q1, mfma32(k0[I], q0, c)); tie(r); return r; };
    auto vmax = [](float a, float c) { return __builtin_elementwise_maximum(a, c); };
    auto tmax = [&](const f32x16& v, float t) {
#pragma unroll
      for (int r = 0; r < 16; r += 2) t = vmax(vmax(v[r], v[r + 1]), t);
      tie(t); return t; };
    f32x16 Sm, S[8];
    float mx = -3.0e38f;
    {
      const f32x16 s0 = qk(IC<0>{}, zero), s8 = qk(IC<8>{}, zero);
      S[1] = qk(IC<1>{}, zero); SB();
#pragma unroll
      for (int r = 0; r < 16; ++r) Sm[r] = (r & 3) + 8 * (r >> 2) + 4 * h <= r32 ? s8[r] : s0[r];
      tie(Sm); SB();
    }
    sfor<7>([&](auto I) {
      if constexpr (I < 6) S[I + 2] = qk(IC<I + 2>{}, zero);
      SB();
      if constexpr (I == 0) mx = tmax(Sm, mx); else mx = tmax(S[I], mx);
      SB();
    });
    const unsigned va = m * KVT + 3072 + hd * 1536 + (4 * h + q4) * 48 + (blk == 0 ? (p4 & 1) * 16 + (p4 >> 1) * 8 : (p4 ? 40 : 32));
    v4u vf[18];
    auto rdv = [&](auto I) { vf[2 * I + 1] = trpair(L, va + I * KVT + 768, va + I * KVT + 1152); vf[2 * I] = trpair(L, va + I * KVT, va + I * KVT + 384); };
    rdv(IC<8>{}); rdv(IC<0>{}); SB();
    mx = tmax(S[7], mx);
    { f32x2 t = swp32(mx); mx = vmax(t.x, t.y); }
    f32x16 negm;
#pragma unroll
    for (int r = 0; r < 16; ++r) negm[r] = -mx;
    tie(negm);
    f32x16 O = {};
    S[1] = qk(IC<1>{}, negm); rdv(IC<1>{}); SB();
#pragma unroll
    for (int s = 0; s < 2; ++s) {
      v4u p0, p8;
#pragma unroll
      for (int e = 0; e < 4; ++e) {
        const int r = 8 * s + 2 * e, key = (r & 3) + 8 * (r >> 2) + 4 * h;
        const float a = __builtin_amdgcn_exp2f(Sm[r] - mx), c = __builtin_amdgcn_exp2f(Sm[r + 1] - mx);
        const float a8 = key <= r32 ? a : 0.f, c8_ = key + 1 <= r32 ? c : 0.f;
        p8[e] = pk2(a8, c8_); p0[e] = pk2(a - a8, c - c8_);
      }
      O = mfma32(vf[s], p0, O); O = mfma32(vf[16 + s], p8, O); tie(O); SB();
    }
    sfor<7>([&](auto I) {
      constexpr int i = I + 1;
      if constexpr (i < 7) { S[i + 1] = qk(IC<i + 1>{}, negm); rdv(IC<i + 1>{}); SB(); }
      v4u pf[2];
#pragma unroll
      for (int e = 0; e < 8; ++e) pf[e / 4][e % 4] = pk2(__builtin_amdgcn_exp2f(S[i][2 * e]), __builtin_amdgcn_exp2f(S[i][2 * e + 1]));
      O = mfma32(vf[2 * i], pf[0], O); O = mfma32(vf[2 * i + 1], pf[1], O); tie(O); SB();
    });
    const float inv = __builtin_amdgcn_rcpf(swap32_hi(O[8]));
    const int tok = 128 * p + 32 * m + r32;
    const unsigned ob = WS_OF + (tok >> 4) * 20480 + (tok & 15) * 16;
    const int c = 5 * j + 2 * hd + h, c2 = 5 * j + 4;
    STAMP(11);
    st16(v4u{pk2(O[0] * inv, O[1] * inv), pk2(O[2] * inv, O[3] * inv), pk2(O[4] * inv, O[5] * inv), pk2(O[6] * inv, O[7] * inv)}, rws, ob + (((c >> 2) & 1) * 10 + (c >> 3)) * 1024 + (c & 3) * 256);
    if (h == 0) st8(v2u{pk2(O[8] * inv, O[9] * inv), pk2(O[10] * inv, O[11] * inv)}, rws, ob + (((c2 >> 2) & 1) * 10 + (c2 >> 3)) * 1024 + (c2 & 3) * 256 + hd * 8);
  }
  waitv<0>();
  bar();
  const int tt = 8 * p + 4 * rh + m;
  unsigned x2k[6];
  {
    v2u xr[3];
    const unsigned xo = (16 * tt + i16) * 1280 + (80 * c8 + 4 * g4) * 2;
    if (tid == 0) __hip_atomic_fetch_add((GAS unsigned*)(P.ws + CT_C2 + (p << 7)), 1u, RLX_AGENT);
    STAMP(12);
    v4u mp[4];
    ld8p(xr[0], xo, rx, kh * 64); ld8p(xr[1], xo, rx, kh * 64 + 32); ld8p(xr[2], xo, rx, 128);
    dman<4, 0, 8192>(LB + kh * 20480 + m * 5120, l16, rws, w3); dman<1, 4096, 12288>(LB + kh * 20480 + m * 5120, l16, rws, w3);
    if (w == 0) poll(caddr, cbase, lane == 7 ? 80u : 16u, lane == 3 || lane == 7);
    bar();
    STAMP(13);
    v4u af[10], wf[50];
    const unsigned ab = WS_OF + (tt * 20 + kh * 10) * 1024;
    sfor<10>([&](auto I) { ldf<I>(af[I], l16, rws, ab); });
    sfor<4>([&](auto I) { ld16(mp[I], ((lane < 20 ? 1280 : 3200 - 80) + 80 * c8 + 4 * (lane < 40 ? lane : 20)) * 4, rws, WS_MODP + I * 15360); });
    f32x4 acc[5] = {};
    unsigned wa = L_W3A + kh * 32768 + l16; tie(wa);
    const unsigned wb = kh * 20480 + l16;
    auto rdw = [&](auto N) {
      constexpr int n = 5 * (N / 5) + 4 - N % 5;
      if constexpr (n < 32) wf[n] = lds16(L, wa + (n & 3) * 8192 + (n >> 2) * 1024); else wf[n] = lds16(L, wb + (n & 3) * 5120 + ((n >> 2) - 8) * 1024);
    };
    sfor<10>([&](auto E) {
      constexpr int e = E;
      if constexpr (!(e & 1)) { waitv<12 - e>(); tie(af[e]); tie(af[e + 1]); }
      if constexpr (e == 0) { rbar(); sfor<8>(rdw); }
      sfor<5>([&](auto K) {
        constexpr int n = 5 * e + K;
        acc[K] = mfma16(wf[n], af[e], acc[K]); tie(acc[K]);
        if constexpr (n + 8 < 50) rdw(IC<n + 8>{});
        SB();
      });
    });
    STAMP(14);
    waitv<0>();
    mod_put(mp, false, w == 5 && lane < 40, L_MODV + 5120 + l16);
    asm volatile("global_load_dword %0, %1, off sc1" : "=v"(cbv) : "v"(caddr) : "memory");
    const unsigned xs = 40960 + m * 5120;
    if (kh == 0) { *(LAS f32x4*)(L + xs + l16) = acc[2]; *(LAS f32x4*)(L + xs + 1024 + l16) = acc[3]; }
    else { *(LAS f32x4*)(L + xs + 2048 + l16) = acc[0]; *(LAS f32x4*)(L + xs + 3072 + l16) = acc[1]; *(LAS f32x4*)(L + xs + 4096 + l16) = acc[4]; }
    bar();
    tie(xr[0]); tie(xr[1]); tie(xr[2]);
    f32x4 r0, r1, r2 = {};
    if (kh == 0) { r0 = acc[0] + ldsf4(L, xs + 2048 + l16); r1 = acc[1] + ldsf4(L, xs + 3072 + l16); r2 = acc[4] + ldsf4(L, xs + 4096 + l16); }
    else { r0 = acc[2] + ldsf4(L, xs + l16); r1 = acc[3] + ldsf4(L, xs + 1024 + l16); }
    const f32x4 ga0 = ldsf4(L, L_MODV + 5120 + (32 * kh + 4 * g4) * 4), ga1 = ldsf4(L, L_MODV + 5120 + (32 * kh + 16 + 4 * g4) * 4), ga2 = ldsf4(L, L_MODV + 5120 + (64 + 4 * g4) * 4);
    x2k[0] = pk2(bflo(xr[0].x) + ga0[0] * r0[0], bfhi(xr[0].x) + ga0[1] * r0[1]); x2k[1] = pk2(bflo(xr[0].y) + ga0[2] * r0[2], bfhi(xr[0].y) + ga0[3] * r0[3]);
    x2k[2] = pk2(bflo(xr[1].x) + ga1[0] * r1[0], bfhi(xr[1].x) + ga1[1] * r1[1]); x2k[3] = pk2(bflo(xr[1].y) + ga1[2] * r1[2], bfhi(xr[1].y) + ga1[3] * r1[3]);
    x2k[4] = pk2(bflo(xr[2].x) + ga2[0] * r2[0], bfhi(xr[2].x) + ga2[1] * r2[1]); x2k[5] = pk2(bflo(xr[2].y) + ga2[2] * r2[2], bfhi(xr[2].y) + ga2[3] * r2[3]);
    if (kh) { x2k[4] = 0; x2k[5] = 0; }
    const unsigned xb = WS_X2F + ((tt >> 1) * 40 + 5 * c8) * 1024 + ((tt & 1) * 16 + i16) * 16;
    st16(v4u{x2k[0], x2k[1], x2k[2], x2k[3]}, rws, xb + (2 * kh + (g4 >> 1)) * 1024 + (g4 & 1) * 512);
    if (kh == 0) st8(v2u{x2k[4], x2k[5]}, rws, xb + 4096 + (g4 >> 1) * 512 + (g4 & 1) * 8);
    float s1 = 0.f, s2 = 0.f;
#pragma unroll
    for (int e = 0; e < 6; ++e) { s1 = dot2(x2k[e], 0x3f803f80u, s1); s2 = dot2(x2k[e], x2k[e], s2); }
    s1 = swap32_add(swap16_add(s1)); s2 = swap32_add(swap16_add(s2));
    if (g4 == 0) st8(v2u{__builtin_bit_cast(unsigned, s1), __builtin_bit_cast(unsigned, s2)}, rws, WS_ST + ((tt >> 1) * 8 + c8) * 512 + ((tt & 1) * 16 + i16) * 16 + kh * 8);
  }
  waitv<0>();
  tie(cbv);
  if (w == 0 && !all64(lane < 8 || lane >= 16 || cbv - cbase >= 28u)) poll(caddr, cbase, 28u, lane >= 8 && lane < 16);
  bar();
  {
    const unsigned w4 = WS_WT4 + j * 245760 + kh * 122880 + m * 30720, w5 = WS_WT5 + c8 * 245760 + kh * 122880 + m * 30720 - 30720;
    if (tid == 0) __hip_atomic_fetch_add((GAS unsigned*)(P.ws + CT_C3 + (p << 7)), 1u, RLX_AGENT);
    STAMP(15);
    dman<4, 0, 0>(ringD, l16, rws, w4); dman<4, 4096, 4096>(ringD, l16, rws, w4); dman<4, 8192, 8192>(ringD, l16, rws, w4);
    if (w == 0) poll(caddr, cbase, 16u, lane == 4);
    bar();
    STAMP(16);
    v4u af[20], a5[24], stv[4], uv[2], wf[240]; unsigned pk;
    const unsigned ab = WS_X2F + ((4 * p + m) * 40 + 20 * kh) * 1024, ab5 = WS_ACT + (tt * 48 + 24 * kh) * 1024;
    sfor<12>([&](auto I) { ldf<I>(af[I], l16, rws, ab); });
    const int tok = 128 * p + 32 * m + r32;
    const unsigned ao = WS_ACT + ((tok >> 4) * 48 + (j & 1) * 24 + (j >> 1)) * 1024 + kh * 512 + h * 256 + (tok & 15) * 16;
    const int rq = (w < 6 ? w : 0) * 16 + i16;
    const unsigned uo = (g4 * 4 + rq / 48) * 12288 + (j * 192 + 4 * (rq % 48)) * 4;
    f32x16 aK[3] = {}, aS[3] = {};
    f32x4 acc[5] = {};
    const unsigned wbK = ringL + kh * 15360, wbS = ringL + 15360 - kh * 15360;
    unsigned xw = L_BSRC + w * 4096 + l16, xr_ = L_BSRC + (w ^ 4) * 4096 + l16, ub = L_UVS + kh * 128 + h * 16; tie(xw); tie(xr_); tie(ub);
    float rs2, nm2, av[8]; f32x4 eo[4], eu[4], ev[4];
    constexpr auto isS = [](int n) { return (n / 10) % 4 == 3 ? n % 10 < 5 : (n & 1) != 0; };
    constexpr auto stp = [](int n) { return (n / 10) % 4 == 3 ? (n / 10) * 5 + n % 5 : n / 2; };
    auto rdw = [&](auto N) {
      constexpr int n = N < 120 ? (N ^ 1) : 5 * (N / 5) + 4 - N % 5;
      if constexpr (n < 120) wf[n] = lds16(L, (isS(n) ? wbS : wbK) + ring_off(2 * stp(n))); else wf[n] = lds16(L, ringL + ring_off(n));
    };
    auto vmem = [&](auto Q, auto K) {
      constexpr int k = SC.start[Q] + K;
      if constexpr (k < SC.start[Q + 1]) {
        constexpr int ty = SC.ty[k], ix = SC.ix[k];
        if constexpr (ty == OP_A) ldf<ix>(af[ix], l16, rws, ab);
        else if constexpr (ty == OP_W) dman<SC.cn[k], (ix % 15) * 1024, ix * 1024>(ringD, l16, rws, ix < 30 ? w4 : w5);
        else if constexpr (ty == OP_ST) ld16(stv[ix], r32 * 16 + h * 2048 + ix * 512, rws, WS_ST + (4 * p + m) * 4096);
        else if constexpr (ty == OP_UV) ld16(uv[ix], uo, rws, WS_UV + ix * 24576);
        else if constexpr (ty == OP_PEEK) asm volatile("global_load_dword %0, %1, off sc1" : "=v"(pk) : "v"(caddr) : "memory");
        else if constexpr (ty == OP_A5) ldf<ix>(a5[ix], l16, rws, ab5);
      }
    };
    auto epiA = [&](auto G, auto QD) { const f32x16 sd = aS[G]; *(LAS f32x4*)(L + xw + QD * 1024) = f32x4{sd[4 * QD], sd[4 * QD + 1], sd[4 * QD + 2], sd[4 * QD + 3]}; };
    auto epi = [&](auto G, auto K) {
      if constexpr (K < 2) sfor<2>([&](auto I) { constexpr int qd = 2 * K + I; eo[qd] = ldsf4(L, xr_ + qd * 1024); eu[qd] = ldsf4(L, ub + G * 256 + qd * 32); ev[qd] = ldsf4(L, ub + 768 + G * 256 + qd * 32); });
      else if constexpr (K < 18) { constexpr int r = K - 2; float t = fmaf(aK[G][r] + eo[r / 4][r % 4], rs2, fmaf(nm2, eu[r / 4][r % 4], ev[r / 4][r % 4])); tie(t); aK[G][r] = t; }
      else if constexpr (K < 26) { constexpr int k = K - 18; const float gv = aK[G][k]; float t = gv * __builtin_amdgcn_rcpf(1.f + __builtin_amdgcn_exp2f(-1.44269504f * gv)) * aK[G][8 + k]; tie(t); av[k] = t; }
      else if constexpr (K == 29) st16(v4u{pk2(av[0], av[1]), pk2(av[2], av[3]), pk2(av[4], av[5]), pk2(av[6], av[7])}, rws, ao + G * 8192);
    };
    auto top = [&](auto Q) {
      constexpr int q = Q;
      waitv<sc_wait(q)>();
      if constexpr (q < 4) sfor<5>([&](auto I) { tie(af[5 * q + I]); });
      if constexpr (q >= 13) { tie(a5[2 * (q - 13)]); tie(a5[2 * (q - 13) + 1]); }
      if constexpr (q == 11 || q == 15 || q == 19) {
        constexpr int ln = q == 11 ? 5 : q == 15 ? 16 : 17;
        tie(pk);
        if (w == 0 && !all64(lane != ln || pk - cbase >= 16u)) poll(caddr, cbase, 16u, lane == ln);
      }
      if constexpr (q == 4 || q == 8 || q == 12) bar(); else rbar();
      if constexpr (q == 8 || q == 12 || q == 15) { if (tid == 64) __hip_atomic_fetch_add((GAS unsigned*)(P.ws + CT_C4 + p * 384 + (q == 8 ? 0 : q == 12 ? 128 : 256)), 1u, RLX_AGENT); }
    };
    sfor<12>([&](auto Q) {
      constexpr int q = Q, g = q / 4;
      top(Q);
      if constexpr (q == 0) sfor<6>(rdw);
      sfor<10>([&](auto K) {
        constexpr int n = 10 * q + K, i = stp(n) % 20;
        if constexpr (isS(n)) { aS[g] = mfma32(wf[n], af[i], aS[g]); tie(aS[g]); } else { aK[g] = mfma32(wf[n], af[i], aK[g]); tie(aK[g]); }
        if constexpr (n + 6 < 120) rdw(IC<n + 6>{});
        vmem(Q, K);
        if constexpr (q == 3 && K == 5) {
          waitv<SC.before[4] - 1 - SC.at(OP_UV, 1)>();
          sfor<4>([&](auto I) { tie(stv[I]); }); tie(uv[0]); tie(uv[1]);
          float s1 = 0.f, s2 = 0.f;
#pragma unroll
          for (int e = 0; e < 4; ++e) { f32x4 v = __builtin_bit_cast(f32x4, stv[e]); s1 += v[0] + v[2]; s2 += v[1] + v[3]; }
          s1 = swap32_add(s1); s2 = swap32_add(s2);
          const float mean = s1 * (1.f / 640.f);
          rs2 = __builtin_amdgcn_rsqf(fmaxf(s2 * (1.f / 640.f) - mean * mean, 0.f) + 1e-5f); nm2 = -mean * rs2; tie(rs2); tie(nm2);
        }
        if constexpr (q == 3 && K == 4 + 2) {
          f32x4 u = __builtin_bit_cast(f32x4, uv[0]) + __builtin_bit_cast(f32x4, uv[1]);
#pragma unroll
          for (int e = 0; e < 4; ++e) {
            float t = swap32_add(swap16_add(u[e])); tie(t); u[e] = t;
          }
          if (w < 6 && lane < 16) *(LAS f32x4*)(L + L_UVS + rq * 16) = u;
        }
        if constexpr (q % 4 == 3 && K >= 6) epiA(IC<g>{}, IC<K - 6>{});
        if constexpr (g > 0 && q % 4 < 3) epi(IC<g - 1>{}, IC<10 * (q % 4) + K>{});
        SB();
      });
    });
    STAMP(17);
    top(IC<12>{});
    sfor<2>([&](auto K) { vmem(IC<12>{}, K); });
    sfor<30>([&](auto K) { epi(IC<2>{}, K); });
    STAMP(19);
    sfor<12>([&](auto E) {
      constexpr int e = E;
      top(IC<13 + e>{});
      if constexpr (e == 0) sfor<8>([&](auto N) { rdw(IC<120 + N>{}); });
      sfor<10>([&](auto K) {
        constexpr int n = 10 * e + K;
        acc[n % 5] = mfma16(wf[120 + n], a5[n / 5], acc[n % 5]); tie(acc[n % 5]);
        if constexpr (n + 8 < 120) rdw(IC<128 + n>{});
        vmem(IC<13 + e>{}, K);
        SB();
      });
    });
    STAMP(20);
    const unsigned xs = L_BSRC + m * 5120;
    if (kh == 0) { *(LAS f32x4*)(L + xs + l16) = acc[2]; *(LAS f32x4*)(L + xs + 1024 + l16) = acc[3]; }
    else { *(LAS f32x4*)(L + xs + 2048 + l16) = acc[0]; *(LAS f32x4*)(L + xs + 3072 + l16) = acc[1]; *(LAS f32x4*)(L + xs + 4096 + l16) = acc[4]; }
    bar();
    f32x4 r0, r1, r2 = {};
    if (kh == 0) { r0 = acc[0] + ldsf4(L, xs + 2048 + l16); r1 = acc[1] + ldsf4(L, xs + 3072 + l16); r2 = acc[4] + ldsf4(L, xs + 4096 + l16); }
    else { r0 = acc[2] + ldsf4(L, xs + l16); r1 = acc[3] + ldsf4(L, xs + 1024 + l16); }
    const f32x4 g0 = ldsf4(L, L_MODV + 5440 + (32 * kh + 4 * g4) * 4), g1 = ldsf4(L, L_MODV + 5440 + (32 * kh + 16 + 4 * g4) * 4), g2 = ldsf4(L, L_MODV + 5440 + (64 + 4 * g4) * 4);
    GAS char* o = (GAS char*)P.out + (size_t)(16 * tt + i16) * 1280 + (80 * c8 + 4 * g4) * 2;
    *(GAS v2u*)(o + kh * 64) = v2u{pk2(bflo(x2k[0]) + g0[0] * r0[0], bfhi(x2k[0]) + g0[1] * r0[1]), pk2(bflo(x2k[1]) + g0[2] * r0[2], bfhi(x2k[1]) + g0[3] * r0[3])};
    *(GAS v2u*)(o + kh * 64 + 32) = v2u{pk2(bflo(x2k[2]) + g1[0] * r1[0], bfhi(x2k[2]) + g1[1] * r1[1]), pk2(bflo(x2k[3]) + g1[2] * r1[2], bfhi(x2k[3]) + g1[3] * r1[3])};
    if (kh == 0) *(GAS v2u*)(o + 128) = v2u{pk2(bflo(x2k[4]) + g2[0] * r2[0], bfhi(x2k[4]) + g2[1] * r2[1]), pk2(bflo(x2k[5]) + g2[2] * r2[2], bfhi(x2k[5]) + g2[3] * r2[3])};
    if (b == 0 && tid == 0) __hip_atomic_store((GAS unsigned*)(P.ws + CT_E), cbase + 1u, RLX_AGENT);
    STAMP(21);
  }
}

#if defined(__HIP_DEVICE_COMPILE__)
#pragma clang attribute pop
#endif
extern "C" void kernel_launch(void* const* d_in, const int* in_sizes, int n_in, void* d_out, int out_size, void* d_ws, size_t ws_size,
                              hipStream_t stream) {
  Params P;
  P.x = (const u16*)d_in[0]; P.wqkv = (const u16*)d_in[1]; P.wo = (const u16*)d_in[2];
  P.wg = (const u16*)d_in[4]; P.wu = (const u16*)d_in[5]; P.wd = (const u16*)d_in[6];
  P.c = (const u16*)d_in[7]; P.wmod = (const u16*)d_in[8];
  P.out = (u16*)d_out; P.ws = (char*)d_ws;
  fused_block<<<256, 512, 0, stream>>>(P);
}
```

```cpp
#include <hip/hip_runtime.h>
#include <hip/hip_bf16.h>
#include <utility>

typedef unsigned short u16;
typedef unsigned v4u __attribute__((ext_vector_type(4)));
typedef unsigned v2u __attribute__((ext_vector_type(2)));
typedef short v4s __attribute__((ext_vector_type(4)));
typedef float f32x2 __attribute__((ext_vector_type(2)));
typedef float f32x4 __attribute__((ext_vector_type(4)));
typedef float f32x16 __attribute__((ext_vector_type(16)));
typedef __bf16 bf16x2 __attribute__((ext_vector_type(2)));
typedef __bf16 bf16x8 __attribute__((ext_vector_type(8)));
typedef _Float16 f16x2 __attribute__((ext_vector_type(2)));
typedef __amdgpu_buffer_rsrc_t rsrc_t;
#define LAS __attribute__((address_space(3)))
#define GAS __attribute__((address_space(1)))
#define DI __device__ __forceinline__
#define RLX_AGENT __ATOMIC_RELAXED, __HIP_MEMORY_SCOPE_AGENT

#define STAMPS 0

constexpr int WS_CTRL = 0;
constexpr int WS_MODP = 65536;
constexpr int WS_UV   = 131072;
constexpr int WS_ST   = 327680;
constexpr int WS_WT1  = 655360;
constexpr int WS_WT3  = WS_WT1 + 16 * 163840;
constexpr int WS_WT4  = WS_WT3 + 8 * 106496;
constexpr int WS_WT5  = WS_WT4 + 16 * 245760;
constexpr int WS_KV   = WS_WT5 + 8 * 245760;
constexpr int WS_OF   = WS_KV + 64 * 16 * 5120;
constexpr int WS_X2F  = WS_OF + 128 * 20 * 1024;
constexpr int WS_ACT  = WS_X2F + 64 * 40 * 1024;
constexpr int WS_XH   = WS_ACT + 128 * 48 * 1024;
constexpr int WS_END  = WS_XH + 64 * 40 * 1024;
constexpr int WS_DBG  = WS_END;
constexpr int CT_E = 0, CT_FLAG = 4096  , CT_C1 = 16384  , CT_C2 = 49152, CT_C3 = CT_C2 + 2048,
              CT_C4 = CT_C3 + 2048  , CT_CB = CT_C4 + 6144  , CT_CW = CT_CB + 1024, CT_CM = CT_CW + 128;
constexpr int L_WQ = 0, L_RED = 10240;
constexpr int L_BSRC = 122880;
constexpr int L_MODV = 155648;
constexpr int L_CNT = 162816;
constexpr int L_UVS = 155648;
constexpr int KVT = 6144;
constexpr int L_Q = 73728, L_QHI = 81920, L_RED2 = 112640  , L_W3A = 86016;
constexpr unsigned SPIN_CAP = 1u << 20;

#if defined(__HIP_DEVICE_COMPILE__)
#pragma clang attribute push (__attribute__((target("no-packed-fp32-ops"))), apply_to = function)
#endif
struct Params { const u16 *x, *wqkv, *wo, *wg, *wu, *wd, *c, *wmod; u16* out; char* ws; };

DI f32x16 mfma32(v4u a, v4u b, f32x16 c) { return __builtin_amdgcn_mfma_f32_32x32x16_bf16(__builtin_bit_cast(bf16x8, a), __builtin_bit_cast(bf16x8, b), c, 0, 0, 0); }
DI f32x4 mfma16(v4u a, v4u b, f32x4 c) { return __builtin_amdgcn_mfma_f32_16x16x32_bf16(__builtin_bit_cast(bf16x8, a), __builtin_bit_cast(bf16x8, b), c, 0, 0, 0); }
DI unsigned pk2(float a, float b) { f32x2 v = {a, b}; return __builtin_bit_cast(unsigned, __builtin_convertvector(v, bf16x2)); }
DI float bflo(unsigned x) { return __builtin_bit_cast(float, x << 16); }
DI float bfhi(unsigned x) { return __builtin_bit_cast(float, x & 0xffff0000u); }
DI float bfr(float a) { return bflo(pk2(a, a)); }
DI f32x2 swp32(float v) {
  unsigned a = __builtin_bit_cast(unsigned, v), c = a; asm("" : "+v"(c));
  auto r = __builtin_amdgcn_permlane32_swap(a, c, false, false);
  unsigned r0 = r[0], r1 = r[1];
  return f32x2{__builtin_bit_cast(float, r0), __builtin_bit_cast(float, r1)};
}
DI f32x2 swp16(float v) {
  unsigned a = __builtin_bit_cast(unsigned, v), c = a; asm("" : "+v"(c));
  auto r = __builtin_amdgcn_permlane16_swap(a, c, false, false);
  unsigned r0 = r[0], r1 = r[1];
  return f32x2{__builtin_bit_cast(float, r0), __builtin_bit_cast(float, r1)};
}
DI float swap32_add(float v) { f32x2 r = swp32(v); return r.x + r.y; }
DI float swap16_add(float v) { f32x2 r = swp16(v); return r.x + r.y; }
DI float swap32_max(float v) { f32x2 r = swp32(v); return fmaxf(r.x, r.y); }
DI float swap32_hi(float v) { return swp32(v).y; }
DI bool all64(bool c) { return __builtin_amdgcn_ballot_w64(!c) == 0ull; }
DI float ror8_add(float v) { return v + __builtin_bit_cast(float, __builtin_amdgcn_update_dpp(0, __builtin_bit_cast(int, v), 0x128, 0xf, 0xf, true)); }

template <bool SC1> DI void dma(unsigned lds_addr, unsigned voff, rsrc_t rs, unsigned soff) {
  if (SC1) asm volatile("s_mov_b32 m0, %0\n\ts_nop 0\n\tbuffer_load_dwordx4 %1, %2, %3 offen sc1 lds" ::"s"(lds_addr), "v"(voff), "s"(rs), "s"(soff) : "memory");
  else     asm volatile("s_mov_b32 m0, %0\n\ts_nop 0\n\tbuffer_load_dwordx4 %1, %2, %3 offen lds" ::"s"(lds_addr), "v"(voff), "s"(rs), "s"(soff) : "memory");
}
template <int N, int LO, int GO> DI void dman(unsigned lb, unsigned voff, rsrc_t rs, unsigned gb) {
  unsigned t;
#define DMA_HEAD "s_add_u32 m0, %1, %5\n\ts_add_u32 %0, %4, %6\n\tbuffer_load_dwordx4 %2, %3, %0 offen sc1 lds"
#define DMA_MORE(o) "\n\tbuffer_load_dwordx4 %2, %3, %0 offen offset:" #o " sc1 lds"
#define DMA_ARGS : "=&s"(t) : "s"(lb), "v"(voff), "s"(rs), "s"(gb), "n"(LO), "n"(GO) : "memory", "scc"
  if constexpr (N == 1) asm volatile(DMA_HEAD DMA_ARGS);
  if constexpr (N == 2) asm volatile(DMA_HEAD DMA_MORE(1024) DMA_ARGS);
  if constexpr (N == 3) asm volatile(DMA_HEAD DMA_MORE(1024) DMA_MORE(2048) DMA_ARGS);
  if constexpr (N == 4) asm volatile(DMA_HEAD DMA_MORE(1024) DMA_MORE(2048) DMA_MORE(3072) DMA_ARGS);
}
template <int I> DI void ldf(v4u& d, unsigned voff, rsrc_t rs, unsigned base) {
  asm volatile("buffer_load_dwordx4 %0, %1, %2, %3 offen offset:%4 sc1" : "=v"(d) : "v"(voff), "s"(rs), "s"(base + (I / 4) * 4096), "n"((I % 4) * 1024) : "memory");
}
DI void ld16(v4u& d, unsigned voff, rsrc_t rs, unsigned soff) { asm volatile("buffer_load_dwordx4 %0, %1, %2, %3 offen sc1" : "=v"(d) : "v"(voff), "s"(rs), "s"(soff) : "memory"); }
DI void ld16p(v4u& d, unsigned voff, rsrc_t rs, unsigned soff) { asm volatile("buffer_load_dwordx4 %0, %1, %2, %3 offen" : "=v"(d) : "v"(voff), "s"(rs), "s"(soff) : "memory"); }
DI void ld8p(v2u& d, unsigned voff, rsrc_t rs, unsigned soff) { asm volatile("buffer_load_dwordx2 %0, %1, %2, %3 offen" : "=v"(d) : "v"(voff), "s"(rs), "s"(soff) : "memory"); }
DI void ld2p(unsigned& d, unsigned voff, rsrc_t rs, unsigned soff) { asm volatile("buffer_load_ushort %0, %1, %2, %3 offen" : "=v"(d) : "v"(voff), "s"(rs), "s"(soff) : "memory"); }
DI void ld4(unsigned& d, unsigned voff, rsrc_t rs, unsigned soff) { asm volatile("buffer_load_dword %0, %1, %2, %3 offen sc1" : "=v"(d) : "v"(voff), "s"(rs), "s"(soff) : "memory"); }
template <int N> DI void waitv() { asm volatile("s_waitcnt vmcnt(%0)" ::"n"(N) : "memory"); }
template <class T> DI void tie(T& a) { asm volatile("" : "+v"(a)); }
DI void rbar() { asm volatile("s_barrier" ::: "memory"); }
DI void bar() { asm volatile("s_waitcnt lgkmcnt(0)\n\ts_barrier" ::: "memory"); }
DI void st16(v4u v, rsrc_t rs, unsigned off) { __builtin_amdgcn_raw_buffer_store_b128(v, rs, off, 0, 16); }
DI void st8(v2u v, rsrc_t rs, unsigned off) { __builtin_amdgcn_raw_buffer_store_b64(v, rs, off, 0, 16); }
DI v4u trpair(LAS char* L, unsigned a0, unsigned a1) {
  v4s lo = __builtin_amdgcn_ds_read_tr16_b64_v4i16((LAS v4s*)(L + a0));
  v4s hi = __builtin_amdgcn_ds_read_tr16_b64_v4i16((LAS v4s*)(L + a1));
  v2u l = __builtin_bit_cast(v2u, lo), h = __builtin_bit_cast(v2u, hi);
  return v4u{l.x, l.y, h.x, h.y};
}
DI v4u lds16(LAS char* L, unsigned a) { return *(LAS v4u*)(L + a); }
DI f32x4 ldsf4(LAS char* L, unsigned a) { return *(LAS f32x4*)(L + a); }

template <int... I, class F> DI void sfor_impl(std::integer_sequence<int, I...>, F&& f) { (f(std::integral_constant<int, I>{}), ...); }
template <int N, class F> DI void sfor(F&& f) { sfor_impl(std::make_integer_sequence<int, N>{}, f); }
#define SB() __builtin_amdgcn_sched_barrier(0)

template <int N> using IC = std::integral_constant<int, N>;
DI void affine4(unsigned x0, unsigned x1, f32x4 a, f32x4 s, unsigned& o0, unsigned& o1) {
  float t0, t1;
  const float a0 = a[0], a1 = a[1], a2 = a[2], a3 = a[3], s0 = s[0], s1 = s[1], s2 = s[2], s3 = s[3];
  asm volatile("v_fma_mix_f32 %0, %4, %6, %10 op_sel_hi:[1,0,0]\n\tv_fma_mix_f32 %2, %4, %7, %11 op_sel:[1,0,0] op_sel_hi:[1,0,0]\n\t"
      "v_fma_mix_f32 %1, %5, %8, %12 op_sel_hi:[1,0,0]\n\tv_fma_mix_f32 %3, %5, %9, %13 op_sel:[1,0,0] op_sel_hi:[1,0,0]\n\t"
      "v_cvt_pk_bf16_f32 %0, %0, %2\n\tv_cvt_pk_bf16_f32 %1, %1, %3"
      : "=&v"(o0), "=&v"(o1), "=&v"(t0), "=&v"(t1) : "v"(x0), "v"(x1), "v"(a0), "v"(a1), "v"(a2), "v"(a3), "v"(s0), "v"(s1), "v"(s2), "v"(s3));
}
constexpr int ring_off(int pc) { return (pc & 3) * 15360 + ((pc >> 2) % 15) * 1024; }
DI unsigned strm_off(int pc, int rounds) { return ((pc & 3) * rounds + (pc >> 2)) * 1024; }
DI float dot2(unsigned a, unsigned b, float c) { return __builtin_amdgcn_fdot2_f32_bf16(__builtin_bit_cast(bf16x2, a), __builtin_bit_cast(bf16x2, b), c, false); }
DI f32x2 xswp32(float a, float c) { auto r = __builtin_amdgcn_permlane32_swap(__builtin_bit_cast(unsigned, a), __builtin_bit_cast(unsigned, c), false, false); unsigned r0 = r[0], r1 = r[1]; return f32x2{__builtin_bit_cast(float, r0), __builtin_bit_cast(float, r1)}; }
DI f32x2 xswp16(float a, float c) { auto r = __builtin_amdgcn_permlane16_swap(__builtin_bit_cast(unsigned, a), __builtin_bit_cast(unsigned, c), false, false); unsigned r0 = r[0], r1 = r[1]; return f32x2{__builtin_bit_cast(float, r0), __builtin_bit_cast(float, r1)}; }

enum { OP_A, OP_W, OP_ST, OP_UV, OP_STORE, OP_PEEK, OP_A5, OP_MW, OP_MC };
struct Sched {
  int n, tot; int ty[128], ix[128], cn[128], pos[128], start[27], before[27];
  constexpr void put(int t, int i, int c) { ty[n] = t; ix[n] = i; cn[n] = c; pos[n] = tot; tot += c; ++n; }
  constexpr void puts(int t, int i, int c) { for (int k = 0; k < c; ++k) put(t, i + k, 1); }
  constexpr void putw(int r0, int r1) {
    while (r0 < r1) { int c = r1 - r0; if (c > 4) c = 4; if (r0 % 15 + c > 15) c = 15 - r0 % 15; put(OP_W, r0, c); r0 += c; } }
  constexpr void seg(int q) { start[q] = n; before[q] = tot; }
  constexpr int at(int t, int i) const { for (int k = 0; k < n; ++k) if (ty[k] == t && i >= ix[k] && i < ix[k] + cn[k]) return pos[k] + i - ix[k]; return -1; }
};
constexpr int cmax(int a, int b) { return a > b ? a : b; }
constexpr Sched make_sched() {
  Sched s{}; int wr = 12;
  s.putw(0, 12); s.puts(OP_A, 0, 12);
  for (int q = 0; q < 25; ++q) {
    s.seg(q);
    if (q == 10 || q == 14 || q == 18) s.put(OP_PEEK, (q - 10) / 4, 1);
    if (q == 0) s.puts(OP_A, 12, 8);
    if (q == 2) { s.puts(OP_ST, 0, 4); s.puts(OP_UV, 0, 2); }
    if (q == 11) s.puts(OP_A5, 0, 8);
    if (q == 15 || q == 16) s.puts(OP_A5, 8 + 4 * (q - 15), 4);
    if (q == 19 || q == 20) s.puts(OP_A5, 16 + 4 * (q - 19), 4);
    int lim = (10 * (q <= 12 ? q : q - 1) + 60) / 4; if (lim > 60) lim = 60; if (lim > wr + 4) lim = wr + 4; if (q < 2) lim = wr;
    s.putw(wr, lim); wr = lim;
    if (q == 6 || q == 10 || q == 12) s.put(OP_STORE, q == 6 ? 0 : q == 10 ? 1 : 2, 1);
  }
  s.seg(25);
  return s;
}
constexpr Sched SC = make_sched();
constexpr int sc_wait(int q) {
  int p = -1;
  if (q <= 11) p = SC.at(OP_W, (10 * (q + 1) + (q == 11 ? 0 : q % 4 == 2 ? 10 : 6) + 3) / 4 - 1);
  if (q >= 13) p = cmax(SC.at(OP_W, (10 * q + (q < 24 ? 10 : 0) + 3) / 4 - 1), SC.at(OP_A5, 2 * (q - 13) + 1));
  if (q < 4) p = cmax(p, SC.at(OP_A, 5 * q + 4));
  if (q == 8 || q == 12 || q == 15) p = cmax(p, SC.at(OP_STORE, q == 8 ? 0 : q == 12 ? 1 : 2));
  if (q == 11 || q == 15 || q == 19) p = cmax(p, SC.at(OP_PEEK, (q - 11) / 4));
  return SC.before[q] - 1 - p;
}
constexpr Sched make_sched1() {
  Sched s{}; int a = 5, r = 7;
  s.putw(0, 4); s.puts(OP_A, 0, 3); s.putw(4, 7); s.puts(OP_A, 3, 2);
  for (int q = 0; q < 14; ++q) {
    s.seg(q);
    int na = q < 5 ? 3 : 0, nw = q < 4 ? 2 : q < 9 ? 1 : 0;
    s.puts(OP_A, a, na); a += na; s.putw(r, r + nw); r += nw;
  }
  s.seg(14);
  return s;
}
constexpr Sched S1 = make_sched1();
constexpr int s1_first(int q) { return q <= 10 ? 6 * q : q == 11 ? 64 : q == 12 ? 70 : q == 13 ? 75 : 80; }
constexpr int s1_wait(int q) {
  int pc = s1_first(q + 1) + 6; if (pc > 80 || q == 11) pc = 80;
  int p = S1.at(OP_W, (pc + 3) / 4 - 1);
  if (q < 10) p = cmax(p, S1.at(OP_A, q < 9 ? 2 * q + 2 : 19));
  return S1.before[q] - 1 - p;
}

#define STAMP(i)

DI void poll(const GAS unsigned* addr, unsigned base, unsigned want, bool active) {
#pragma nounroll
  for (unsigned s = 0; s < SPIN_CAP; ++s) {
    unsigned v = __hip_atomic_load(addr, RLX_AGENT);
    if (all64(!active || (v - base) >= want)) break;
    __builtin_amdgcn_s_sleep(2);
  }
  __builtin_amdgcn_fence(__ATOMIC_ACQUIRE, "wavefront");
}
DI unsigned lds_arrive(LAS char* L, int off, int lane) {
  unsigned o = 0;
  asm volatile("s_waitcnt lgkmcnt(0)" ::: "memory");
  if (lane == 0) o = __hip_atomic_fetch_add((LAS unsigned*)(L + off), 1u, __ATOMIC_RELAXED, __HIP_MEMORY_SCOPE_WORKGROUP);
  o = __builtin_amdgcn_readfirstlane(o);
  asm volatile("" ::: "memory");
  return o;
}
template <int N> DI void mod_rows(const v4u* wm, const unsigned* cv, LAS char* red, int lane) {
  float acc[8] = {0, 0, 0, 0, 0, 0, 0, 0};
#pragma unroll
  for (int i = 0; i < N; ++i) {
    float cf = bflo(cv[i]);
    float sv = bfr(cf * __builtin_amdgcn_rcpf(1.f + __builtin_amdgcn_exp2f(-1.44269504f * cf)));
#pragma unroll
    for (int e = 0; e < 4; ++e) { acc[2 * e] += sv * bflo(wm[i][e]); acc[2 * e + 1] += sv * bfhi(wm[i][e]); }
  }
  float u[4], v[2];
#pragma unroll
  for (int e = 0; e < 4; ++e) { f32x2 r = xswp32(acc[e], acc[e + 4]); u[e] = r.x + r.y; }
#pragma unroll
  for (int e = 0; e < 2; ++e) { f32x2 r = xswp16(u[e], u[e + 2]); v[e] = ror8_add(r.x + r.y); }
  if (!(lane & 8)) *(LAS f32x2*)(red + (lane & 7) * 32 + (lane >> 4) * 8) = f32x2{v[0], v[1]};
}
DI void lds_wait(LAS char* L, int off, unsigned want) {
#pragma nounroll
  for (unsigned s = 0; s < SPIN_CAP && *(volatile LAS unsigned*)(L + off) < want; ++s) __builtin_amdgcn_s_sleep(1);
  asm volatile("" ::: "memory");
}

__global__ __launch_bounds__(512, 2) void fused_block(Params P) {
  __shared__ __attribute__((aligned(16))) char lds_raw[163840];
  LAS char* L = (LAS char*)lds_raw;
  const unsigned LB = (unsigned)(size_t)L;
  const int tid = threadIdx.x, lane = tid & 63;
  const int w = __builtin_amdgcn_readfirstlane(tid >> 6);
  const int b = blockIdx.x;
  const int p = 4 * ((b & 7) >> 1) + ((b >> 3) & 3), j = 8 * (b & 1) + (b >> 5);
  const int m = w & 3, kh = w >> 2, h = lane >> 5, r32 = lane & 31;
  const int g4 = lane >> 4, i16 = lane & 15, q4 = (lane & 15) >> 2, p4 = lane & 3, blk = (lane >> 4) & 1;
  const rsrc_t rws = __builtin_amdgcn_make_buffer_rsrc((void*)P.ws, 0, WS_END, 0x00020000);
  const rsrc_t rx = __builtin_amdgcn_make_buffer_rsrc((void*)P.x, 0, 2048 * 1280, 0x00020000);
  const unsigned l16 = lane * 16;
  const unsigned ringL = kh * 61440 + l16, ringD = LB + kh * 61440 + m * 15360;
  STAMP(0);
  unsigned long long pc0; asm volatile("s_getpc_b64 %0" : "=s"(pc0));
  unsigned cbase = 0;
  const GAS unsigned* caddr = (const GAS unsigned*)P.ws;
  const int c40 = b % 40, c20 = (b - 160) % 20;
  const int cc = b < 160 ? (c40 < 20 ? c40 : c40 + 10) : (c20 < 10 ? 20 + c20 : 40 + c20), kq = b < 160 ? b / 40 : ((b - 160) / 20) & 3;
  const char* wm0 = (const char*)P.wmod + (size_t)(160 * kq) * 7680 + cc * 128;
  if (w <= 5) {
    const int bj = b < 240 ? b : 0, s = bj / 6, q = bj % 6;
    const int lr = (lane * 205) >> 11, lc = lane - 10 * lr;
    const bool lo = w < 4;
    v4u t[8]; unsigned cv[5];
    {
      const char* px = (const char*)P.x + (size_t)b * 10240 + (w & 1) * 640;
      const rsrc_t r1 = __builtin_amdgcn_make_buffer_rsrc((void*)(lo ? wm0 : px), 0, lo && b >= 160 ? 0 : 0x200000, 0x00020000);
      const rsrc_t r2 = __builtin_amdgcn_make_buffer_rsrc((void*)(lo ? (const char*)P.wqkv + (size_t)(16 * s) * 3840 + q * 640 + w * 160 : px), 0, lo ? 0x10000 : 0, 0x00020000);
      const rsrc_t rc = __builtin_amdgcn_make_buffer_rsrc((void*)(P.c + 160 * kq), 0, lo && b < 160 ? 320 : 0, 0x00020000);
      const unsigned vx = (lane & 7) * 1280 + (lane >> 3) * 16;
      const unsigned v1 = lo ? (tid >> 3) * 7680 + (tid & 7) * 16 : vx, st1 = lo ? 245760 : 128;
      const unsigned v2 = (lr < 6 ? lr : 5) * 3840 + lc * 16;
      sfor<5>([&](auto I) { ld16p(t[I], v1, r1, I * st1); });
      ld16p(t[5], v2, r2, 0); ld16p(t[6], v2, r2, 23040); ld16p(t[7], lr >= 4 ? v2 - 7680 : v2, r2, 46080);
      sfor<5>([&](auto I) { ld2p(cv[I], (tid >> 3) * 2, rc, I * 64); });
    }
    if (w == 0) {
      int off = CT_E;
      if (lane == 1) off = CT_C1 + (((p >= 1 ? p - 1 : 0) * 16 + j) << 7);
      if (lane == 2) off = CT_C1 + (((p >= 2 ? p - 2 : 0) * 16 + j) << 7);
      if (lane == 3) off = CT_C2 + (p << 7);
      if (lane == 4) off = CT_C3 + (p << 7);
      if (lane == 5) off = CT_C4 + p * 384;
      if (lane == 16 || lane == 17) off = CT_C4 + p * 384 + (lane - 15) * 128;
      if (lane == 6) off = CT_CW;
      if (lane == 7) off = CT_CM;
      if (lane >= 8 && lane < 16) off = CT_CB + ((lane - 8) << 7);
      caddr = (const GAS unsigned*)(P.ws + off);
    }
    asm volatile("global_load_dword %0, %1, off sc1" : "=v"(cbase) : "v"(caddr) : "memory");
    if (tid < 2) *(LAS v4u*)(L + L_CNT + tid * 16) = v4u{0u, 0u, 0u, 0u};
    bar();
    waitv<0>();
    sfor<8>([&](auto I) { tie(t[I]); });
    sfor<5>([&](auto I) { tie(cv[I]); });
    tie(cbase);
    if (tid == 0) *(volatile LAS unsigned*)(L + L_CNT + 12) = 1u;
    if (lo) {
      const unsigned wql = L_WQ + w * 2560;
      if (lr < 6) { *(LAS v4u*)(L + wql + l16) = t[5]; *(LAS v4u*)(L + wql + 960 + l16) = t[6]; }
      if (lr < 4) *(LAS v4u*)(L + wql + 1920 + l16) = t[7];
      if (b < 160) {
        mod_rows<5>(t, cv, L + L_RED + w * 256, lane);
        if (lds_arrive(L, L_CNT, lane) == 3 && lane < 16) {
          f32x4 u = (ldsf4(L, L_RED + l16) + ldsf4(L, L_RED + 256 + l16)) + (ldsf4(L, L_RED + 512 + l16) + ldsf4(L, L_RED + 768 + l16));
          st16(__builtin_bit_cast(v4u, u), rws, WS_MODP + (kq * 3840 + cc * 64 + lane * 4) * 4);
        }
      }
      if (b < 240) {
        asm volatile("s_waitcnt lgkmcnt(0)" ::: "memory");
        const int part = q >> 1, jb = 8 * (q & 1) + 2 * w;
        const int sg = s >= 20 ? s - 20 : s, pk0 = 3 * (sg & ~1);
        const unsigned dst0 = WS_WT1 + jb * 163840 + (s >= 20 ? 81920 : 0);
        const unsigned dmain = dst0 + strm_off(part == 0 ? 60 + sg : pk0 + 2 * (sg & 1) + part - 1, 20), dmix = dst0 + strm_off(pk0 + 4 + (sg & 1), 20);
        const unsigned rowa = wql + (8 * h + q4) * 160;
        const unsigned a = rowa + (20 * blk + 4 * p4) * 2, a2 = rowa + (40 * blk + 16 + 20 * (p4 & 1)) * 2;
        v4u f0 = trpair(L, a, a + 640), f1 = trpair(L, a + 80, a + 720), f2 = trpair(L, a2, a2 + 640);
        st16(f0, rws, dmain + l16);
        st16(f1, rws, dmain + 163840 + l16);
        if (i16 >= 8) f2 = v4u{0, 0, 0, 0};
        const int r = (part == 0 ? 16 : part == 1 ? 0 : 8) + i16;
        if (part == 0 || i16 < 8) st16(f2, rws, dmix + blk * 163840 + (h * 32 + r) * 16);
      }
    } else {
      float s1 = 0.f, s2 = 0.f, s3 = 0.f, s4 = 0.f;
#pragma unroll
      for (int i = 0; i < 5; ++i) {
        s1 = dot2(t[i][0], 0x3f803f80u, s1); s2 = dot2(t[i][0], t[i][0], s2); s3 = dot2(t[i][1], 0x3f803f80u, s3); s4 = dot2(t[i][1], t[i][1], s4);
        s1 = dot2(t[i][2], 0x3f803f80u, s1); s2 = dot2(t[i][2], t[i][2], s2); s3 = dot2(t[i][3], 0x3f803f80u, s3); s4 = dot2(t[i][3], t[i][3], s4);
      }
      s1 = swap32_add(swap16_add(ror8_add(s1 + s3))); s2 = swap32_add(swap16_add(ror8_add(s2 + s4)));
      if (lane < 8) *(LAS f32x2*)(L + L_RED + 1024 + (w & 1) * 64 + lane * 8) = f32x2{s1, s2};
      lds_arrive(L, L_CNT + 8, lane);
      lds_wait(L, L_CNT + 8, 2u);
      const f32x2 o2 = *(LAS f32x2*)(L + L_RED + 1024 + ((w & 1) ^ 1) * 64 + (lane & 7) * 8);
      const float mean = (s1 + o2.x) * (1.f / 640.f);
      const float rstd = __builtin_amdgcn_rsqf(fmaxf((s2 + o2.y) * (1.f / 640.f) - mean * mean, 0.f) + 1e-5f), nm = -mean * rstd;
      const int tok = 8 * b + (lane & 7);
      const unsigned xo = WS_XH + (tok >> 5) * 40960 + (w & 1) * 20480 + (lane >> 3) * 512 + (tok & 31) * 16;
#pragma unroll
      for (int i = 0; i < 5; ++i) {
        v4u o;
#pragma unroll
        for (int e = 0; e < 4; ++e) { f32x2 v = {fmaf(bflo(t[i][e]), rstd, nm), fmaf(bfhi(t[i][e]), rstd, nm)}; o[e] = __builtin_bit_cast(unsigned, __builtin_convertvector(v, f16x2)); }
        st16(o, rws, xo + i * 4096);
      }
    }
    waitv<0>();
    if (lds_arrive(L, L_CNT + 4, lane) == 5 && lane < 8) __hip_atomic_store((GAS unsigned*)(P.ws + CT_FLAG + lane * 1024 + b * 4), __builtin_amdgcn_readfirstlane(cbase) + 1u, RLX_AGENT);
    STAMP(1);
  } else {
    bar();
    if (b >= 224) {
      const int v = w - 6, kr = (b - 224) >> 3, cw = b & 7;
      const rsrc_t ro = __builtin_amdgcn_make_buffer_rsrc((void*)((const char*)P.wo + (size_t)(160 * kr) * 1280 + cw * 160), 0, 0x00100000, 0x00020000);
      int row = (v * 64 + lane) / 10, cl = (v * 64 + lane) % 10;
      lds_wait(L, L_CNT + 12, 1u);
      for (int i = v; i < 25; i += 2) {
        dma<false>(LB + L_BSRC + i * 1024, row * 1280 + cl * 16, ro, 0);
        row += 12; cl += 8; if (cl >= 10) { cl -= 10; ++row; }
      }
      waitv<0>();
      lds_arrive(L, L_CNT + 16, lane);
      lds_wait(L, L_CNT + 16, 2u);
      for (int u = v; u < 25; u += 2) {
        const int sl = u / 5, t = u % 5, c = 4 * sl + g4, jl = c / 5, ii = c % 5;
        const int r0 = ii < 4 ? 40 * jl + 20 * (ii >> 1) + 4 * (ii & 1) : 40 * jl + 16;
        const int r1 = ii < 4 ? r0 + 8 : r0 + 20;
        const unsigned ca = L_BSRC + q4 * 160 + (16 * t + 4 * p4) * 2;
        v4u f = trpair(L, ca + r0 * 160, ca + r1 * 160);
        const int s = 5 * kr + sl;
        st16(f, rws, WS_WT3 + cw * 106496 + (s & 1) * 53248 + strm_off((s >> 1) * 5 + t, 13) + l16);
      }
      waitv<0>();
    }
  }
  const int c8 = 4 * (b & 1) + ((b >> 5) & 3), rh = b >> 7;
  int col = tid < 320 ? 16 * tid : tid < 340 ? (2560 + 80 * ((b >> 3) & 7) + 4 * (tid - 320)) * 4 : tid < 360 ? (1920 + 80 * ((b >> 3) & 7) + 4 * (tid - 340)) * 4 : 0;
  unsigned mdst = L_MODV + (tid < 160 ? tid * 32 + 16 : tid < 320 ? (tid - 160) * 32 : 5760 + (tid - 320) * 16);
  const rsrc_t rb = __builtin_amdgcn_make_buffer_rsrc((void*)(b < 128 ? (const char*)((b >> 6) ? P.wu : P.wg) + (size_t)(80 * ((b >> 3) & 7)) * 3072 + (b & 7) * 384
                                                                       : (const char*)P.wd + (size_t)(32 * (((b - 128) >> 1) % 48)) * 1280 + (b & 1) * 640), 0, b < 224 ? 0x00400000 : 0, 0x00020000);
  unsigned bo[4];
#pragma unroll
  for (int i = 0; i < 4; ++i) {
    int c = (i * 8 + w) * 64 + lane; if (c >= (b < 128 ? 1920 : 1280)) c = 0;
    bo[i] = b < 128 ? (c / 24) * 3072 + (c % 24) * 16 : (c / 40) * 1280 + (c % 40) * 16; tie(bo[i]);
  }
  tie(col); tie(mdst);
  if (w == 4) {
    const unsigned tag = __builtin_amdgcn_readfirstlane(cbase) + 1u;
#pragma nounroll
    for (unsigned s = 0; s < SPIN_CAP; ++s) {
      v4u f; ld16(f, l16, rws, CT_FLAG + (b & 7) * 1024); waitv<0>(); tie(f);
      if (all64(f.x == tag && f.y == tag && f.z == tag && f.w == tag)) break;
      __builtin_amdgcn_s_sleep(1);
    }
    __builtin_amdgcn_fence(__ATOMIC_ACQUIRE, "wavefront");
  }
  bar();
  STAMP(4);

  auto mod_put = [&](v4u* mp, bool isc, bool on, unsigned dst) {
    sfor<4>([&](auto I) { tie(mp[I]); });
    f32x4 v = (__builtin_bit_cast(f32x4, mp[0]) + __builtin_bit_cast(f32x4, mp[1])) + (__builtin_bit_cast(f32x4, mp[2]) + __builtin_bit_cast(f32x4, mp[3]));
#pragma unroll
    for (int e = 0; e < 4; ++e) { float t = bfr(v[e]); v[e] = isc ? bfr(1.f + t) : t; }
    if (on) *(LAS f32x4*)(L + dst) = v;
  };
  const bool mod2 = b >= 160 && b < 240;
  v4u m2w[3]; unsigned m2c[3];
  {
    v4u mp[4], xf[20];
    sfor<4>([&](auto I) { ld16(mp[I], col, rws, WS_MODP + I * 15360); });
    const unsigned w1 = WS_WT1 + j * 163840 + kh * 81920 + m * 20480;
    const unsigned ab = WS_XH + ((4 * p + m) * 40 + 20 * kh) * 1024;
    rbar();
    dman<4, 0, 0>(ringD, l16, rws, w1);
    sfor<3>([&](auto I) { ldf<I>(xf[I], l16, rws, ab); });
    if (tid == 384 && b >= 224) __hip_atomic_fetch_add((GAS unsigned*)(P.ws + CT_CW), 1u, RLX_AGENT);
    waitv<7>();
    mod_put(mp, tid >= 160 && tid < 340, tid < 360, mdst);
    dman<3, 4096, 4096>(ringD, l16, rws, w1);
    ldf<3>(xf[3], l16, rws, ab); ldf<4>(xf[4], l16, rws, ab);
    const rsrc_t rm = __builtin_amdgcn_make_buffer_rsrc((void*)wm0, 0, mod2 ? 160 * 7680 : 0, 0x00020000);
    const rsrc_t rc = __builtin_amdgcn_make_buffer_rsrc((void*)(P.c + 160 * kq), 0, mod2 ? 320 : 0, 0x00020000);
    STAMP(5);
    f32x16 acc[4] = {};
    const unsigned wbK = ringL + kh * 15360, wbS = ringL + 15360 - kh * 15360;
    unsigned cb = L_MODV + (80 * kh + 2 * h) * 32; tie(cb);
    unsigned xw = kh * 61440 + m * 15360 + 5120 + l16, xr = (kh ^ 1) * 61440 + m * 15360 + 5120 + l16;
    v4u wf[80], hf[20], c0, c1, c2; f32x4 ca[40], cs[40], eo[6];
    const unsigned kvg = WS_KV + ((4 * p + m) * 16 + j) * 5120, kvl = (8 + m) * KVT;
    auto rdw = [&](auto N) {
      constexpr int n = N < 60 ? 3 * (N / 3) + 2 - N % 3 : (N ^ 1), i = n / 3, pk0 = 3 * (i & ~1);
      if constexpr (n >= 60) wf[n] = lds16(L, ringL + ring_off(n));
      else if constexpr (n % 3 == 1) wf[n] = lds16(L, ringL + ring_off(pk0 + 4 + (i & 1)));
      else wf[n] = lds16(L, (n % 3 == 0 ? wbS : wbK) + ring_off(pk0 + 2 * (i & 1)));
    };
    auto rdc = [&](auto R) { ca[R] = ldsf4(L, cb + (R / 2) * 128 + (R & 1) * 32); cs[R] = ldsf4(L, cb + (R / 2) * 128 + (R & 1) * 32 + 16); };
    auto aff = [&](auto I, auto E) { constexpr int e = 2 * E; unsigned o0, o1; affine4(xf[I][e], xf[I][e + 1], ca[2 * I + E], cs[2 * I + E], o0, o1); hf[I][e] = o0; hf[I][e + 1] = o1; };
    auto quad = [&](const f32x16& a, int q) { return f32x4{a[4 * q], a[4 * q + 1], a[4 * q + 2], a[4 * q + 3]}; };
    sfor<14>([&](auto Q) {
      constexpr int q = Q, n0 = s1_first(q);
      waitv<(q == 13 ? 10 : q == 12 ? 63 : s1_wait(q))>();
      if constexpr (q == 0) tie(xf[0]);
      if constexpr (q < 10) { tie(xf[2 * q + 1]); if constexpr (q < 9) tie(xf[2 * q + 2]); }
      if constexpr (q == 0 || q == 11) bar(); else rbar();
      if constexpr (q == 13) { if (tid == 0) __hip_atomic_fetch_add((GAS unsigned*)(P.ws + CT_C1 + ((p * 16 + j) << 7)), 1u, RLX_AGENT); }
      if constexpr (q == 0) { sfor<6>(rdc); sfor<6>(rdw); aff(IC<0>{}, IC<0>{}); aff(IC<0>{}, IC<1>{}); asm volatile("s_nop 1"); SB(); }
      sfor<s1_first(q + 1) - n0>([&](auto K) {
        constexpr int n = n0 + K, i = n < 60 ? n / 3 : n - 60, t = n < 60 ? n % 3 : 3, k = S1.start[q] + K;
        acc[t] = mfma32(wf[n], hf[i], acc[t]); tie(acc[t]);
        SB();
        if constexpr (t < 2 && i + 1 < 20) aff(IC<i + 1>{}, IC<t>{});
        if constexpr (t < 2 && i + 3 < 20) rdc(IC<2 * (i + 3) + t>{});
        if constexpr (n + 6 < 80) rdw(IC<n + 6>{});
        if constexpr (k < S1.start[q + 1]) {
          constexpr int ty = S1.ty[k], ix = S1.ix[k];
          if constexpr (ty == OP_A) ldf<ix>(xf[ix], l16, rws, ab);
          else if constexpr (ty == OP_W) dman<S1.cn[k], (ix % 15) * 1024, ix * 1024>(ringD, l16, rws, w1);
        }
        if constexpr (n >= 59 && n < 63) *(LAS f32x4*)(L + xw + (n - 59) * 1024) = quad(acc[0], n - 59);
        if constexpr (n == 63) { if (kh) { *(LAS f32x4*)(L + xw + 4096) = quad(acc[1], 0); *(LAS f32x4*)(L + xw + 5120) = quad(acc[1], 1); } }
        if constexpr (n == 64) sfor<6>([&](auto I) { eo[I] = ldsf4(L, xr + I * 1024); });
        if constexpr (n == 65 || n == 66) {
          constexpr int o = 8 * (n - 65);
          v4u c = {pk2(acc[2][o] + eo[o / 4][0], acc[2][o + 1] + eo[o / 4][1]), pk2(acc[2][o + 2] + eo[o / 4][2], acc[2][o + 3] + eo[o / 4][3]),
                   pk2(acc[2][o + 4] + eo[o / 4 + 1][0], acc[2][o + 5] + eo[o / 4 + 1][1]), pk2(acc[2][o + 6] + eo[o / 4 + 1][2], acc[2][o + 7] + eo[o / 4 + 1][3])};
          st16(c, rws, kvg + (kh ? 3072 + r32 * 32 + h * 16 : l16) + (n - 65) * 1024);
          if constexpr (n == 65) c0 = c; else c1 = c;
        }
        if constexpr (n == 67) {
          c2 = v4u{pk2(acc[1][0] + eo[4][0], acc[1][1] + eo[4][1]), pk2(acc[1][2] + eo[4][2], acc[1][3] + eo[4][3]),
                   pk2(acc[1][4] + eo[5][0], acc[1][5] + eo[5][1]), pk2(acc[1][6] + eo[5][2], acc[1][7] + eo[5][3])};
          if (!kh) st16(c2, rws, kvg + 2048 + l16);
        }
        if constexpr (n >= 68 && n < 72) dma<false>(LB + L_BSRC + ((n - 68) * 8 + w) * 1024, bo[n - 68], rb, 0);
        if constexpr (n >= 72 && n < 75) { ld16p(m2w[n - 72], (tid >> 3) * 7680 + (tid & 7) * 16, rm, (n - 72) * 491520); ld2p(m2c[n - 72], (tid >> 3) * 2, rc, (n - 72) * 128); }
        SB();
      });
    });
    STAMP(6);
    const unsigned xq = 5120 + (m >> 1) * 15360 + (m & 1) * 5120 + l16;
    if (!kh) {
#pragma unroll
      for (int qd = 0; qd < 4; ++qd) *(LAS f32x4*)(L + xq + qd * 1024) = quad(acc[3], qd);
      *(LAS f32x4*)(L + xq + 4096) = quad(acc[1], 2);
    }
    bar();
    {
      const unsigned lo = kh ? 3072 + r32 * 48 + h * 16 : l16, ls = kh ? 1536 : 1024;
      *(LAS v4u*)(L + kvl + lo) = c0; *(LAS v4u*)(L + kvl + lo + ls) = c1;
    }
    if (kh == 0) {
      *(LAS v4u*)(L + kvl + 2048 + l16) = v4u{c2.x, c2.y, 0u, 0u};
      *(LAS v4u*)(L + kvl + 3072 + h * 1536 + r32 * 48 + 32) = v4u{c2.z, c2.w, 0x3f80u, 0u};
    } else {
      const float qs = 0.22360679775f * 1.44269504089f;
      float qv[20];
#pragma unroll
      for (int qd = 0; qd < 5; ++qd) {
        const f32x4 u = ldsf4(L, xq + qd * 1024);
#pragma unroll
        for (int e = 0; e < 4; ++e) qv[4 * qd + e] = ((qd < 4 ? acc[3][4 * qd + e] : acc[1][8 + e]) + u[e]) * qs;
      }
      *(LAS v4u*)(L + L_Q + m * 2048 + l16) = v4u{pk2(qv[0], qv[1]), pk2(qv[2], qv[3]), pk2(qv[4], qv[5]), pk2(qv[6], qv[7])};
      *(LAS v4u*)(L + L_Q + m * 2048 + 1024 + l16) = v4u{pk2(qv[8], qv[9]), pk2(qv[10], qv[11]), pk2(qv[12], qv[13]), pk2(qv[14], qv[15])};
      *(LAS v2u*)(L + L_QHI + m * 512 + lane * 8) = v2u{pk2(qv[16], qv[17]), pk2(qv[18], qv[19])};
    }
    STAMP(7);
  }
  if (w == 0) poll(caddr, cbase, lane == 6 ? 32u : 1u, (lane == 1 && p >= 1) || (lane == 2 && p >= 2) || lane == 6);
  waitv<0>();
  if (mod2) {
    sfor<3>([&](auto I) { tie(m2w[I]); tie(m2c[I]); });
    mod_rows<3>(m2w, m2c, L + L_RED2 + w * 256, lane);
  }
  bar();
  STAMP(8);
  v4u kv[5];
  const int gt = 4 * p - 8 + w;
  {
    const unsigned ko = gt >= 0 ? WS_KV + (gt * 16 + j) * 5120 : 0x7ff00000u;
    sfor<5>([&](auto I) { ldf<I>(kv[I], l16, rws, ko); });
  }
  if (mod2 && w == 7 && lane < 16) {
    f32x4 u = ldsf4(L, L_RED2 + l16);
#pragma unroll
    for (int i = 1; i < 8; ++i) u += ldsf4(L, L_RED2 + i * 256 + l16);
    st16(__builtin_bit_cast(v4u, u), rws, WS_MODP + (kq * 3840 + cc * 64 + lane * 4) * 4);
  }
  if (b < 128) {
    if (w < 6) {
      const int mat = b >> 6, kp = (b >> 3) & 7, cg = b & 7;
      const int tl = 2 * w + blk;
      const int jd = 2 * cg + tl / 6, t = tl % 6;
      float us = 0.f, vs = 0.f;
#pragma unroll
      for (int sg = 0; sg < 5; ++sg) {
        const int k0 = sg < 4 ? 32 * (sg >> 1) + 4 * (2 * (sg & 1) + h) : 64 + 8 * h;
        const int k1 = sg < 4 ? k0 + 16 : k0 + 4;
        const unsigned ca = L_BSRC + q4 * 384 + (16 * tl + 4 * p4) * 2;
        v4u f = trpair(L, ca + k0 * 384, ca + k1 * 384);
        f32x4 a0 = ldsf4(L, L_MODV + 5760 + k0 * 4), a1 = ldsf4(L, L_MODV + 5760 + k1 * 4);
        f32x4 b0 = ldsf4(L, L_MODV + 6080 + k0 * 4), b1 = ldsf4(L, L_MODV + 6080 + k1 * 4);
        v4u o;
        o[0] = pk2(bflo(f[0]) * a0[0], bfhi(f[0]) * a0[1]); o[1] = pk2(bflo(f[1]) * a0[2], bfhi(f[1]) * a0[3]);
        o[2] = pk2(bflo(f[2]) * a1[0], bfhi(f[2]) * a1[1]); o[3] = pk2(bflo(f[3]) * a1[2], bfhi(f[3]) * a1[3]);
        vs += bflo(f[0]) * b0[0] + bfhi(f[0]) * b0[1] + bflo(f[1]) * b0[2] + bfhi(f[1]) * b0[3] + bflo(f[2]) * b1[0] + bfhi(f[2]) * b1[1] + bflo(f[3]) * b1[2] + bfhi(f[3]) * b1[3];
        us = dot2(o[0], 0x3f803f80u, us); us = dot2(o[1], 0x3f803f80u, us); us = dot2(o[2], 0x3f803f80u, us); us = dot2(o[3], 0x3f803f80u, us);
        const int s = 5 * kp + sg;
        st16(o, rws, WS_WT4 + jd * 245760 + (s / 20) * 122880 + strm_off((t >> 1) * 40 + (s % 20) * 2 + (t & 1), 30) + (h * 32 + mat * 16 + i16) * 16);
      }
      us = swap32_add(us); vs = swap32_add(vs);
      const int row = (jd * 6 + t) * 32 + mat * 16 + i16;
      __builtin_amdgcn_raw_buffer_store_b32(__builtin_bit_cast(unsigned, h ? vs : us), rws, WS_UV + ((kp * 2 + h) * 3072 + row) * 4, 0, 16);
    }
  }
  if (b >= 128 && b < 224) {
    const int st0 = (b - 128) >> 1, st = 16 * (st0 % 3) + st0 / 3, ch = b & 1;
    for (int tt = w; tt < 20; tt += 8) {
      const unsigned a = L_BSRC + (16 * (g4 >> 1) + 4 * (g4 & 1) + q4) * 640 + (16 * tt + 4 * p4) * 2;
      v4u f = trpair(L, a, a + 8 * 640);
      st16(f, rws, WS_WT5 + (4 * ch + tt / 5) * 245760 + (st & 1) * 122880 + strm_off((st >> 1) * 5 + tt % 5, 30) + l16);
    }
  }
  STAMP(9);
  const unsigned w3 = WS_WT3 + c8 * 106496 + kh * 53248 + m * 13312;
  unsigned cbv;
  {
    waitv<0>();
    sfor<5>([&](auto I) { tie(kv[I]); });
    {
      const unsigned tb = w * KVT, vo = 3072 + (lane >> 1) * 48 + (lane & 1) * 16;
      *(LAS v4u*)(L + tb + l16) = kv[0]; *(LAS v4u*)(L + tb + 1024 + l16) = kv[1];
      *(LAS v4u*)(L + tb + 2048 + l16) = v4u{kv[2].x, kv[2].y, gt >= 0 ? 0u : 0xfe96u, 0u};
      *(LAS v4u*)(L + tb + 3072 + h * 1536 + r32 * 48 + 32) = v4u{kv[2].z, kv[2].w, 0x3f80u, 0u};
      *(LAS v4u*)(L + tb + vo) = kv[3]; *(LAS v4u*)(L + tb + vo + 1536) = kv[4];
    }
    bar();
    if (tid == 0 && mod2) __hip_atomic_fetch_add((GAS unsigned*)(P.ws + CT_CM), 1u, RLX_AGENT);
    if (tid == 64 && b < 224) __hip_atomic_fetch_add((GAS unsigned*)(P.ws + CT_CB + ((b & 7) << 7)), 1u, RLX_AGENT);
    STAMP(10);
    const int hd = kh;
    const v4u q0 = lds16(L, L_Q + m * 2048 + hd * 1024 + l16);
    v2u qh = *(LAS v2u*)(L + L_QHI + m * 512 + lane * 8);
    if (h != hd) qh = v2u{0, 0};
    const v4u q1 = {qh.x, qh.y, 0x3f80u, 0};
    const f32x16 zero = {};
    const unsigned ka = m * KVT + hd * 1024 + l16, kb = m * KVT + 2048 + l16;
    v4u k0[9], k1[9];
    sfor<9>([&](auto I) { constexpr int i = I == 0 ? 0 : I == 1 ? 8 : I - 1; k0[i] = lds16(L, ka + i * KVT); k1[i] = lds16(L, kb + i * KVT); });
    dman<4, L_W3A, 0>(LB + kh * 32768 + m * 8192, l16, rws, w3);
    dman<4, L_W3A + 4096, 4096>(LB + kh * 32768 + m * 8192, l16, rws, w3);
    auto qk = [&](auto I, f32x16 c) { f32x16 r = mfma32(k1[I], q1, mfma32(k0[I], q0, c)); tie(r); return r; };
    auto vmax = [](float a, float c) { return __builtin_elementwise_maximum(a, c); };
    auto tmax = [&](const f32x16& v, float t) {
#pragma unroll
      for (int r = 0; r < 16; r += 2) t = vmax(vmax(v[r], v[r + 1]), t);
      tie(t); return t; };
    f32x16 Sm, S[8];
    float mx = -3.0e38f;
    {
      const f32x16 s0 = qk(IC<0>{}, zero), s8 = qk(IC<8>{}, zero);
      S[1] = qk(IC<1>{}, zero); SB();
#pragma unroll
      for (int r = 0; r < 16; ++r) Sm[r] = (r & 3) + 8 * (r >> 2) + 4 * h <= r32 ? s8[r] : s0[r];
      tie(Sm); SB();
    }
    sfor<7>([&](auto I) {
      if constexpr (I < 6) S[I + 2] = qk(IC<I + 2>{}, zero);
      SB();
      if constexpr (I == 0) mx = tmax(Sm, mx); else mx = tmax(S[I], mx);
      SB();
    });
    const unsigned va = m * KVT + 3072 + hd * 1536 + (4 * h + q4) * 48 + (blk == 0 ? (p4 & 1) * 16 + (p4 >> 1) * 8 : (p4 ? 40 : 32));
    v4u vf[18];
    auto rdv = [&](auto I) { vf[2 * I + 1] = trpair(L, va + I * KVT + 768, va + I * KVT + 1152); vf[2 * I] = trpair(L, va + I * KVT, va + I * KVT + 384); };
    rdv(IC<8>{}); rdv(IC<0>{}); SB();
    mx = tmax(S[7], mx);
    { f32x2 t = swp32(mx); mx = vmax(t.x, t.y); }
    f32x16 negm;
#pragma unroll
    for (int r = 0; r < 16; ++r) negm[r] = -mx;
    tie(negm);
    f32x16 O = {};
    S[1] = qk(IC<1>{}, negm); rdv(IC<1>{}); SB();
#pragma unroll
    for (int s = 0; s < 2; ++s) {
      v4u p0, p8;
#pragma unroll
      for (int e = 0; e < 4; ++e) {
        const int r = 8 * s + 2 * e, key = (r & 3) + 8 * (r >> 2) + 4 * h;
        const float a = __builtin_amdgcn_exp2f(Sm[r] - mx), c = __builtin_amdgcn_exp2f(Sm[r + 1] - mx);
        const float a8 = key <= r32 ? a : 0.f, c8_ = key + 1 <= r32 ? c : 0.f;
        p8[e] = pk2(a8, c8_); p0[e] = pk2(a - a8, c - c8_);
      }
      O = mfma32(vf[s], p0, O); O = mfma32(vf[16 + s], p8, O); tie(O); SB();
    }
    sfor<7>([&](auto I) {
      constexpr int i = I + 1;
      if constexpr (i < 7) { S[i + 1] = qk(IC<i + 1>{}, negm); rdv(IC<i + 1>{}); SB(); }
      v4u pf[2];
#pragma unroll
      for (int e = 0; e < 8; ++e) pf[e / 4][e % 4] = pk2(__builtin_amdgcn_exp2f(S[i][2 * e]), __builtin_amdgcn_exp2f(S[i][2 * e + 1]));
      O = mfma32(vf[2 * i], pf[0], O); O = mfma32(vf[2 * i + 1], pf[1], O); tie(O); SB();
    });
    const float inv = __builtin_amdgcn_rcpf(swap32_hi(O[8]));
    const int tok = 128 * p + 32 * m + r32;
    const unsigned ob = WS_OF + (tok >> 4) * 20480 + (tok & 15) * 16;
    const int c = 5 * j + 2 * hd + h, c2 = 5 * j + 4;
    STAMP(11);
    st16(v4u{pk2(O[0] * inv, O[1] * inv), pk2(O[2] * inv, O[3] * inv), pk2(O[4] * inv, O[5] * inv), pk2(O[6] * inv, O[7] * inv)}, rws, ob + (((c >> 2) & 1) * 10 + (c >> 3)) * 1024 + (c & 3) * 256);
    if (h == 0) st8(v2u{pk2(O[8] * inv, O[9] * inv), pk2(O[10] * inv, O[11] * inv)}, rws, ob + (((c2 >> 2) & 1) * 10 + (c2 >> 3)) * 1024 + (c2 & 3) * 256 + hd * 8);
  }
  waitv<0>();
  bar();
  const int tt = 8 * p + 4 * rh + m;
  unsigned x2k[6];
  {
    v2u xr[3];
    const unsigned xo = (16 * tt + i16) * 1280 + (80 * c8 + 4 * g4) * 2;
    if (tid == 0) __hip_atomic_fetch_add((GAS unsigned*)(P.ws + CT_C2 + (p << 7)), 1u, RLX_AGENT);
    STAMP(12);
    v4u mp[4];
    ld8p(xr[0], xo, rx, kh * 64); ld8p(xr[1], xo, rx, kh * 64 + 32); ld8p(xr[2], xo, rx, 128);
    dman<4, 0, 8192>(LB + kh * 20480 + m * 5120, l16, rws, w3); dman<1, 4096, 12288>(LB + kh * 20480 + m * 5120, l16, rws, w3);
    if (w == 0) poll(caddr, cbase, lane == 7 ? 80u : 16u, lane == 3 || lane == 7);
    bar();
    STAMP(13);
    v4u af[10], wf[50];
    const unsigned ab = WS_OF + (tt * 20 + kh * 10) * 1024;
    sfor<10>([&](auto I) { ldf<I>(af[I], l16, rws, ab); });
    sfor<4>([&](auto I) { ld16(mp[I], ((lane < 20 ? 1280 : 3200 - 80) + 80 * c8 + 4 * (lane < 40 ? lane : 20)) * 4, rws, WS_MODP + I * 15360); });
    f32x4 acc[5] = {};
    unsigned wa = L_W3A + kh * 32768 + l16; tie(wa);
    const unsigned wb = kh * 20480 + l16;
    auto rdw = [&](auto N) {
      constexpr int n = 5 * (N / 5) + 4 - N % 5;
      if constexpr (n < 32) wf[n] = lds16(L, wa + (n & 3) * 8192 + (n >> 2) * 1024); else wf[n] = lds16(L, wb + (n & 3) * 5120 + ((n >> 2) - 8) * 1024);
    };
    sfor<10>([&](auto E) {
      constexpr int e = E;
      if constexpr (!(e & 1)) { waitv<12 - e>(); tie(af[e]); tie(af[e + 1]); }
      if constexpr (e == 0) { rbar(); sfor<8>(rdw); }
      sfor<5>([&](auto K) {
        constexpr int n = 5 * e + K;
        acc[K] = mfma16(wf[n], af[e], acc[K]); tie(acc[K]);
        if constexpr (n + 8 < 50) rdw(IC<n + 8>{});
        SB();
      });
    });
    STAMP(14);
    waitv<0>();
    mod_put(mp, false, w == 5 && lane < 40, L_MODV + 5120 + l16);
    asm volatile("global_load_dword %0, %1, off sc1" : "=v"(cbv) : "v"(caddr) : "memory");
    const unsigned xs = 40960 + m * 5120;
    if (kh == 0) { *(LAS f32x4*)(L + xs + l16) = acc[2]; *(LAS f32x4*)(L + xs + 1024 + l16) = acc[3]; }
    else { *(LAS f32x4*)(L + xs + 2048 + l16) = acc[0]; *(LAS f32x4*)(L + xs + 3072 + l16) = acc[1]; *(LAS f32x4*)(L + xs + 4096 + l16) = acc[4]; }
    bar();
    tie(xr[0]); tie(xr[1]); tie(xr[2]);
    f32x4 r0, r1, r2 = {};
    if (kh == 0) { r0 = acc[0] + ldsf4(L, xs + 2048 + l16); r1 = acc[1] + ldsf4(L, xs + 3072 + l16); r2 = acc[4] + ldsf4(L, xs + 4096 + l16); }
    else { r0 = acc[2] + ldsf4(L, xs + l16); r1 = acc[3] + ldsf4(L, xs + 1024 + l16); }
    const f32x4 ga0 = ldsf4(L, L_MODV + 5120 + (32 * kh + 4 * g4) * 4), ga1 = ldsf4(L, L_MODV + 5120 + (32 * kh + 16 + 4 * g4) * 4), ga2 = ldsf4(L, L_MODV + 5120 + (64 + 4 * g4) * 4);
    x2k[0] = pk2(bflo(xr[0].x) + ga0[0] * r0[0], bfhi(xr[0].x) + ga0[1] * r0[1]); x2k[1] = pk2(bflo(xr[0].y) + ga0[2] * r0[2], bfhi(xr[0].y) + ga0[3] * r0[3]);
    x2k[2] = pk2(bflo(xr[1].x) + ga1[0] * r1[0], bfhi(xr[1].x) + ga1[1] * r1[1]); x2k[3] = pk2(bflo(xr[1].y) + ga1[2] * r1[2], bfhi(xr[1].y) + ga1[3] * r1[3]);
    x2k[4] = pk2(bflo(xr[2].x) + ga2[0] * r2[0], bfhi(xr[2].x) + ga2[1] * r2[1]); x2k[5] = pk2(bflo(xr[2].y) + ga2[2] * r2[2], bfhi(xr[2].y) + ga2[3] * r2[3]);
    if (kh) { x2k[4] = 0; x2k[5] = 0; }
    const unsigned xb = WS_X2F + ((tt >> 1) * 40 + 5 * c8) * 1024 + ((tt & 1) * 16 + i16) * 16;
    st16(v4u{x2k[0], x2k[1], x2k[2], x2k[3]}, rws, xb + (2 * kh + (g4 >> 1)) * 1024 + (g4 & 1) * 512);
    if (kh == 0) st8(v2u{x2k[4], x2k[5]}, rws, xb + 4096 + (g4 >> 1) * 512 + (g4 & 1) * 8);
    float s1 = 0.f, s2 = 0.f;
#pragma unroll
    for (int e = 0; e < 6; ++e) { s1 = dot2(x2k[e], 0x3f803f80u, s1); s2 = dot2(x2k[e], x2k[e], s2); }
    s1 = swap32_add(swap16_add(s1)); s2 = swap32_add(swap16_add(s2));
    if (g4 == 0) st8(v2u{__builtin_bit_cast(unsigned, s1), __builtin_bit_cast(unsigned, s2)}, rws, WS_ST + ((tt >> 1) * 8 + c8) * 512 + ((tt & 1) * 16 + i16) * 16 + kh * 8);
  }
  waitv<0>();
  tie(cbv);
  if (w == 0 && !all64(lane < 8 || lane >= 16 || cbv - cbase >= 28u)) poll(caddr, cbase, 28u, lane >= 8 && lane < 16);
  bar();
  {
    const unsigned w4 = WS_WT4 + j * 245760 + kh * 122880 + m * 30720, w5 = WS_WT5 + c8 * 245760 + kh * 122880 + m * 30720 - 30720;
    if (tid == 0) __hip_atomic_fetch_add((GAS unsigned*)(P.ws + CT_C3 + (p << 7)), 1u, RLX_AGENT);
    STAMP(15);
    dman<4, 0, 0>(ringD, l16, rws, w4); dman<4, 4096, 4096>(ringD, l16, rws, w4); dman<4, 8192, 8192>(ringD, l16, rws, w4);
    if (w == 0) poll(caddr, cbase, 16u, lane == 4);
    bar();
    STAMP(16);
    v4u af[20], a5[24], stv[4], uv[2], wf[240]; unsigned pk;
    const unsigned ab = WS_X2F + ((4 * p + m) * 40 + 20 * kh) * 1024, ab5 = WS_ACT + (tt * 48 + 24 * kh) * 1024;
    sfor<12>([&](auto I) { ldf<I>(af[I], l16, rws, ab); });
    const int tok = 128 * p + 32 * m + r32;
    const unsigned ao = WS_ACT + ((tok >> 4) * 48 + (j & 1) * 24 + (j >> 1)) * 1024 + kh * 512 + h * 256 + (tok & 15) * 16;
    const int rq = (w < 6 ? w : 0) * 16 + i16;
    const unsigned uo = (g4 * 4 + rq / 48) * 12288 + (j * 192 + 4 * (rq % 48)) * 4;
    f32x16 aK[3] = {}, aS[3] = {};
    f32x4 acc[5] = {};
    const unsigned wbK = ringL + kh * 15360, wbS = ringL + 15360 - kh * 15360;
    unsigned xw = L_BSRC + w * 4096 + l16, xr_ = L_BSRC + (w ^ 4) * 4096 + l16, ub = L_UVS + kh * 128 + h * 16; tie(xw); tie(xr_); tie(ub);
    float rs2, nm2, av[8]; f32x4 eo[4], eu[4], ev[4];
    constexpr auto isS = [](int n) { return (n / 10) % 4 == 3 ? n % 10 < 5 : (n & 1) != 0; };
    constexpr auto stp = [](int n) { return (n / 10) % 4 == 3 ? (n / 10) * 5 + n % 5 : n / 2; };
    auto rdw = [&](auto N) {
      constexpr int n = N < 120 ? (N ^ 1) : 5 * (N / 5) + 4 - N % 5;
      if constexpr (n < 120) wf[n] = lds16(L, (isS(n) ? wbS : wbK) + ring_off(2 * stp(n))); else wf[n] = lds16(L, ringL + ring_off(n));
    };
    auto vmem = [&](auto Q, auto K) {
      constexpr int k = SC.start[Q] + K;
      if constexpr (k < SC.start[Q + 1]) {
        constexpr int ty = SC.ty[k], ix = SC.ix[k];
        if constexpr (ty == OP_A) ldf<ix>(af[ix], l16, rws, ab);
        else if constexpr (ty == OP_W) dman<SC.cn[k], (ix % 15) * 1024, ix * 1024>(ringD, l16, rws, ix < 30 ? w4 : w5);
        else if constexpr (ty == OP_ST) ld16(stv[ix], r32 * 16 + h * 2048 + ix * 512, rws, WS_ST + (4 * p + m) * 4096);
        else if constexpr (ty == OP_UV) ld16(uv[ix], uo, rws, WS_UV + ix * 24576);
        else if constexpr (ty == OP_PEEK) asm volatile("global_load_dword %0, %1, off sc1" : "=v"(pk) : "v"(caddr) : "memory");
        else if constexpr (ty == OP_A5) ldf<ix>(a5[ix], l16, rws, ab5);
      }
    };
    auto epiA = [&](auto G, auto QD) { const f32x16 sd = aS[G]; *(LAS f32x4*)(L + xw + QD * 1024) = f32x4{sd[4 * QD], sd[4 * QD + 1], sd[4 * QD + 2], sd[4 * QD + 3]}; };
    auto epi = [&](auto G, auto K) {
      if constexpr (K < 2) sfor<2>([&](auto I) { constexpr int qd = 2 * K + I; eo[qd] = ldsf4(L, xr_ + qd * 1024); eu[qd] = ldsf4(L, ub + G * 256 + qd * 32); ev[qd] = ldsf4(L, ub + 768 + G * 256 + qd * 32); });
      else if constexpr (K < 18) { constexpr int r = K - 2; float t = fmaf(aK[G][r] + eo[r / 4][r % 4], rs2, fmaf(nm2, eu[r / 4][r % 4], ev[r / 4][r % 4])); tie(t); aK[G][r] = t; }
      else if constexpr (K < 26) { constexpr int k = K - 18; const float gv = aK[G][k]; float t = gv * __builtin_amdgcn_rcpf(1.f + __builtin_amdgcn_exp2f(-1.44269504f * gv)) * aK[G][8 + k]; tie(t); av[k] = t; }
      else if constexpr (K == 29) st16(v4u{pk2(av[0], av[1]), pk2(av[2], av[3]), pk2(av[4], av[5]), pk2(av[6], av[7])}, rws, ao + G * 8192);
    };
    auto top = [&](auto Q) {
      constexpr int q = Q;
      waitv<sc_wait(q)>();
      if constexpr (q < 4) sfor<5>([&](auto I) { tie(af[5 * q + I]); });
      if constexpr (q >= 13) { tie(a5[2 * (q - 13)]); tie(a5[2 * (q - 13) + 1]); }
      if constexpr (q == 11 || q == 15 || q == 19) {
        constexpr int ln = q == 11 ? 5 : q == 15 ? 16 : 17;
        tie(pk);
        if (w == 0 && !all64(lane != ln || pk - cbase >= 16u)) poll(caddr, cbase, 16u, lane == ln);
      }
      if constexpr (q == 4 || q == 8 || q == 12) bar(); else rbar();
      if constexpr (q == 8 || q == 12 || q == 15) { if (tid == 64) __hip_atomic_fetch_add((GAS unsigned*)(P.ws + CT_C4 + p * 384 + (q == 8 ? 0 : q == 12 ? 128 : 256)), 1u, RLX_AGENT); }
    };
    sfor<12>([&](auto Q) {
      constexpr int q = Q, g = q / 4;
      top(Q);
      if constexpr (q == 0) sfor<6>(rdw);
      sfor<10>([&](auto K) {
        constexpr int n = 10 * q + K, i = stp(n) % 20;
        if constexpr (isS(n)) { aS[g] = mfma32(wf[n], af[i], aS[g]); tie(aS[g]); } else { aK[g] = mfma32(wf[n], af[i], aK[g]); tie(aK[g]); }
        if constexpr (n + 6 < 120) rdw(IC<n + 6>{});
        vmem(Q, K);
        if constexpr (q == 3 && K == 5) {
          waitv<SC.before[4] - 1 - SC.at(OP_UV, 1)>();
          sfor<4>([&](auto I) { tie(stv[I]); }); tie(uv[0]); tie(uv[1]);
          float s1 = 0.f, s2 = 0.f;
#pragma unroll
          for (int e = 0; e < 4; ++e) { f32x4 v = __builtin_bit_cast(f32x4, stv[e]); s1 += v[0] + v[2]; s2 += v[1] + v[3]; }
          s1 = swap32_add(s1); s2 = swap32_add(s2);
          const float mean = s1 * (1.f / 640.f);
          rs2 = __builtin_amdgcn_rsqf(fmaxf(s2 * (1.f / 640.f) - mean * mean, 0.f) + 1e-5f); nm2 = -mean * rs2; tie(rs2); tie(nm2);
        }
        if constexpr (q == 3 && K == 4 + 2) {
          f32x4 u = __builtin_bit_cast(f32x4, uv[0]) + __builtin_bit_cast(f32x4, uv[1]);
#pragma unroll
          for (int e = 0; e < 4; ++e) {
            float t = swap32_add(swap16_add(u[e])); tie(t); u[e] = t;
          }
          if (w < 6 && lane < 16) *(LAS f32x4*)(L + L_UVS + rq * 16) = u;
        }
        if constexpr (q % 4 == 3 && K >= 6) epiA(IC<g>{}, IC<K - 6>{});
        if constexpr (g > 0 && q % 4 < 3) epi(IC<g - 1>{}, IC<10 * (q % 4) + K>{});
        SB();
      });
    });
    STAMP(17);
    top(IC<12>{});
    sfor<2>([&](auto K) { vmem(IC<12>{}, K); });
    sfor<30>([&](auto K) { epi(IC<2>{}, K); });
    STAMP(19);
    sfor<12>([&](auto E) {
      constexpr int e = E;
      top(IC<13 + e>{});
      if constexpr (e == 0) sfor<8>([&](auto N) { rdw(IC<120 + N>{}); });
      sfor<10>([&](auto K) {
        constexpr int n = 10 * e + K;
        acc[n % 5] = mfma16(wf[120 + n], a5[n / 5], acc[n % 5]); tie(acc[n % 5]);
        if constexpr (n + 8 < 120) rdw(IC<128 + n>{});
        vmem(IC<13 + e>{}, K);
        SB();
      });
    });
    STAMP(20);
    const unsigned xs = L_BSRC + m * 5120;
    if (kh == 0) { *(LAS f32x4*)(L + xs + l16) = acc[2]; *(LAS f32x4*)(L + xs + 1024 + l16) = acc[3]; }
    else { *(LAS f32x4*)(L + xs + 2048 + l16) = acc[0]; *(LAS f32x4*)(L + xs + 3072 + l16) = acc[1]; *(LAS f32x4*)(L + xs + 4096 + l16) = acc[4]; }
    bar();
    f32x4 r0, r1, r2 = {};
    if (kh == 0) { r0 = acc[0] + ldsf4(L, xs + 2048 + l16); r1 = acc[1] + ldsf4(L, xs + 3072 + l16); r2 = acc[4] + ldsf4(L, xs + 4096 + l16); }
    else { r0 = acc[2] + ldsf4(L, xs + l16); r1 = acc[3] + ldsf4(L, xs + 1024 + l16); }
    const f32x4 g0 = ldsf4(L, L_MODV + 5440 + (32 * kh + 4 * g4) * 4), g1 = ldsf4(L, L_MODV + 5440 + (32 * kh + 16 + 4 * g4) * 4), g2 = ldsf4(L, L_MODV + 5440 + (64 + 4 * g4) * 4);
    GAS char* o = (GAS char*)P.out + (size_t)(16 * tt + i16) * 1280 + (80 * c8 + 4 * g4) * 2;
    *(GAS v2u*)(o + kh * 64) = v2u{pk2(bflo(x2k[0]) + g0[0] * r0[0], bfhi(x2k[0]) + g0[1] * r0[1]), pk2(bflo(x2k[1]) + g0[2] * r0[2], bfhi(x2k[1]) + g0[3] * r0[3])};
    *(GAS v2u*)(o + kh * 64 + 32) = v2u{pk2(bflo(x2k[2]) + g1[0] * r1[0], bfhi(x2k[2]) + g1[1] * r1[1]), pk2(bflo(x2k[3]) + g1[2] * r1[2], bfhi(x2k[3]) + g1[3] * r1[3])};
    if (kh == 0) *(GAS v2u*)(o + 128) = v2u{pk2(bflo(x2k[4]) + g2[0] * r2[0], bfhi(x2k[4]) + g2[1] * r2[1]), pk2(bflo(x2k[5]) + g2[2] * r2[2], bfhi(x2k[5]) + g2[3] * r2[3])};
    if (b == 0 && tid == 0) __hip_atomic_store((GAS unsigned*)(P.ws + CT_E), cbase + 1u, RLX_AGENT);
    STAMP(21);
  }
}

#if defined(__HIP_DEVICE_COMPILE__)
#pragma clang attribute pop
#endif
extern "C" void kernel_launch(void* const* d_in, const int* in_sizes, int n_in, void* d_out, int out_size, void* d_ws, size_t ws_size,
                              hipStream_t stream) {
  Params P;
  P.x = (const u16*)d_in[0]; P.wqkv = (const u16*)d_in[1]; P.wo = (const u16*)d_in[2];
  P.wg = (const u16*)d_in[4]; P.wu = (const u16*)d_in[5]; P.wd = (const u16*)d_in[6];
  P.c = (const u16*)d_in[7]; P.wmod = (const u16*)d_in[8];
  P.out = (u16*)d_out; P.ws = (char*)d_ws;
  fused_block<<<256, 512, 0, stream>>>(P);
}
```

```cpp
#include <hip/hip_runtime.h>
#include <hip/hip_bf16.h>
#include <utility>

typedef unsigned short u16;
typedef unsigned v4u __attribute__((ext_vector_type(4)));
typedef unsigned v2u __attribute__((ext_vector_type(2)));
typedef short v4s __attribute__((ext_vector_type(4)));
typedef float f32x2 __attribute__((ext_vector_type(2)));
typedef float f32x4 __attribute__((ext_vector_type(4)));
typedef float f32x16 __attribute__((ext_vector_type(16)));
typedef __bf16 bf16x2 __attribute__((ext_vector_type(2)));
typedef __bf16 bf16x8 __attribute__((ext_vector_type(8)));
typedef _Float16 f16x2 __attribute__((ext_vector_type(2)));
typedef __amdgpu_buffer_rsrc_t rsrc_t;
#define LAS __attribute__((address_space(3)))
#define GAS __attribute__((address_space(1)))
#define DI __device__ __forceinline__
#define RLX_AGENT __ATOMIC_RELAXED, __HIP_MEMORY_SCOPE_AGENT

#define DELAY0 20
#define STAMPS 0

constexpr int WS_CTRL = 0;
constexpr int WS_MODP = 65536;
constexpr int WS_ST   = 131072;
constexpr int WS_UV   = 393216;
constexpr int WS_WT1  = 786432;
constexpr int WS_WT3  = WS_WT1 + 16 * 163840;
constexpr int WS_WT4  = WS_WT3 + 8 * 106496;
constexpr int WS_WT5  = WS_WT4 + 16 * 245760;
constexpr int WS_KV   = WS_WT5 + 8 * 245760;
constexpr int WS_OF   = WS_KV + 64 * 16 * 6144;
constexpr int WS_X2F  = WS_OF + 128 * 20 * 1024;
constexpr int WS_ACT  = WS_X2F + 64 * 40 * 1024;
constexpr int WS_XH   = WS_ACT + 128 * 48 * 1024;
constexpr int WS_END  = WS_XH + 64 * 40 * 1024;
constexpr int WS_DBG  = WS_END;
constexpr int CT_E = 0, CT_FLAG = 4096  , CT_C1 = 16384  , CT_C2 = 49152, CT_C3 = CT_C2 + 2048,
              CT_C4 = CT_C3 + 2048  , CT_CB = CT_C4 + 6144  , CT_CD = CT_CB + 1024  , CT_CM = CT_CD + 1024, CT_GO = 12288  ;
constexpr int L_WQ = 0, L_RED = 10240;
constexpr int L_BSRC = 122880;
constexpr int L_MODV = 155648;
constexpr int L_CNT = 162816;
constexpr int L_UVS = 155648;
constexpr int KVT = 6144;
constexpr int L_Q = 73728, L_QHI = 81920, L_RED2 = 112640  , L_W3A = 86016, L_GSRC = 122880  ;
constexpr unsigned SPIN_CAP = 1u << 20;

#if defined(__HIP_DEVICE_COMPILE__)
#pragma clang attribute push (__attribute__((target("no-packed-fp32-ops"))), apply_to = function)
#endif
struct Params { const u16 *x, *wqkv, *wo, *wg, *wu, *wd, *c, *wmod; u16* out; char* ws; };

DI f32x16 mfma32(v4u a, v4u b, f32x16 c) { return __builtin_amdgcn_mfma_f32_32x32x16_bf16(__builtin_bit_cast(bf16x8, a), __builtin_bit_cast(bf16x8, b), c, 0, 0, 0); }
DI f32x4 mfma16(v4u a, v4u b, f32x4 c) { return __builtin_amdgcn_mfma_f32_16x16x32_bf16(__builtin_bit_cast(bf16x8, a), __builtin_bit_cast(bf16x8, b), c, 0, 0, 0); }
DI unsigned pk2(float a, float b) { f32x2 v = {a, b}; return __builtin_bit_cast(unsigned, __builtin_convertvector(v, bf16x2)); }
DI float bflo(unsigned x) { return __builtin_bit_cast(float, x << 16); }
DI float bfhi(unsigned x) { return __builtin_bit_cast(float, x & 0xffff0000u); }
DI float bfr(float a) { return bflo(pk2(a, a)); }
DI f32x2 swp32(float v) {
  unsigned a = __builtin_bit_cast(unsigned, v), c = a; asm("" : "+v"(c));
  auto r = __builtin_amdgcn_permlane32_swap(a, c, false, false);
  unsigned r0 = r[0], r1 = r[1];
  return f32x2{__builtin_bit_cast(float, r0), __builtin_bit_cast(float, r1)};
}
DI f32x2 swp16(float v) {
  unsigned a = __builtin_bit_cast(unsigned, v), c = a; asm("" : "+v"(c));
  auto r = __builtin_amdgcn_permlane16_swap(a, c, false, false);
  unsigned r0 = r[0], r1 = r[1];
  return f32x2{__builtin_bit_cast(float, r0), __builtin_bit_cast(float, r1)};
}
DI float swap32_add(float v) { f32x2 r = swp32(v); return r.x + r.y; }
DI float swap16_add(float v) { f32x2 r = swp16(v); return r.x + r.y; }
DI float swap32_max(float v) { f32x2 r = swp32(v); return fmaxf(r.x, r.y); }
DI float swap32_hi(float v) { return swp32(v).y; }
DI bool all64(bool c) { return __builtin_amdgcn_ballot_w64(!c) == 0ull; }
DI float ror8_add(float v) { return v + __builtin_bit_cast(float, __builtin_amdgcn_update_dpp(0, __builtin_bit_cast(int, v), 0x128, 0xf, 0xf, true)); }

template <bool SC1> DI void dma(unsigned lds_addr, unsigned voff, rsrc_t rs, unsigned soff) {
  if (SC1) asm volatile("s_mov_b32 m0, %0\n\ts_nop 0\n\tbuffer_load_dwordx4 %1, %2, %3 offen sc1 lds" ::"s"(lds_addr), "v"(voff), "s"(rs), "s"(soff) : "memory");
  else     asm volatile("s_mov_b32 m0, %0\n\ts_nop 0\n\tbuffer_load_dwordx4 %1, %2, %3 offen lds" ::"s"(lds_addr), "v"(voff), "s"(rs), "s"(soff) : "memory");
}
template <int N, int LO, int GO> DI void dman(unsigned lb, unsigned voff, rsrc_t rs, unsigned gb) {
  unsigned t;
#define DMA_HEAD "s_add_u32 m0, %1, %5\n\ts_add_u32 %0, %4, %6\n\tbuffer_load_dwordx4 %2, %3, %0 offen sc1 lds"
#define DMA_MORE(o) "\n\tbuffer_load_dwordx4 %2, %3, %0 offen offset:" #o " sc1 lds"
#define DMA_ARGS : "=&s"(t) : "s"(lb), "v"(voff), "s"(rs), "s"(gb), "n"(LO), "n"(GO) : "memory", "scc"
  if constexpr (N == 1) asm volatile(DMA_HEAD DMA_ARGS);
  if constexpr (N == 2) asm volatile(DMA_HEAD DMA_MORE(1024) DMA_ARGS);
  if constexpr (N == 3) asm volatile(DMA_HEAD DMA_MORE(1024) DMA_MORE(2048) DMA_ARGS);
  if constexpr (N == 4) asm volatile(DMA_HEAD DMA_MORE(1024) DMA_MORE(2048) DMA_MORE(3072) DMA_ARGS);
}
template <int I> DI void ldf(v4u& d, unsigned voff, rsrc_t rs, unsigned base) {
  asm volatile("buffer_load_dwordx4 %0, %1, %2, %3 offen offset:%4 sc1" : "=v"(d) : "v"(voff), "s"(rs), "s"(base + (I / 4) * 4096), "n"((I % 4) * 1024) : "memory");
}
template <int O> DI void ldh(v2u& d, unsigned voff, rsrc_t rs, unsigned base) {
  asm volatile("buffer_load_dwordx2 %0, %1, %2, %3 offen offset:%4 sc1" : "=v"(d) : "v"(voff), "s"(rs), "s"(base), "n"(O) : "memory");
}
DI void peek(unsigned& d, const GAS unsigned* a) { asm volatile("global_load_dword %0, %1, off sc1" : "=v"(d) : "v"(a) : "memory"); }
DI void ld16(v4u& d, unsigned voff, rsrc_t rs, unsigned soff) { asm volatile("buffer_load_dwordx4 %0, %1, %2, %3 offen sc1" : "=v"(d) : "v"(voff), "s"(rs), "s"(soff) : "memory"); }
DI void ld16p(v4u& d, unsigned voff, rsrc_t rs, unsigned soff) { asm volatile("buffer_load_dwordx4 %0, %1, %2, %3 offen" : "=v"(d) : "v"(voff), "s"(rs), "s"(soff) : "memory"); }
DI void ld8p(v2u& d, unsigned voff, rsrc_t rs, unsigned soff) { asm volatile("buffer_load_dwordx2 %0, %1, %2, %3 offen" : "=v"(d) : "v"(voff), "s"(rs), "s"(soff) : "memory"); }
DI void ld2p(unsigned& d, unsigned voff, rsrc_t rs, unsigned soff) { asm volatile("buffer_load_ushort %0, %1, %2, %3 offen" : "=v"(d) : "v"(voff), "s"(rs), "s"(soff) : "memory"); }
DI void ld4(unsigned& d, unsigned voff, rsrc_t rs, unsigned soff) { asm volatile("buffer_load_dword %0, %1, %2, %3 offen sc1" : "=v"(d) : "v"(voff), "s"(rs), "s"(soff) : "memory"); }
template <int N> DI void waitv() { asm volatile("s_waitcnt vmcnt(%0)" ::"n"(N) : "memory"); }
template <class T> DI void tie(T& a) { asm volatile("" : "+v"(a)); }
DI void rbar() { asm volatile("s_barrier" ::: "memory"); }
DI void bar() { asm volatile("s_waitcnt lgkmcnt(0)\n\ts_barrier" ::: "memory"); }
DI void st16(v4u v, rsrc_t rs, unsigned off) { __builtin_amdgcn_raw_buffer_store_b128(v, rs, off, 0, 16); }
DI void st8(v2u v, rsrc_t rs, unsigned off) { __builtin_amdgcn_raw_buffer_store_b64(v, rs, off, 0, 16); }
DI v4u trpair(LAS char* L, unsigned a0, unsigned a1) {
  v4s lo = __builtin_amdgcn_ds_read_tr16_b64_v4i16((LAS v4s*)(L + a0));
  v4s hi = __builtin_amdgcn_ds_read_tr16_b64_v4i16((LAS v4s*)(L + a1));
  v2u l = __builtin_bit_cast(v2u, lo), h = __builtin_bit_cast(v2u, hi);
  return v4u{l.x, l.y, h.x, h.y};
}
DI v4u lds16(LAS char* L, unsigned a) { return *(LAS v4u*)(L + a); }
DI f32x4 ldsf4(LAS char* L, unsigned a) { return *(LAS f32x4*)(L + a); }

template <int... I, class F> DI void sfor_impl(std::integer_sequence<int, I...>, F&& f) { (f(std::integral_constant<int, I>{}), ...); }
template <int N, class F> DI void sfor(F&& f) { sfor_impl(std::make_integer_sequence<int, N>{}, f); }
#define SB() __builtin_amdgcn_sched_barrier(0)

template <int N> using IC = std::integral_constant<int, N>;
DI void affine4(unsigned x0, unsigned x1, f32x4 a, f32x4 s, unsigned& o0, unsigned& o1) {
  float t0, t1;
  const float a0 = a[0], a1 = a[1], a2 = a[2], a3 = a[3], s0 = s[0], s1 = s[1], s2 = s[2], s3 = s[3];
  asm volatile("v_fma_mix_f32 %0, %4, %6, %10 op_sel_hi:[1,0,0]\n\tv_fma_mix_f32 %2, %4, %7, %11 op_sel:[1,0,0] op_sel_hi:[1,0,0]\n\t"
      "v_fma_mix_f32 %1, %5, %8, %12 op_sel_hi:[1,0,0]\n\tv_fma_mix_f32 %3, %5, %9, %13 op_sel:[1,0,0] op_sel_hi:[1,0,0]\n\t"
      "v_cvt_pk_bf16_f32 %0, %0, %2\n\tv_cvt_pk_bf16_f32 %1, %1, %3"
      : "=&v"(o0), "=&v"(o1), "=&v"(t0), "=&v"(t1) : "v"(x0), "v"(x1), "v"(a0), "v"(a1), "v"(a2), "v"(a3), "v"(s0), "v"(s1), "v"(s2), "v"(s3));
}
constexpr int ring_off(int pc) { return (pc & 3) * 15360 + ((pc >> 2) % 15) * 1024; }
DI unsigned strm_off(int pc, int rounds) { return ((pc & 3) * rounds + (pc >> 2)) * 1024; }
DI float dot2(unsigned a, unsigned b, float c) { return __builtin_amdgcn_fdot2_f32_bf16(__builtin_bit_cast(bf16x2, a), __builtin_bit_cast(bf16x2, b), c, false); }
DI f32x2 xswp32(float a, float c) { auto r = __builtin_amdgcn_permlane32_swap(__builtin_bit_cast(unsigned, a), __builtin_bit_cast(unsigned, c), false, false); unsigned r0 = r[0], r1 = r[1]; return f32x2{__builtin_bit_cast(float, r0), __builtin_bit_cast(float, r1)}; }
DI f32x2 xswp16(float a, float c) { auto r = __builtin_amdgcn_permlane16_swap(__builtin_bit_cast(unsigned, a), __builtin_bit_cast(unsigned, c), false, false); unsigned r0 = r[0], r1 = r[1]; return f32x2{__builtin_bit_cast(float, r0), __builtin_bit_cast(float, r1)}; }

enum { OP_A, OP_W, OP_ST, OP_UV, OP_STORE, OP_PEEK, OP_A5, OP_MW, OP_MC };
struct Sched {
  int n, tot; int ty[128], ix[128], cn[128], pos[128], start[27], before[27];
  constexpr void put(int t, int i, int c) { ty[n] = t; ix[n] = i; cn[n] = c; pos[n] = tot; tot += c; ++n; }
  constexpr void puts(int t, int i, int c) { for (int k = 0; k < c; ++k) put(t, i + k, 1); }
  constexpr void putw(int r0, int r1) {
    while (r0 < r1) { int c = r1 - r0; if (c > 2) c = 2; if (r0 % 15 + c > 15) c = 15 - r0 % 15; put(OP_W, r0, c); r0 += c; } }
  constexpr void seg(int q) { start[q] = n; before[q] = tot; }
  constexpr int slot(int q, int K, int ns) const {
    int c = start[q + 1] - start[q]; if (c && ty[start[q + 1] - 1] == OP_STORE) --c;
    for (int k = 0; k < c; ++k) if ((k * ns) / c == K) return start[q] + k;
    return -1; }
  constexpr int at(int t, int i) const { for (int k = 0; k < n; ++k) if (ty[k] == t && i >= ix[k] && i < ix[k] + cn[k]) return pos[k] + i - ix[k]; return -1; }
};
constexpr int cmax(int a, int b) { return a > b ? a : b; }
constexpr Sched make_sched() {
  Sched s{};
  s.putw(0, 12); s.puts(OP_A, 0, 12);
  const int wto[25] = {12, 12, 15, 20, 25, 27, 30, 32, 35, 37, 40, 42, 45, 45, 47, 48, 50, 52, 54, 55, 57, 59, 60, 60, 60};
  const int a5to[25] = {0, 0, 0, 0, 0, 0, 0, 0, 0, 0, 0, 4, 8, 8, 8, 11, 13, 15, 16, 18, 20, 22, 24, 24, 24};
  for (int q = 0; q < 25; ++q) {
    s.seg(q);
    if (q == 10 || q == 14 || q == 18) s.put(OP_PEEK, (q - 10) / 4, 1);
    if (q < 2) { s.puts(OP_A, 12 + 4 * q, 4); s.put(OP_ST, q, 1); }
    if (q == 2) s.puts(OP_ST, 2, 2);
    if (q) s.puts(OP_A5, a5to[q - 1], a5to[q] - a5to[q - 1]);
    if (q) s.putw(wto[q - 1], wto[q]);
    if (q == 6 || q == 10 || q == 12) s.put(OP_STORE, q == 6 ? 0 : q == 10 ? 1 : 2, 1);
  }
  s.seg(25);
  return s;
}
constexpr Sched SC = make_sched();
constexpr int sc_wait(int q) {
  int p = -1;
  if (q <= 11) p = SC.at(OP_W, (10 * (q + 1) + (q == 11 ? 0 : q % 4 == 2 ? 10 : 6) + 3) / 4 - 1);
  if (q >= 13) p = cmax(SC.at(OP_W, (10 * q + (q < 24 ? 10 : 0) + 3) / 4 - 1), SC.at(OP_A5, 2 * (q - 13) + 1));
  if (q < 4) p = cmax(p, SC.at(OP_A, 5 * q + 4));
  if (q == 4) p = cmax(p, SC.at(OP_ST, 3));
  if (q == 8 || q == 12 || q == 15) p = cmax(p, SC.at(OP_STORE, q == 8 ? 0 : q == 12 ? 1 : 2));
  if (q == 11 || q == 15 || q == 19) p = cmax(p, SC.at(OP_PEEK, (q - 11) / 4));
  return SC.before[q] - 1 - p;
}
constexpr Sched make_sched1() {
  Sched s{}; int a = 5, r = 7;
  s.putw(0, 4); s.puts(OP_A, 0, 3); s.putw(4, 7); s.puts(OP_A, 3, 2);
  for (int q = 0; q < 12; ++q) {
    s.seg(q);
    int na = q < 3 ? 3 : q < 6 ? 2 : 0, nw = q < 5 ? 2 : q < 8 ? 1 : 0;
    s.puts(OP_A, a, na); a += na; s.putw(r, r + nw); r += nw;
  }
  s.seg(12);
  return s;
}
constexpr Sched S1 = make_sched1();
constexpr int s1_first(int q) { return q <= 10 ? 6 * q : q == 11 ? 64 : 80; }
constexpr int s1_wait(int q) {
  int pc = s1_first(q + 1) + 6; if (pc > 80 || q == 11) pc = 80;
  int p = S1.at(OP_W, (pc + 3) / 4 - 1);
  if (q < 10) p = cmax(p, S1.at(OP_A, q < 9 ? 2 * q + 2 : 19));
  return S1.before[q] - 1 - p;
}

#define STAMP(i)

DI void poll(const GAS unsigned* addr, unsigned base, unsigned want, bool active) {
#pragma nounroll
  for (unsigned s = 0; s < SPIN_CAP; ++s) {
    unsigned v = base + want;
    if (active) v = __hip_atomic_load(addr, RLX_AGENT);
    if (all64((v - base) >= want)) break;
    __builtin_amdgcn_s_sleep(2);
  }
  __builtin_amdgcn_fence(__ATOMIC_ACQUIRE, "wavefront");
}
DI unsigned lds_arrive(LAS char* L, int off, int lane) {
  unsigned o = 0;
  asm volatile("s_waitcnt lgkmcnt(0)" ::: "memory");
  if (lane == 0) o = __hip_atomic_fetch_add((LAS unsigned*)(L + off), 1u, __ATOMIC_RELAXED, __HIP_MEMORY_SCOPE_WORKGROUP);
  o = __builtin_amdgcn_readfirstlane(o);
  asm volatile("" ::: "memory");
  return o;
}
template <int N> DI void mod_rows(const v4u* wm, const unsigned* cv, LAS char* red, int lane) {
  float acc[8] = {0, 0, 0, 0, 0, 0, 0, 0};
#pragma unroll
  for (int i = 0; i < N; ++i) {
    float cf = bflo(cv[i]);
    float sv = bfr(cf * __builtin_amdgcn_rcpf(1.f + __builtin_amdgcn_exp2f(-1.44269504f * cf)));
#pragma unroll
    for (int e = 0; e < 4; ++e) { acc[2 * e] += sv * bflo(wm[i][e]); acc[2 * e + 1] += sv * bfhi(wm[i][e]); }
  }
  float u[4], v[2];
#pragma unroll
  for (int e = 0; e < 4; ++e) { f32x2 r = xswp32(acc[e], acc[e + 4]); u[e] = r.x + r.y; }
#pragma unroll
  for (int e = 0; e < 2; ++e) { f32x2 r = xswp16(u[e], u[e + 2]); v[e] = ror8_add(r.x + r.y); }
  if (!(lane & 8)) *(LAS f32x2*)(red + (lane & 7) * 32 + (lane >> 4) * 8) = f32x2{v[0], v[1]};
}
DI void lds_wait(LAS char* L, int off, unsigned want) {
#pragma nounroll
  for (unsigned s = 0; s < SPIN_CAP && *(volatile LAS unsigned*)(L + off) < want; ++s) __builtin_amdgcn_s_sleep(1);
  asm volatile("" ::: "memory");
}

__global__ __launch_bounds__(512, 2) void fused_block(Params P) {
  __shared__ __attribute__((aligned(16))) char lds_raw[163840];
  LAS char* L = (LAS char*)lds_raw;
  const unsigned LB = (unsigned)(size_t)L;
  const int tid = threadIdx.x, lane = tid & 63;
  const int w = __builtin_amdgcn_readfirstlane(tid >> 6);
  const int b = blockIdx.x;
  const int p = 4 * ((b & 7) >> 1) + ((b >> 3) & 3), j = 8 * (b & 1) + (b >> 5);
  const int m = w & 3, kh = w >> 2, h = lane >> 5, r32 = lane & 31;
  const int g4 = lane >> 4, i16 = lane & 15, q4 = (lane & 15) >> 2, p4 = lane & 3, blk = (lane >> 4) & 1;
  const rsrc_t rws = __builtin_amdgcn_make_buffer_rsrc((void*)P.ws, 0, WS_END, 0x00020000);
  const rsrc_t rx = __builtin_amdgcn_make_buffer_rsrc((void*)P.x, 0, 2048 * 1280, 0x00020000);
  const unsigned l16 = lane * 16;
  const unsigned ringL = kh * 61440 + l16, ringD = LB + kh * 61440 + m * 15360;
  STAMP(0);
  unsigned long long pc0; asm volatile("s_getpc_b64 %0" : "=s"(pc0));
  unsigned cbase = 0;
  const GAS unsigned* caddr = (const GAS unsigned*)P.ws;
  const int c40 = b % 40, c20 = (b - 160) % 20;
  const int cc = b < 160 ? (c40 < 20 ? c40 : c40 + 10) : (c20 < 10 ? 20 + c20 : 40 + c20), kq = b < 160 ? b / 40 : ((b - 160) / 20) & 3;
  const char* wm0 = (const char*)P.wmod + (size_t)(160 * kq) * 7680 + cc * 128;
  {
    const int bj = b < 240 ? b : 0, s = bj / 6, q = bj % 6;
    const int lr = (lane * 205) >> 11, lc = lane - 10 * lr;
    const bool lo = w < 4;
    v4u t[8]; unsigned cv[5], dst[2];
    {
      const char* px = (const char*)P.x + (size_t)b * 10240;
      const rsrc_t r1 = __builtin_amdgcn_make_buffer_rsrc((void*)(lo ? wm0 : px), 0, lo && b >= 160 ? 0 : 0x200000, 0x00020000);
      const rsrc_t r2 = __builtin_amdgcn_make_buffer_rsrc((void*)((const char*)P.wqkv + (size_t)(16 * s) * 3840 + q * 640 + m * 160), 0, lo ? 0x10000 : 0, 0x00020000);
      const rsrc_t rc = __builtin_amdgcn_make_buffer_rsrc((void*)(P.c + 160 * kq), 0, lo && b < 160 ? 320 : 0, 0x00020000);
      const unsigned v1 = lo ? (tid >> 3) * 7680 + (tid & 7) * 16 : (lane & 7) * 1280 + (lane >> 3) * 16 + m * 128, st1 = lo ? 245760 : 512;
      const unsigned v2 = (lr < 6 ? lr : 5) * 3840 + lc * 16;
      ld16p(t[0], v1, r1, 0); ld16p(t[1], v1, r1, st1); ld16p(t[2], v1, r1, w < 6 ? 2 * st1 : 0x7ff00000u);
      if (lo) {
        ld16p(t[3], v1, r1, 3 * st1); ld16p(t[4], v1, r1, 4 * st1);
        ld16p(t[5], v2, r2, 0); ld16p(t[6], v2, r2, 23040); ld16p(t[7], lr >= 4 ? v2 - 7680 : v2, r2, 46080);
        sfor<5>([&](auto I) { ld2p(cv[I], (tid >> 3) * 2, rc, I * 64); });
      } else if (w >= 6) {
        const rsrc_t ro = __builtin_amdgcn_make_buffer_rsrc((void*)P.wo, 0, 640 * 1280, 0x00020000);
#pragma unroll
        for (int u = 0; u < 2; ++u) {
          const int sl = 2 * (w - 6) + u, f = sl < 3 ? 3 * b + sl : 768 + (b & 31), cw = f / 100, st = (f % 100) / 5, tl = f % 5;
          const int e = (lane >> 1) & 7;
          const int ch = st < 16 ? 40 * st + 20 * (g4 >> 1) + 4 * (g4 & 1) + (e < 4 ? e : e + 4) : 40 * (4 * (st - 16) + g4) + 16 + (e < 4 ? e : e + 16);
          dma<false>(LB + 16384 + sl * 1024, ch * 1280 + (80 * cw + 16 * tl) * 2 + (lane & 1) * 16, ro, 0);
          dst[u] = WS_WT3 + cw * 106496 + (st & 1) * 53248 + strm_off((st >> 1) * 5 + tl, 13) + l16;
        }
      } else {
        ld16(t[3], l16, rws, b < 8 && w == 5 ? CT_FLAG + b * 1024 : 0x7ff00000);
        ld16p(t[4], l16, __builtin_amdgcn_make_buffer_rsrc((void*)(pc0 & ~255ull), 0, 35840, 0x00020000), (w == 5 ? 0 : 32768) + (b >> 3) * 1024);
      }
    }
    if (w == 4) {
      int off = CT_E;
      if (lane == 1) off = CT_C1 + (((p >= 1 ? p - 1 : 0) * 16 + j) << 7);
      if (lane == 2) off = CT_C1 + (((p >= 2 ? p - 2 : 0) * 16 + j) << 7);
      if (lane == 3) off = CT_C2 + (p << 7);
      if (lane == 4) off = CT_C3 + (p << 7);
      if (lane == 5) off = CT_C4 + p * 384;
      if (lane == 16 || lane == 17) off = CT_C4 + p * 384 + (lane - 15) * 128;
      if (lane == 7) off = CT_CM;
      if (lane >= 8 && lane < 16) off = CT_CB + ((lane - 8) << 7);
      caddr = (const GAS unsigned*)(P.ws + off);
    }
    peek(cbase, caddr);
    if (tid < 2) *(LAS v4u*)(L + L_CNT + tid * 16) = v4u{0u, 0u, 0u, 0u};
    bar();
    waitv<0>();
    sfor<8>([&](auto I) { tie(t[I]); });
    sfor<5>([&](auto I) { tie(cv[I]); });
    tie(cbase);
    if (lo) {
      const unsigned wql = L_WQ + w * 2560;
      if (lr < 6) { *(LAS v4u*)(L + wql + l16) = t[5]; *(LAS v4u*)(L + wql + 960 + l16) = t[6]; }
      if (lr < 4) *(LAS v4u*)(L + wql + 1920 + l16) = t[7];
      if (b < 160) {
        mod_rows<5>(t, cv, L + L_RED + w * 256, lane);
        if (lds_arrive(L, L_CNT, lane) == 3 && lane < 16) {
          f32x4 u = (ldsf4(L, L_RED + l16) + ldsf4(L, L_RED + 256 + l16)) + (ldsf4(L, L_RED + 512 + l16) + ldsf4(L, L_RED + 768 + l16));
          st16(__builtin_bit_cast(v4u, u), rws, WS_MODP + (kq * 3840 + cc * 64 + lane * 4) * 4);
        }
      }
      if (b < 240) {
        asm volatile("s_waitcnt lgkmcnt(0)" ::: "memory");
        const int part = q >> 1, jb = 8 * (q & 1) + 2 * w;
        const int sg = s >= 20 ? s - 20 : s, pk0 = 3 * (sg & ~1);
        const unsigned dst0 = WS_WT1 + jb * 163840 + (s >= 20 ? 81920 : 0);
        const unsigned dmain = dst0 + strm_off(part == 0 ? 60 + sg : pk0 + 2 * (sg & 1) + part - 1, 20), dmix = dst0 + strm_off(pk0 + 4 + (sg & 1), 20);
        const unsigned rowa = wql + (8 * h + q4) * 160;
        const unsigned a = rowa + (20 * blk + 4 * p4) * 2, a2 = rowa + (40 * blk + 16 + 20 * (p4 & 1)) * 2;
        v4u f0 = trpair(L, a, a + 640), f1 = trpair(L, a + 80, a + 720), f2 = trpair(L, a2, a2 + 640);
        st16(f0, rws, dmain + l16);
        st16(f1, rws, dmain + 163840 + l16);
        if (i16 >= 8) f2 = v4u{0, 0, 0, 0};
        const int r = (part == 0 ? 16 : part == 1 ? 0 : 8) + i16;
        if (part == 0 || i16 < 8) st16(f2, rws, dmix + blk * 163840 + (h * 32 + r) * 16);
      }
    } else {
      float s1 = 0.f, s2 = 0.f, s3 = 0.f, s4 = 0.f;
#pragma unroll
      for (int i = 0; i < 3; ++i) {
        s1 = dot2(t[i][0], 0x3f803f80u, s1); s2 = dot2(t[i][0], t[i][0], s2); s3 = dot2(t[i][1], 0x3f803f80u, s3); s4 = dot2(t[i][1], t[i][1], s4);
        s1 = dot2(t[i][2], 0x3f803f80u, s1); s2 = dot2(t[i][2], t[i][2], s2); s3 = dot2(t[i][3], 0x3f803f80u, s3); s4 = dot2(t[i][3], t[i][3], s4);
      }
      s1 = swap32_add(swap16_add(ror8_add(s1 + s3))); s2 = swap32_add(swap16_add(ror8_add(s2 + s4)));
      if (lane < 8) *(LAS f32x2*)(L + L_RED + 1024 + lane * 32 + m * 8) = f32x2{s1, s2};
      lds_arrive(L, L_CNT + 8, lane);
      if (w >= 6) {
#pragma unroll
        for (int u = 0; u < 2; ++u) {
          const unsigned a = 16384 + (2 * (w - 6) + u) * 1024 + (8 * g4 + q4) * 32 + p4 * 8;
          st16(trpair(L, a, a + 128), rws, dst[u]);
        }
      }
      lds_wait(L, L_CNT + 8, 4u);
      const f32x4 o0 = ldsf4(L, L_RED + 1024 + (lane & 7) * 32), o1 = ldsf4(L, L_RED + 1040 + (lane & 7) * 32);
      const float mean = ((o0[0] + o0[2]) + (o1[0] + o1[2])) * (1.f / 640.f);
      const float rstd = __builtin_amdgcn_rsqf(fmaxf(((o0[1] + o0[3]) + (o1[1] + o1[3])) * (1.f / 640.f) - mean * mean, 0.f) + 1e-5f), nm = -mean * rstd;
      const int tok = 8 * b + (lane & 7);
      const unsigned xo = WS_XH + (tok >> 5) * 40960 + m * 4096 + (lane >> 3) * 512 + (tok & 31) * 16;
#pragma unroll
      for (int i = 0; i < 3; ++i) {
        if (i == 2 && w >= 6) break;
        v4u o;
#pragma unroll
        for (int e = 0; e < 4; ++e) { f32x2 v = {fmaf(bflo(t[i][e]), rstd, nm), fmaf(bfhi(t[i][e]), rstd, nm)}; o[e] = __builtin_bit_cast(unsigned, __builtin_convertvector(v, f16x2)); }
        st16(o, rws, xo + i * 16384);
      }
    }
  }
  waitv<0>();
  if (lds_arrive(L, L_CNT + 4, lane) == 7 && lane < 8) __hip_atomic_store((GAS unsigned*)(P.ws + CT_FLAG + lane * 1024 + b * 4), __builtin_amdgcn_readfirstlane(cbase) + 1u, RLX_AGENT);
  STAMP(1);
  const int c8 = __builtin_amdgcn_readfirstlane(4 * (b & 1) + ((b >> 5) & 3)), rh = b >> 7;
  const int kp = (b >> 4) & 7;
  int col = tid < 320 ? 16 * tid : tid < 340 ? (2560 + 80 * kp + 4 * (tid - 320)) * 4 : tid < 360 ? (1920 + 80 * kp + 4 * (tid - 340)) * 4 : 0;
  unsigned mdst = L_MODV + (tid < 160 ? tid * 32 + 16 : tid < 320 ? (tid - 160) * 32 : 5760 + (tid - 320) * 16);
  const rsrc_t rb = __builtin_amdgcn_make_buffer_rsrc((void*)((const char*)((b >> 7) ? P.wu : P.wg) + (size_t)(80 * kp) * 3072 + (b & 15) * 192), 0, 80 * 3072, 0x00020000);
  tie(col); tie(mdst);
  const bool mod2 = b >= 160 && b < 240;
  v4u m2w[3]; unsigned m2c[3];
  __builtin_amdgcn_s_sleep(DELAY0);
  const int v6 = w < 4 ? w : w - 1;
  if (w == 7) {
    const rsrc_t rd = __builtin_amdgcn_make_buffer_rsrc((void*)((const char*)P.wd + (b / 5) * 40960 + (b % 5) * 256), 0, b < 240 ? 40960 : 0, 0x00020000);
    const int e = (lane >> 1) & 7;
    const unsigned vo = (16 * (g4 >> 1) + 4 * (g4 & 1) + (e < 4 ? e : e + 4)) * 1280 + (lane & 1) * 16;
    sfor<8>([&](auto U) { dma<false>(LB + L_GSRC + (18 + U) * 1024, vo, rd, U * 32); });
  } else if (w != 4) {
    const int e = (lane >> 2) & 7;
#pragma unroll
    for (int u = 0; u < 3; ++u) {
      const int sg = (v6 & 1) * 3 + u;
      const int k = sg < 4 ? 32 * (sg >> 1) + 4 * (2 * (sg & 1) + h) + (e < 4 ? e : e + 12) : 64 + 8 * h + e;
      if (sg < 5) dma<false>(LB + L_GSRC + (3 * v6 + u) * 1024, k * 3072 + (v6 >> 1) * 64 + p4 * 16, rb, 0);
    }
  }
  {
    const rsrc_t rm = __builtin_amdgcn_make_buffer_rsrc((void*)wm0, 0, mod2 ? 160 * 7680 : 0, 0x00020000);
    const rsrc_t rc = __builtin_amdgcn_make_buffer_rsrc((void*)(P.c + 160 * kq), 0, mod2 ? 320 : 0, 0x00020000);
    sfor<3>([&](auto I) { ld16p(m2w[I], (tid >> 3) * 7680 + (tid & 7) * 16, rm, I * 491520); ld2p(m2c[I], (tid >> 3) * 2, rc, I * 128); });
  }
  if (w == 4) {
    const unsigned tag = __builtin_amdgcn_readfirstlane(cbase) + 1u;
    GAS unsigned* go = (GAS unsigned*)(P.ws + CT_GO + (b & 7) * 128);
    if (b < 8) {
#pragma nounroll
      for (unsigned s = 0; s < SPIN_CAP; ++s) {
        v4u f; ld16(f, l16, rws, CT_FLAG + b * 1024); waitv<0>(); tie(f);
        if (all64(f.x == tag && f.y == tag && f.z == tag && f.w == tag)) break;
      }
      if (lane == 0) __hip_atomic_store(go, tag, RLX_AGENT);
    } else {
#pragma nounroll
      for (unsigned s = 0; s < SPIN_CAP && __hip_atomic_load(go, RLX_AGENT) != tag; ++s) __builtin_amdgcn_s_sleep(1);
    }
    __builtin_amdgcn_fence(__ATOMIC_ACQUIRE, "wavefront");
  }
  bar();
  STAMP(4);

  auto mod_put = [&](v4u* mp, bool isc, bool on, unsigned dst) {
    sfor<4>([&](auto I) { tie(mp[I]); });
    f32x4 v = (__builtin_bit_cast(f32x4, mp[0]) + __builtin_bit_cast(f32x4, mp[1])) + (__builtin_bit_cast(f32x4, mp[2]) + __builtin_bit_cast(f32x4, mp[3]));
#pragma unroll
    for (int e = 0; e < 4; ++e) { float t = bfr(v[e]); v[e] = isc ? bfr(1.f + t) : t; }
    if (on) *(LAS f32x4*)(L + dst) = v;
  };
  {
    v4u mp[4], xf[20];
    sfor<4>([&](auto I) { ld16(mp[I], col, rws, WS_MODP + I * 15360); });
    const unsigned w1 = WS_WT1 + j * 163840 + kh * 81920 + m * 20480;
    const unsigned ab = WS_XH + ((4 * p + m) * 40 + 20 * kh) * 1024;
    rbar();
    dman<4, 0, 0>(ringD, l16, rws, w1);
    sfor<3>([&](auto I) { ldf<I>(xf[I], l16, rws, ab); });
    waitv<7>();
    mod_put(mp, tid >= 160 && tid < 340, tid < 360, mdst);
    dman<3, 4096, 4096>(ringD, l16, rws, w1);
    ldf<3>(xf[3], l16, rws, ab); ldf<4>(xf[4], l16, rws, ab);
    STAMP(5);
    f32x16 acc[4] = {};
    const unsigned wbK = ringL + kh * 15360, wbS = ringL + 15360 - kh * 15360;
    unsigned cb = L_MODV + (80 * kh + 2 * h) * 32; tie(cb);
    unsigned xw = kh * 61440 + m * 15360 + 5120 + l16, xr = (kh ^ 1) * 61440 + m * 15360 + 5120 + l16;
    v4u wf[80], hf[20], c0, c1, c2; f32x4 ca[40], cs[40], eo[6];
    const unsigned kvg = WS_KV + ((4 * p + m) * 16 + j) * 6144, kvl = (8 + m) * KVT;
    auto rdw = [&](auto N) {
      constexpr int n = N < 60 ? 3 * (N / 3) + 2 - N % 3 : (N ^ 1), i = n / 3, pk0 = 3 * (i & ~1);
      if constexpr (n >= 60) wf[n] = lds16(L, ringL + ring_off(n));
      else if constexpr (n % 3 == 1) wf[n] = lds16(L, ringL + ring_off(pk0 + 4 + (i & 1)));
      else wf[n] = lds16(L, (n % 3 == 0 ? wbS : wbK) + ring_off(pk0 + 2 * (i & 1)));
    };
    auto rdc = [&](auto R) { ca[R] = ldsf4(L, cb + (R / 2) * 128 + (R & 1) * 32); cs[R] = ldsf4(L, cb + (R / 2) * 128 + (R & 1) * 32 + 16); };
    auto aff = [&](auto I, auto E) { constexpr int e = 2 * E; unsigned o0, o1; affine4(xf[I][e], xf[I][e + 1], ca[2 * I + E], cs[2 * I + E], o0, o1); hf[I][e] = o0; hf[I][e + 1] = o1; };
    auto quad = [&](const f32x16& a, int q) { return f32x4{a[4 * q], a[4 * q + 1], a[4 * q + 2], a[4 * q + 3]}; };
    sfor<12>([&](auto Q) {
      constexpr int q = Q, n0 = s1_first(q);
      waitv<s1_wait(q)>();
      if constexpr (q == 0) tie(xf[0]);
      if constexpr (q < 10) { tie(xf[2 * q + 1]); if constexpr (q < 9) tie(xf[2 * q + 2]); }
      if constexpr (q == 0 || q == 11) bar(); else rbar();
      if constexpr (q == 0) { sfor<6>(rdc); sfor<6>(rdw); aff(IC<0>{}, IC<0>{}); aff(IC<0>{}, IC<1>{}); asm volatile("s_nop 1"); SB(); }
      sfor<s1_first(q + 1) - n0>([&](auto K) {
        constexpr int n = n0 + K, i = n < 60 ? n / 3 : n - 60, t = n < 60 ? n % 3 : 3, k = S1.slot(q, K, 6);
        acc[t] = mfma32(wf[n], hf[i], acc[t]); tie(acc[t]);
        SB();
        if constexpr (t < 2 && i + 1 < 20) aff(IC<i + 1>{}, IC<t>{});
        if constexpr (t < 2 && i + 3 < 20) rdc(IC<2 * (i + 3) + t>{});
        if constexpr (n + 6 < 80) rdw(IC<n + 6>{});
        if constexpr (k >= 0) {
          constexpr int ty = S1.ty[k], ix = S1.ix[k];
          if constexpr (ty == OP_A) ldf<ix>(xf[ix], l16, rws, ab);
          else if constexpr (ty == OP_W) dman<S1.cn[k], (ix % 15) * 1024, ix * 1024>(ringD, l16, rws, w1);
        }
        if constexpr (n >= 59 && n < 63) *(LAS f32x4*)(L + xw + (n - 59) * 1024) = quad(acc[0], n - 59);
        if constexpr (n == 63) { if (kh) { *(LAS f32x4*)(L + xw + 4096) = quad(acc[1], 0); *(LAS f32x4*)(L + xw + 5120) = quad(acc[1], 1); } }
        if constexpr (n == 64) sfor<6>([&](auto I) { eo[I] = ldsf4(L, xr + I * 1024); });
        if constexpr (n == 65 || n == 66) {
          constexpr int o = 8 * (n - 65);
          v4u c = {pk2(acc[2][o] + eo[o / 4][0], acc[2][o + 1] + eo[o / 4][1]), pk2(acc[2][o + 2] + eo[o / 4][2], acc[2][o + 3] + eo[o / 4][3]),
                   pk2(acc[2][o + 4] + eo[o / 4 + 1][0], acc[2][o + 5] + eo[o / 4 + 1][1]), pk2(acc[2][o + 6] + eo[o / 4 + 1][2], acc[2][o + 7] + eo[o / 4 + 1][3])};
          st16(c, rws, kvg + (kh ? 3072 + r32 * 32 + h * 16 : l16) + (n - 65) * 1024);
          if constexpr (n == 65) c0 = c; else c1 = c;
        }
        if constexpr (n == 67) {
          c2 = v4u{pk2(acc[1][0] + eo[4][0], acc[1][1] + eo[4][1]), pk2(acc[1][2] + eo[4][2], acc[1][3] + eo[4][3]),
                   pk2(acc[1][4] + eo[5][0], acc[1][5] + eo[5][1]), pk2(acc[1][6] + eo[5][2], acc[1][7] + eo[5][3])};
          if (!kh) { st16(v4u{c2.x, c2.y, 0u, 0u}, rws, kvg + 2048 + l16); st16(v4u{c2.z, c2.w, 0x3f80u, 0u}, rws, kvg + 5120 + l16); }
        }
        SB();
      });
    });
    STAMP(6);
    const unsigned xq = 5120 + (m >> 1) * 15360 + (m & 1) * 5120 + l16;
    if (!kh) {
#pragma unroll
      for (int qd = 0; qd < 4; ++qd) *(LAS f32x4*)(L + xq + qd * 1024) = quad(acc[3], qd);
      *(LAS f32x4*)(L + xq + 4096) = quad(acc[1], 2);
    }
    if (mod2) {
      sfor<3>([&](auto I) { tie(m2w[I]); tie(m2c[I]); });
      mod_rows<3>(m2w, m2c, L + L_RED2 + w * 256, lane);
    }
    waitv<0>();
    bar();
    if (tid == 0) __hip_atomic_fetch_add((GAS unsigned*)(P.ws + CT_C1 + ((p * 16 + j) << 7)), 1u, RLX_AGENT);
    {
      const unsigned lo = kh ? 3072 + r32 * 48 + h * 16 : l16, ls = kh ? 1536 : 1024;
      *(LAS v4u*)(L + kvl + lo) = c0; *(LAS v4u*)(L + kvl + lo + ls) = c1;
    }
    if (kh == 0) {
      *(LAS v4u*)(L + kvl + 2048 + l16) = v4u{c2.x, c2.y, 0u, 0u};
      *(LAS v4u*)(L + kvl + 3072 + h * 1536 + r32 * 48 + 32) = v4u{c2.z, c2.w, 0x3f80u, 0u};
    } else {
      const float qs = 0.22360679775f * 1.44269504089f;
      float qv[20];
#pragma unroll
      for (int qd = 0; qd < 5; ++qd) {
        const f32x4 u = ldsf4(L, xq + qd * 1024);
#pragma unroll
        for (int e = 0; e < 4; ++e) qv[4 * qd + e] = ((qd < 4 ? acc[3][4 * qd + e] : acc[1][8 + e]) + u[e]) * qs;
      }
      *(LAS v4u*)(L + L_Q + m * 2048 + l16) = v4u{pk2(qv[0], qv[1]), pk2(qv[2], qv[3]), pk2(qv[4], qv[5]), pk2(qv[6], qv[7])};
      *(LAS v4u*)(L + L_Q + m * 2048 + 1024 + l16) = v4u{pk2(qv[8], qv[9]), pk2(qv[10], qv[11]), pk2(qv[12], qv[13]), pk2(qv[14], qv[15])};
      *(LAS v2u*)(L + L_QHI + m * 512 + lane * 8) = v2u{pk2(qv[16], qv[17]), pk2(qv[18], qv[19])};
    }
  if (mod2 && w == 3 && lane < 16) {
    f32x4 u = ldsf4(L, L_RED2 + l16);
#pragma unroll
    for (int i = 1; i < 8; ++i) u += ldsf4(L, L_RED2 + i * 256 + l16);
    st16(__builtin_bit_cast(v4u, u), rws, WS_MODP + (kq * 3840 + cc * 64 + lane * 4) * 4);
  }
    STAMP(7);
  }
  if (w == 4) poll(caddr, cbase, 1u, (lane == 1 && p >= 1) || (lane == 2 && p >= 2));
  else if (w == 7) {
    if (b < 240) {
      const int st0 = b / 5, st = 16 * (st0 % 3) + st0 / 3, ct0 = 8 * (b % 5);
      const unsigned a = L_GSRC + 18432 + (8 * g4 + q4) * 32 + p4 * 8, d0 = WS_WT5 + (st & 1) * 122880 + l16;
#pragma unroll
      for (int u = 0; u < 8; ++u) { const int ct = ct0 + u; st16(trpair(L, a + u * 1024, a + u * 1024 + 128), rws, d0 + (ct / 5) * 245760 + strm_off((st >> 1) * 5 + ct % 5, 30)); }
    }
  } else {
    const int mat = b >> 7, jd = b & 15, t = 2 * (v6 >> 1) + blk;
    const unsigned d0 = WS_WT4 + jd * 245760 + (kp >> 2) * 122880 + blk * 30720 + (h * 32 + mat * 16 + i16) * 16;
    float us = 0.f, vs = 0.f;
#pragma unroll
    for (int u = 0; u < 3; ++u) {
      const int sg = (v6 & 1) * 3 + u;
      if (sg == 5) break;
      const int k0 = sg < 4 ? 32 * (sg >> 1) + 4 * (2 * (sg & 1) + h) : 64 + 8 * h, k1 = sg < 4 ? k0 + 16 : k0 + 4;
      const unsigned ca = L_GSRC + (3 * v6 + u) * 1024 + (8 * h + q4) * 64 + blk * 32 + p4 * 8;
      v4u f = trpair(L, ca, ca + 256);
      f32x4 a0 = ldsf4(L, L_MODV + 5760 + k0 * 4), a1 = ldsf4(L, L_MODV + 5760 + k1 * 4);
      f32x4 b0 = ldsf4(L, L_MODV + 6080 + k0 * 4), b1 = ldsf4(L, L_MODV + 6080 + k1 * 4);
      v4u o;
      o[0] = pk2(bflo(f[0]) * a0[0], bfhi(f[0]) * a0[1]); o[1] = pk2(bflo(f[1]) * a0[2], bfhi(f[1]) * a0[3]);
      o[2] = pk2(bflo(f[2]) * a1[0], bfhi(f[2]) * a1[1]); o[3] = pk2(bflo(f[3]) * a1[2], bfhi(f[3]) * a1[3]);
      vs += bflo(f[0]) * b0[0] + bfhi(f[0]) * b0[1] + bflo(f[1]) * b0[2] + bfhi(f[1]) * b0[3] + bflo(f[2]) * b1[0] + bfhi(f[2]) * b1[1] + bflo(f[3]) * b1[2] + bfhi(f[3]) * b1[3];
      us = dot2(o[0], 0x3f803f80u, us); us = dot2(o[1], 0x3f803f80u, us); us = dot2(o[2], 0x3f803f80u, us); us = dot2(o[3], 0x3f803f80u, us);
      st16(o, rws, d0 + strm_off((v6 >> 1) * 40 + (5 * (kp & 3) + sg) * 2, 30));
    }
    us = swap32_add(us); vs = swap32_add(vs);
    const int row = (jd * 6 + t) * 32 + mat * 16 + i16;
    __builtin_amdgcn_raw_buffer_store_b32(__builtin_bit_cast(unsigned, h ? vs : us), rws, WS_UV + (((kp * 2 + (v6 & 1)) * 2 + h) * 3072 + row) * 4, 0, 16);
  }
  bar();
  STAMP(8);
  const int gt = 4 * p - 8 + w;
  {
    const unsigned ko = gt >= 0 ? WS_KV + (gt * 16 + j) * 6144 : 0x7ff00000u, lo = LB + w * KVT;
    dman<3, 0, 0>(lo, l16, rws, ko);
    sfor<3>([&](auto I) {
      const int c = 64 * I + lane, rw = (c * 171) >> 9, pt = c - 3 * rw;
      dman<1, 3072 + I * 1024, 0>(lo, pt < 2 ? 3072 + rw * 32 + pt * 16 : 5120 + rw * 16, rws, ko);
    });
  }
  STAMP(9);
  const unsigned w3 = WS_WT3 + c8 * 106496 + kh * 53248 + m * 13312;
  unsigned cbv;
  {
    waitv<0>();
    if (gt < 0) *(LAS unsigned*)(L + w * KVT + 2056 + l16) = 0xfe96u;
    bar();
    if (tid == 0 && mod2) __hip_atomic_fetch_add((GAS unsigned*)(P.ws + CT_CM), 1u, RLX_AGENT);
    if (tid == 64) __hip_atomic_fetch_add((GAS unsigned*)(P.ws + CT_CB + ((b & 7) << 7)), 1u, RLX_AGENT);
    STAMP(10);
    const int hd = kh;
    const v4u q0 = lds16(L, L_Q + m * 2048 + hd * 1024 + l16);
    v2u qh = *(LAS v2u*)(L + L_QHI + m * 512 + lane * 8);
    if (h != hd) qh = v2u{0, 0};
    const v4u q1 = {qh.x, qh.y, 0x3f80u, 0};
    const f32x16 zero = {};
    const unsigned ka = m * KVT + hd * 1024 + l16, kb = m * KVT + 2048 + l16;
    v4u k0[9], k1[9];
    sfor<9>([&](auto I) { constexpr int i = I == 0 ? 0 : I == 1 ? 8 : I - 1; k0[i] = lds16(L, ka + i * KVT); k1[i] = lds16(L, kb + i * KVT); });
    dman<4, L_W3A, 0>(LB + kh * 32768 + m * 8192, l16, rws, w3);
    dman<4, L_W3A + 4096, 4096>(LB + kh * 32768 + m * 8192, l16, rws, w3);
    auto qk = [&](auto I, f32x16 c) { f32x16 r = mfma32(k1[I], q1, mfma32(k0[I], q0, c)); tie(r); return r; };
    auto vmax = [](float a, float c) { return __builtin_elementwise_maximum(a, c); };
    auto tmax = [&](const f32x16& v, float t) {
#pragma unroll
      for (int r = 0; r < 16; r += 2) t = vmax(vmax(v[r], v[r + 1]), t);
      tie(t); return t; };
    f32x16 Sm, S[8];
    float mx = -3.0e38f;
    {
      const f32x16 s0 = qk(IC<0>{}, zero), s8 = qk(IC<8>{}, zero);
      S[1] = qk(IC<1>{}, zero); SB();
#pragma unroll
      for (int r = 0; r < 16; ++r) Sm[r] = (r & 3) + 8 * (r >> 2) + 4 * h <= r32 ? s8[r] : s0[r];
      tie(Sm); SB();
    }
    sfor<7>([&](auto I) {
      if constexpr (I < 6) S[I + 2] = qk(IC<I + 2>{}, zero);
      SB();
      if constexpr (I == 0) mx = tmax(Sm, mx); else mx = tmax(S[I], mx);
      SB();
    });
    const unsigned va = m * KVT + 3072 + hd * 1536 + (4 * h + q4) * 48 + (blk == 0 ? (p4 & 1) * 16 + (p4 >> 1) * 8 : (p4 ? 40 : 32));
    v4u vf[18];
    auto rdv = [&](auto I) { vf[2 * I + 1] = trpair(L, va + I * KVT + 768, va + I * KVT + 1152); vf[2 * I] = trpair(L, va + I * KVT, va + I * KVT + 384); };
    rdv(IC<8>{}); rdv(IC<0>{}); SB();
    mx = tmax(S[7], mx);
    { f32x2 t = swp32(mx); mx = vmax(t.x, t.y); }
    f32x16 negm;
#pragma unroll
    for (int r = 0; r < 16; ++r) negm[r] = -mx;
    tie(negm);
    f32x16 O = {};
    S[1] = qk(IC<1>{}, negm); rdv(IC<1>{}); SB();
#pragma unroll
    for (int s = 0; s < 2; ++s) {
      v4u p0, p8;
#pragma unroll
      for (int e = 0; e < 4; ++e) {
        const int r = 8 * s + 2 * e, key = (r & 3) + 8 * (r >> 2) + 4 * h;
        const float a = __builtin_amdgcn_exp2f(Sm[r] - mx), c = __builtin_amdgcn_exp2f(Sm[r + 1] - mx);
        const float a8 = key <= r32 ? a : 0.f, c8_ = key + 1 <= r32 ? c : 0.f;
        p8[e] = pk2(a8, c8_); p0[e] = pk2(a - a8, c - c8_);
      }
      O = mfma32(vf[s], p0, O); O = mfma32(vf[16 + s], p8, O); tie(O); SB();
    }
    sfor<7>([&](auto I) {
      constexpr int i = I + 1;
      if constexpr (i < 7) { S[i + 1] = qk(IC<i + 1>{}, negm); rdv(IC<i + 1>{}); SB(); }
      v4u pf[2];
#pragma unroll
      for (int e = 0; e < 8; ++e) pf[e / 4][e % 4] = pk2(__builtin_amdgcn_exp2f(S[i][2 * e]), __builtin_amdgcn_exp2f(S[i][2 * e + 1]));
      O = mfma32(vf[2 * i], pf[0], O); O = mfma32(vf[2 * i + 1], pf[1], O); tie(O); SB();
    });
    const float inv = __builtin_amdgcn_rcpf(swap32_hi(O[8]));
    const int tok = 128 * p + 32 * m + r32;
    const unsigned ob = WS_OF + (tok >> 4) * 20480;
    STAMP(11);
    st16(v4u{pk2(O[0] * inv, O[1] * inv), pk2(O[2] * inv, O[3] * inv), pk2(O[4] * inv, O[5] * inv), pk2(O[6] * inv, O[7] * inv)}, rws, ob + ((j & 1) * 10 + (j >> 1)) * 1024 + (2 * hd + h) * 256 + (tok & 15) * 16);
    if (h == 0) st8(v2u{pk2(O[8] * inv, O[9] * inv), pk2(O[10] * inv, O[11] * inv)}, rws, ob + (((j >> 2) & 1) * 10 + 8 + (j >> 3)) * 1024 + hd * 512 + (j & 3) * 128 + (tok & 15) * 8);
  }
  waitv<0>();
  bar();
  const int tt = 8 * p + 4 * rh + m;
  unsigned x2k[6];
  {
    v2u xr[3];
    const unsigned xo = (16 * tt + i16) * 1280 + (80 * c8 + 4 * g4) * 2;
    if (tid == 0) __hip_atomic_fetch_add((GAS unsigned*)(P.ws + CT_C2 + (p << 7)), 1u, RLX_AGENT);
    STAMP(12);
    v4u mp[4];
    ld8p(xr[0], xo, rx, kh * 64); ld8p(xr[1], xo, rx, kh * 64 + 32); ld8p(xr[2], xo, rx, 128);
    dman<4, 0, 8192>(LB + kh * 20480 + m * 5120, l16, rws, w3); dman<1, 4096, 12288>(LB + kh * 20480 + m * 5120, l16, rws, w3);
    if (w == 4) poll(caddr, cbase, lane == 7 ? 80u : 16u, lane == 3 || lane == 7);
    bar();
    STAMP(13);
    v4u af[10], wf[50];
    const unsigned ab = WS_OF + (tt * 20 + kh * 10) * 1024;
    v2u ah[4];
    sfor<4>([&](auto I) { ld16(mp[I], w == 5 ? ((lane < 20 ? 1280 : 3200 - 80) + 80 * c8 + 4 * (lane < 40 ? lane : 20)) * 4 : 0x7ff00000, rws, WS_MODP + I * 15360); });
    sfor<8>([&](auto I) { ldf<I>(af[I], l16, rws, ab); });
    sfor<4>([&](auto I) { ldh<I * 512>(ah[I], lane * 8, rws, ab + 8192); });
    peek(cbv, lane >= 8 && lane < 16 ? caddr : (const GAS unsigned*)P.ws);
    f32x4 acc[5] = {};
    unsigned wa = L_W3A + kh * 32768 + l16; tie(wa);
    const unsigned wb = kh * 20480 + l16;
    auto rdw = [&](auto N) {
      constexpr int n = 5 * (N / 5) + 4 - N % 5;
      if constexpr (n < 32) wf[n] = lds16(L, wa + (n & 3) * 8192 + (n >> 2) * 1024); else wf[n] = lds16(L, wb + (n & 3) * 5120 + ((n >> 2) - 8) * 1024);
    };
    sfor<10>([&](auto E) {
      constexpr int e = E;
      if constexpr (!(e & 1) && e < 8) { waitv<11 - e>(); tie(af[e]); tie(af[e + 1]); }
      if constexpr (e == 8) { waitv<1>(); sfor<2>([&](auto I) { tie(ah[2 * I]); tie(ah[2 * I + 1]); af[8 + I] = v4u{ah[2 * I].x, ah[2 * I].y, ah[2 * I + 1].x, ah[2 * I + 1].y}; }); }
      if constexpr (e == 0) { rbar(); sfor<8>(rdw); }
      if constexpr (e == 1) { if (w == 5) mod_put(mp, false, lane < 40, L_MODV + 5120 + l16); }
      sfor<5>([&](auto K) {
        constexpr int n = 5 * e + K;
        acc[K] = mfma16(wf[n], af[e], acc[K]); tie(acc[K]);
        if constexpr (n + 8 < 50) rdw(IC<n + 8>{});
        SB();
      });
    });
    STAMP(14);
    const unsigned xs = 57344 + m * 5120;
    if (kh == 0) { *(LAS f32x4*)(L + xs + l16) = acc[2]; *(LAS f32x4*)(L + xs + 1024 + l16) = acc[3]; }
    else { *(LAS f32x4*)(L + xs + 2048 + l16) = acc[0]; *(LAS f32x4*)(L + xs + 3072 + l16) = acc[1]; *(LAS f32x4*)(L + xs + 4096 + l16) = acc[4]; }
    bar();
    tie(xr[0]); tie(xr[1]); tie(xr[2]);
    f32x4 r0, r1, r2 = {};
    if (kh == 0) { r0 = acc[0] + ldsf4(L, xs + 2048 + l16); r1 = acc[1] + ldsf4(L, xs + 3072 + l16); r2 = acc[4] + ldsf4(L, xs + 4096 + l16); }
    else { r0 = acc[2] + ldsf4(L, xs + l16); r1 = acc[3] + ldsf4(L, xs + 1024 + l16); }
    const f32x4 ga0 = ldsf4(L, L_MODV + 5120 + (32 * kh + 4 * g4) * 4), ga1 = ldsf4(L, L_MODV + 5120 + (32 * kh + 16 + 4 * g4) * 4), ga2 = ldsf4(L, L_MODV + 5120 + (64 + 4 * g4) * 4);
    x2k[0] = pk2(bflo(xr[0].x) + ga0[0] * r0[0], bfhi(xr[0].x) + ga0[1] * r0[1]); x2k[1] = pk2(bflo(xr[0].y) + ga0[2] * r0[2], bfhi(xr[0].y) + ga0[3] * r0[3]);
    x2k[2] = pk2(bflo(xr[1].x) + ga1[0] * r1[0], bfhi(xr[1].x) + ga1[1] * r1[1]); x2k[3] = pk2(bflo(xr[1].y) + ga1[2] * r1[2], bfhi(xr[1].y) + ga1[3] * r1[3]);
    x2k[4] = pk2(bflo(xr[2].x) + ga2[0] * r2[0], bfhi(xr[2].x) + ga2[1] * r2[1]); x2k[5] = pk2(bflo(xr[2].y) + ga2[2] * r2[2], bfhi(xr[2].y) + ga2[3] * r2[3]);
    if (kh) { x2k[4] = 0; x2k[5] = 0; }
    const unsigned xb = WS_X2F + ((tt >> 1) * 40 + 5 * c8) * 1024 + ((tt & 1) * 16 + i16) * 16;
    st16(v4u{x2k[0], x2k[1], x2k[2], x2k[3]}, rws, xb + (2 * kh + (g4 >> 1)) * 1024 + (g4 & 1) * 512);
    if (kh == 0) st8(v2u{x2k[4], x2k[5]}, rws, xb + 4096 + (g4 >> 1) * 512 + (g4 & 1) * 8);
    float s1 = 0.f, s2 = 0.f;
#pragma unroll
    for (int e = 0; e < 6; ++e) { s1 = dot2(x2k[e], 0x3f803f80u, s1); s2 = dot2(x2k[e], x2k[e], s2); }
    s1 = swap32_add(swap16_add(s1)); s2 = swap32_add(swap16_add(s2));
    if (g4 == 0) st8(v2u{__builtin_bit_cast(unsigned, s1), __builtin_bit_cast(unsigned, s2)}, rws, WS_ST + ((tt >> 1) * 16 + 2 * c8 + kh) * 256 + ((tt & 1) * 16 + i16) * 8);
  }
  waitv<0>();
  tie(cbv);
  if (w == 4) *(LAS unsigned*)(L + L_CNT) = all64(lane < 8 || lane >= 16 || cbv - cbase >= 32u);
  bar();
  const bool early = __builtin_amdgcn_readfirstlane(*(LAS unsigned*)(L + L_CNT)) != 0;
  {
    const unsigned w4 = WS_WT4 + j * 245760 + kh * 122880 + m * 30720, w5 = WS_WT5 + c8 * 245760 + kh * 122880 + m * 30720 - 30720;
    if (tid == 0) __hip_atomic_fetch_add((GAS unsigned*)(P.ws + CT_C3 + (p << 7)), 1u, RLX_AGENT);
    STAMP(15);
    if (!early) { if (w == 4) poll(caddr, cbase, 32u, lane >= 8 && lane < 16); bar(); }
    const int rq = (v6 < 6 ? v6 : 0) * 16 + i16;
    v4u uv[4];
    sfor<4>([&](auto I) { ld16(uv[I], w == 4 ? 0x7ff00000 : (g4 * 8 + rq / 48) * 12288 + (j * 192 + 4 * (rq % 48)) * 4, rws, WS_UV + I * 24576); });
    dman<4, 0, 0>(ringD, l16, rws, w4); dman<4, 4096, 4096>(ringD, l16, rws, w4); dman<4, 8192, 8192>(ringD, l16, rws, w4);
    waitv<12>();
    sfor<4>([&](auto I) { tie(uv[I]); });
    if (w == 4) poll(caddr, cbase, 16u, lane == 4);
    else {
      f32x4 u = (__builtin_bit_cast(f32x4, uv[0]) + __builtin_bit_cast(f32x4, uv[1])) + (__builtin_bit_cast(f32x4, uv[2]) + __builtin_bit_cast(f32x4, uv[3]));
#pragma unroll
      for (int e = 0; e < 4; ++e) { float t = u[e]; u[e] = swap32_add(swap16_add(t)); }
      if (v6 < 6 && lane < 16) *(LAS f32x4*)(L + L_UVS + rq * 16) = u;
    }
    bar();
    STAMP(16);
    v4u af[20], a5[24], stv[4], wf[240]; unsigned pk;
    const unsigned ab = WS_X2F + ((4 * p + m) * 40 + 20 * kh) * 1024, ab5 = WS_ACT + (tt * 48 + 24 * kh) * 1024;
    sfor<12>([&](auto I) { ldf<I>(af[I], l16, rws, ab); });
    const int tok = 128 * p + 32 * m + r32;
    const unsigned ao = WS_ACT + ((tok >> 4) * 48 + (j & 1) * 24 + (j >> 1)) * 1024 + kh * 512 + h * 256 + (tok & 15) * 16;
    f32x16 aK[3] = {}, aS[3] = {};
    f32x4 acc[5] = {};
    const unsigned wbK = ringL + kh * 15360, wbS = ringL + 15360 - kh * 15360;
    unsigned xw = L_BSRC + w * 4096 + l16, xr_ = L_BSRC + (w ^ 4) * 4096 + l16, ub = L_UVS + kh * 128 + h * 16; tie(xw); tie(xr_); tie(ub);
    float rs2, nm2, av[8]; f32x4 eo[4], eu[4], ev[4];
    constexpr auto isS = [](int n) { return (n / 10) % 4 == 3 ? n % 10 < 5 : (n & 1) != 0; };
    constexpr auto stp = [](int n) { return (n / 10) % 4 == 3 ? (n / 10) * 5 + n % 5 : n / 2; };
    auto rdw = [&](auto N) {
      constexpr int n = N < 120 ? (N ^ 1) : 5 * (N / 5) + 4 - N % 5;
      if constexpr (n < 120) wf[n] = lds16(L, (isS(n) ? wbS : wbK) + ring_off(2 * stp(n))); else wf[n] = lds16(L, ringL + ring_off(n));
    };
    auto vmem = [&](auto Q, auto K) {
      constexpr int k = SC.slot(Q, K, 10);
      if constexpr (k >= 0) {
        constexpr int ty = SC.ty[k], ix = SC.ix[k];
        if constexpr (ty == OP_A) ldf<ix>(af[ix], l16, rws, ab);
        else if constexpr (ty == OP_W) dman<SC.cn[k], (ix % 15) * 1024, ix * 1024>(ringD, l16, rws, ix < 30 ? w4 : w5);
        else if constexpr (ty == OP_ST) ldf<ix>(stv[ix], l16, rws, WS_ST + (4 * p + m) * 4096);
        else if constexpr (ty == OP_PEEK) peek(pk, lane == (ix ? 15 + ix : 5) ? caddr : (const GAS unsigned*)P.ws);
        else if constexpr (ty == OP_A5) ldf<ix>(a5[ix], l16, rws, ab5);
      }
    };
    auto epiA = [&](auto G, auto QD) { const f32x16 sd = aS[G]; *(LAS f32x4*)(L + xw + QD * 1024) = f32x4{sd[4 * QD], sd[4 * QD + 1], sd[4 * QD + 2], sd[4 * QD + 3]}; };
    auto epi = [&](auto G, auto K) {
      if constexpr (K < 2) sfor<2>([&](auto I) { constexpr int qd = 2 * K + I; eo[qd] = ldsf4(L, xr_ + qd * 1024); eu[qd] = ldsf4(L, ub + G * 256 + qd * 32); ev[qd] = ldsf4(L, ub + 768 + G * 256 + qd * 32); });
      else if constexpr (K < 18) { constexpr int r = K - 2; float t = fmaf(aK[G][r] + eo[r / 4][r % 4], rs2, fmaf(nm2, eu[r / 4][r % 4], ev[r / 4][r % 4])); tie(t); aK[G][r] = t; }
      else if constexpr (K < 26) { constexpr int k = K - 18; const float gv = aK[G][k]; float t = gv * __builtin_amdgcn_rcpf(1.f + __builtin_amdgcn_exp2f(-1.44269504f * gv)) * aK[G][8 + k]; tie(t); av[k] = t; }
      else if constexpr (K == 29) st16(v4u{pk2(av[0], av[1]), pk2(av[2], av[3]), pk2(av[4], av[5]), pk2(av[6], av[7])}, rws, ao + G * 8192);
    };
    auto top = [&](auto Q) {
      constexpr int q = Q;
      waitv<sc_wait(q)>();
      if constexpr (q < 4) sfor<5>([&](auto I) { tie(af[5 * q + I]); });
      if constexpr (q >= 13) { tie(a5[2 * (q - 13)]); tie(a5[2 * (q - 13) + 1]); }
      if constexpr (q == 11 || q == 15 || q == 19) {
        constexpr int ln = q == 11 ? 5 : q == 15 ? 16 : 17;
        tie(pk);
        if (w == 4 && !all64(lane != ln || pk - cbase >= 16u)) poll(caddr, cbase, 16u, lane == ln);
      }
      if constexpr (q == 4 || q == 8 || q == 12) bar(); else rbar();
      if constexpr (q == 8 || q == 12 || q == 15) { if (tid == 64) __hip_atomic_fetch_add((GAS unsigned*)(P.ws + CT_C4 + p * 384 + (q == 8 ? 0 : q == 12 ? 128 : 256)), 1u, RLX_AGENT); }
    };
    sfor<12>([&](auto Q) {
      constexpr int q = Q, g = q / 4;
      top(Q);
      if constexpr (q == 0) sfor<6>(rdw);
      sfor<10>([&](auto K) {
        constexpr int n = 10 * q + K, i = stp(n) % 20;
        if constexpr (isS(n)) { aS[g] = mfma32(wf[n], af[i], aS[g]); tie(aS[g]); } else { aK[g] = mfma32(wf[n], af[i], aK[g]); tie(aK[g]); }
        if constexpr (n + 6 < 120) rdw(IC<n + 6>{});
        vmem(Q, K);
        if constexpr (q == 4 && K == 1) {
          sfor<4>([&](auto I) { tie(stv[I]); });
          f32x4 v = (__builtin_bit_cast(f32x4, stv[0]) + __builtin_bit_cast(f32x4, stv[1])) + (__builtin_bit_cast(f32x4, stv[2]) + __builtin_bit_cast(f32x4, stv[3]));
          float u[4];
#pragma unroll
          for (int e = 0; e < 4; ++e) { float a = v[e]; a = swap32_add(swap16_add(a)); u[e] = __builtin_bit_cast(float, __builtin_amdgcn_ds_bpermute((r32 >> 1) * 4, __builtin_bit_cast(int, a))); }
          const float s1 = (lane & 1) ? u[2] : u[0], s2 = (lane & 1) ? u[3] : u[1];
          const float mean = s1 * (1.f / 640.f);
          rs2 = __builtin_amdgcn_rsqf(fmaxf(s2 * (1.f / 640.f) - mean * mean, 0.f) + 1e-5f); nm2 = -mean * rs2; tie(rs2); tie(nm2);
        }
        if constexpr (q % 4 == 3 && K >= 6) epiA(IC<g>{}, IC<K - 6>{});
        if constexpr (g > 0 && q % 4 < 3) epi(IC<g - 1>{}, IC<10 * (q % 4) + K>{});
        SB();
      });
    });
    STAMP(17);
    top(IC<12>{});
    sfor<30>([&](auto K) { if constexpr (K % 3 == 0) vmem(IC<12>{}, IC<K / 3>{}); epi(IC<2>{}, K); SB(); });
    STAMP(19);
    sfor<12>([&](auto E) {
      constexpr int e = E;
      top(IC<13 + e>{});
      if constexpr (e == 0) sfor<8>([&](auto N) { rdw(IC<120 + N>{}); });
      sfor<10>([&](auto K) {
        constexpr int n = 10 * e + K;
        acc[n % 5] = mfma16(wf[120 + n], a5[n / 5], acc[n % 5]); tie(acc[n % 5]);
        if constexpr (n + 8 < 120) rdw(IC<128 + n>{});
        vmem(IC<13 + e>{}, K);
        SB();
      });
    });
    STAMP(20);
    const unsigned xs = L_BSRC + m * 5120;
    if (kh == 0) { *(LAS f32x4*)(L + xs + l16) = acc[2]; *(LAS f32x4*)(L + xs + 1024 + l16) = acc[3]; }
    else { *(LAS f32x4*)(L + xs + 2048 + l16) = acc[0]; *(LAS f32x4*)(L + xs + 3072 + l16) = acc[1]; *(LAS f32x4*)(L + xs + 4096 + l16) = acc[4]; }
    bar();
    f32x4 r0, r1, r2 = {};
    if (kh == 0) { r0 = acc[0] + ldsf4(L, xs + 2048 + l16); r1 = acc[1] + ldsf4(L, xs + 3072 + l16); r2 = acc[4] + ldsf4(L, xs + 4096 + l16); }
    else { r0 = acc[2] + ldsf4(L, xs + l16); r1 = acc[3] + ldsf4(L, xs + 1024 + l16); }
    const f32x4 g0 = ldsf4(L, L_MODV + 5440 + (32 * kh + 4 * g4) * 4), g1 = ldsf4(L, L_MODV + 5440 + (32 * kh + 16 + 4 * g4) * 4), g2 = ldsf4(L, L_MODV + 5440 + (64 + 4 * g4) * 4);
    GAS char* o = (GAS char*)P.out + (size_t)(16 * tt + i16) * 1280 + (80 * c8 + 4 * g4) * 2;
    *(GAS v2u*)(o + kh * 64) = v2u{pk2(bflo(x2k[0]) + g0[0] * r0[0], bfhi(x2k[0]) + g0[1] * r0[1]), pk2(bflo(x2k[1]) + g0[2] * r0[2], bfhi(x2k[1]) + g0[3] * r0[3])};
    *(GAS v2u*)(o + kh * 64 + 32) = v2u{pk2(bflo(x2k[2]) + g1[0] * r1[0], bfhi(x2k[2]) + g1[1] * r1[1]), pk2(bflo(x2k[3]) + g1[2] * r1[2], bfhi(x2k[3]) + g1[3] * r1[3])};
    if (kh == 0) *(GAS v2u*)(o + 128) = v2u{pk2(bflo(x2k[4]) + g2[0] * r2[0], bfhi(x2k[4]) + g2[1] * r2[1]), pk2(bflo(x2k[5]) + g2[2] * r2[2], bfhi(x2k[5]) + g2[3] * r2[3])};
    if (b == 0 && tid == 0) __hip_atomic_store((GAS unsigned*)(P.ws + CT_E), cbase + 1u, RLX_AGENT);
    STAMP(21);
  }
}

#if defined(__HIP_DEVICE_COMPILE__)
#pragma clang attribute pop
#endif
extern "C" void kernel_launch(void* const* d_in, const int* in_sizes, int n_in, void* d_out, int out_size, void* d_ws, size_t ws_size,
                              hipStream_t stream) {
  Params P;
  P.x = (const u16*)d_in[0]; P.wqkv = (const u16*)d_in[1]; P.wo = (const u16*)d_in[2];
  P.wg = (const u16*)d_in[4]; P.wu = (const u16*)d_in[5]; P.wd = (const u16*)d_in[6];
  P.c = (const u16*)d_in[7]; P.wmod = (const u16*)d_in[8];
  P.out = (u16*)d_out; P.ws = (char*)d_ws;
  fused_block<<<256, 512, 0, stream>>>(P);
}
```

```cpp
#include <hip/hip_runtime.h>
#include <hip/hip_bf16.h>
#include <utility>

typedef unsigned short u16;
typedef unsigned v4u __attribute__((ext_vector_type(4)));
typedef unsigned v2u __attribute__((ext_vector_type(2)));
typedef short v4s __attribute__((ext_vector_type(4)));
typedef float f32x2 __attribute__((ext_vector_type(2)));
typedef float f32x4 __attribute__((ext_vector_type(4)));
typedef float f32x16 __attribute__((ext_vector_type(16)));
typedef __bf16 bf16x2 __attribute__((ext_vector_type(2)));
typedef __bf16 bf16x8 __attribute__((ext_vector_type(8)));
typedef _Float16 f16x2 __attribute__((ext_vector_type(2)));
typedef __amdgpu_buffer_rsrc_t rsrc_t;
#define LAS __attribute__((address_space(3)))
#define GAS __attribute__((address_space(1)))
#define DI __device__ __forceinline__
#define RLX_AGENT __ATOMIC_RELAXED, __HIP_MEMORY_SCOPE_AGENT

#define DELAY0 20
#define R4 10
#define STAMPS 0

constexpr int WS_CTRL = 0;
constexpr int WS_MODP = 65536;
constexpr int WS_ST   = 131072;
constexpr int WS_UV   = 393216;
constexpr int WS_WT1  = 786432;
constexpr int WS_WT3  = WS_WT1 + 16 * 163840;
constexpr int WS_WT4  = WS_WT3 + 8 * 106496;
constexpr int WS_WT5  = WS_WT4 + 16 * 245760;
constexpr int WS_KV   = WS_WT5 + 8 * 245760;
constexpr int WS_OF   = WS_KV + 64 * 16 * 6144;
constexpr int WS_X2F  = WS_OF + 128 * 20 * 1024;
constexpr int WS_ACT  = WS_X2F + 64 * 40 * 1024;
constexpr int WS_XH   = WS_ACT + 128 * 48 * 1024;
constexpr int WS_END  = WS_XH + 64 * 40 * 1024;
constexpr int WS_DBG  = WS_END;
constexpr int CT_E = 0, CT_FLAG = 4096  , CT_C1 = 16384  , CT_C2 = 49152, CT_C3 = CT_C2 + 2048,
              CT_C4 = CT_C3 + 2048  , CT_CB = CT_C4 + 6144  , CT_CD = CT_CB + 1024  , CT_CM = CT_CD + 1024, CT_GO = 12288  ;
constexpr int L_WQ = 0, L_RED = 10240;
constexpr int L_BSRC = 122880;
constexpr int L_MODV = 155648;
constexpr int L_CNT = 162816;
constexpr int L_UVS = 155648;
constexpr int KVT = 6144;
constexpr int L_Q = 73728, L_QHI = 81920, L_RED2 = 112640  , L_W3A = 86016, L_GSRC = 122880  ;
constexpr unsigned SPIN_CAP = 1u << 20;

#if defined(__HIP_DEVICE_COMPILE__)
#pragma clang attribute push (__attribute__((target("no-packed-fp32-ops"))), apply_to = function)
#endif
struct Params { const u16 *x, *wqkv, *wo, *wg, *wu, *wd, *c, *wmod; u16* out; char* ws; };

DI f32x16 mfma32(v4u a, v4u b, f32x16 c) { return __builtin_amdgcn_mfma_f32_32x32x16_bf16(__builtin_bit_cast(bf16x8, a), __builtin_bit_cast(bf16x8, b), c, 0, 0, 0); }
DI f32x4 mfma16(v4u a, v4u b, f32x4 c) { return __builtin_amdgcn_mfma_f32_16x16x32_bf16(__builtin_bit_cast(bf16x8, a), __builtin_bit_cast(bf16x8, b), c, 0, 0, 0); }
DI unsigned pk2(float a, float b) { f32x2 v = {a, b}; return __builtin_bit_cast(unsigned, __builtin_convertvector(v, bf16x2)); }
DI float bflo(unsigned x) { return __builtin_bit_cast(float, x << 16); }
DI float bfhi(unsigned x) { return __builtin_bit_cast(float, x & 0xffff0000u); }
DI float bfr(float a) { return bflo(pk2(a, a)); }
DI f32x2 swp32(float v) {
  unsigned a = __builtin_bit_cast(unsigned, v), c = a; asm("" : "+v"(c));
  auto r = __builtin_amdgcn_permlane32_swap(a, c, false, false);
  unsigned r0 = r[0], r1 = r[1];
  return f32x2{__builtin_bit_cast(float, r0), __builtin_bit_cast(float, r1)};
}
DI f32x2 swp16(float v) {
  unsigned a = __builtin_bit_cast(unsigned, v), c = a; asm("" : "+v"(c));
  auto r = __builtin_amdgcn_permlane16_swap(a, c, false, false);
  unsigned r0 = r[0], r1 = r[1];
  return f32x2{__builtin_bit_cast(float, r0), __builtin_bit_cast(float, r1)};
}
DI float swap32_add(float v) { f32x2 r = swp32(v); return r.x + r.y; }
DI float swap16_add(float v) { f32x2 r = swp16(v); return r.x + r.y; }
DI float swap32_max(float v) { f32x2 r = swp32(v); return fmaxf(r.x, r.y); }
DI float swap32_hi(float v) { return swp32(v).y; }
DI bool all64(bool c) { return __builtin_amdgcn_ballot_w64(!c) == 0ull; }
DI float ror8_add(float v) { return v + __builtin_bit_cast(float, __builtin_amdgcn_update_dpp(0, __builtin_bit_cast(int, v), 0x128, 0xf, 0xf, true)); }

template <bool SC1> DI void dma(unsigned lds_addr, unsigned voff, rsrc_t rs, unsigned soff) {
  if (SC1) asm volatile("s_mov_b32 m0, %0\n\ts_nop 0\n\tbuffer_load_dwordx4 %1, %2, %3 offen sc1 lds" ::"s"(lds_addr), "v"(voff), "s"(rs), "s"(soff) : "memory");
  else     asm volatile("s_mov_b32 m0, %0\n\ts_nop 0\n\tbuffer_load_dwordx4 %1, %2, %3 offen lds" ::"s"(lds_addr), "v"(voff), "s"(rs), "s"(soff) : "memory");
}
template <int N, int LO, int GO> DI void dman(unsigned lb, unsigned voff, rsrc_t rs, unsigned gb) {
  unsigned t;
#define DMA_HEAD "s_add_u32 m0, %1, %5\n\ts_add_u32 %0, %4, %6\n\tbuffer_load_dwordx4 %2, %3, %0 offen sc1 lds"
#define DMA_MORE(o) "\n\tbuffer_load_dwordx4 %2, %3, %0 offen offset:" #o " sc1 lds"
#define DMA_ARGS : "=&s"(t) : "s"(lb), "v"(voff), "s"(rs), "s"(gb), "n"(LO), "n"(GO) : "memory", "scc"
  if constexpr (N == 1) asm volatile(DMA_HEAD DMA_ARGS);
  if constexpr (N == 2) asm volatile(DMA_HEAD DMA_MORE(1024) DMA_ARGS);
  if constexpr (N == 3) asm volatile(DMA_HEAD DMA_MORE(1024) DMA_MORE(2048) DMA_ARGS);
  if constexpr (N == 4) asm volatile(DMA_HEAD DMA_MORE(1024) DMA_MORE(2048) DMA_MORE(3072) DMA_ARGS);
}
template <int I> DI void ldf(v4u& d, unsigned voff, rsrc_t rs, unsigned base) {
  asm volatile("buffer_load_dwordx4 %0, %1, %2, %3 offen offset:%4 sc1" : "=v"(d) : "v"(voff), "s"(rs), "s"(base + (I / 4) * 4096), "n"((I % 4) * 1024) : "memory");
}
template <int O> DI void ldh(v2u& d, unsigned voff, rsrc_t rs, unsigned base) {
  asm volatile("buffer_load_dwordx2 %0, %1, %2, %3 offen offset:%4 sc1" : "=v"(d) : "v"(voff), "s"(rs), "s"(base), "n"(O) : "memory");
}
DI void peek(unsigned& d, const GAS unsigned* a) { asm volatile("global_load_dword %0, %1, off sc1" : "=v"(d) : "v"(a) : "memory"); }
DI void ld16(v4u& d, unsigned voff, rsrc_t rs, unsigned soff) { asm volatile("buffer_load_dwordx4 %0, %1, %2, %3 offen sc1" : "=v"(d) : "v"(voff), "s"(rs), "s"(soff) : "memory"); }
DI void ld16p(v4u& d, unsigned voff, rsrc_t rs, unsigned soff) { asm volatile("buffer_load_dwordx4 %0, %1, %2, %3 offen" : "=v"(d) : "v"(voff), "s"(rs), "s"(soff) : "memory"); }
DI void ld8p(v2u& d, unsigned voff, rsrc_t rs, unsigned soff) { asm volatile("buffer_load_dwordx2 %0, %1, %2, %3 offen" : "=v"(d) : "v"(voff), "s"(rs), "s"(soff) : "memory"); }
DI void ld2p(unsigned& d, unsigned voff, rsrc_t rs, unsigned soff) { asm volatile("buffer_load_ushort %0, %1, %2, %3 offen" : "=v"(d) : "v"(voff), "s"(rs), "s"(soff) : "memory"); }
DI void ld4(unsigned& d, unsigned voff, rsrc_t rs, unsigned soff) { asm volatile("buffer_load_dword %0, %1, %2, %3 offen sc1" : "=v"(d) : "v"(voff), "s"(rs), "s"(soff) : "memory"); }
template <int N> DI void waitv() { asm volatile("s_waitcnt vmcnt(%0)" ::"n"(N) : "memory"); }
template <class T> DI void tie(T& a) { asm volatile("" : "+v"(a)); }
DI void rbar() { asm volatile("s_barrier" ::: "memory"); }
DI void bar() { asm volatile("s_waitcnt lgkmcnt(0)\n\ts_barrier" ::: "memory"); }
DI void st16(v4u v, rsrc_t rs, unsigned off) { __builtin_amdgcn_raw_buffer_store_b128(v, rs, off, 0, 16); }
DI void st8(v2u v, rsrc_t rs, unsigned off) { __builtin_amdgcn_raw_buffer_store_b64(v, rs, off, 0, 16); }
DI v4u trpair(LAS char* L, unsigned a0, unsigned a1) {
  v4s lo = __builtin_amdgcn_ds_read_tr16_b64_v4i16((LAS v4s*)(L + a0));
  v4s hi = __builtin_amdgcn_ds_read_tr16_b64_v4i16((LAS v4s*)(L + a1));
  v2u l = __builtin_bit_cast(v2u, lo), h = __builtin_bit_cast(v2u, hi);
  return v4u{l.x, l.y, h.x, h.y};
}
DI v4u lds16(LAS char* L, unsigned a) { return *(LAS v4u*)(L + a); }
DI f32x4 ldsf4(LAS char* L, unsigned a) { return *(LAS f32x4*)(L + a); }

template <int... I, class F> DI void sfor_impl(std::integer_sequence<int, I...>, F&& f) { (f(std::integral_constant<int, I>{}), ...); }
template <int N, class F> DI void sfor(F&& f) { sfor_impl(std::make_integer_sequence<int, N>{}, f); }
#define SB() __builtin_amdgcn_sched_barrier(0)

template <int N> using IC = std::integral_constant<int, N>;
DI void affine4(unsigned x0, unsigned x1, f32x4 a, f32x4 s, unsigned& o0, unsigned& o1) {
  float t0, t1;
  const float a0 = a[0], a1 = a[1], a2 = a[2], a3 = a[3], s0 = s[0], s1 = s[1], s2 = s[2], s3 = s[3];
  asm volatile("v_fma_mix_f32 %0, %4, %6, %10 op_sel_hi:[1,0,0]\n\tv_fma_mix_f32 %2, %4, %7, %11 op_sel:[1,0,0] op_sel_hi:[1,0,0]\n\t"
      "v_fma_mix_f32 %1, %5, %8, %12 op_sel_hi:[1,0,0]\n\tv_fma_mix_f32 %3, %5, %9, %13 op_sel:[1,0,0] op_sel_hi:[1,0,0]\n\t"
      "v_cvt_pk_bf16_f32 %0, %0, %2\n\tv_cvt_pk_bf16_f32 %1, %1, %3"
      : "=&v"(o0), "=&v"(o1), "=&v"(t0), "=&v"(t1) : "v"(x0), "v"(x1), "v"(a0), "v"(a1), "v"(a2), "v"(a3), "v"(s0), "v"(s1), "v"(s2), "v"(s3));
}
constexpr int ring_off(int pc) { return (pc & 3) * 15360 + ((pc >> 2) % 15) * 1024; }
DI unsigned strm_off(int pc, int rounds) { return ((pc & 3) * rounds + (pc >> 2)) * 1024; }
DI float dot2(unsigned a, unsigned b, float c) { return __builtin_amdgcn_fdot2_f32_bf16(__builtin_bit_cast(bf16x2, a), __builtin_bit_cast(bf16x2, b), c, false); }
DI f32x2 xswp32(float a, float c) { auto r = __builtin_amdgcn_permlane32_swap(__builtin_bit_cast(unsigned, a), __builtin_bit_cast(unsigned, c), false, false); unsigned r0 = r[0], r1 = r[1]; return f32x2{__builtin_bit_cast(float, r0), __builtin_bit_cast(float, r1)}; }
DI f32x2 xswp16(float a, float c) { auto r = __builtin_amdgcn_permlane16_swap(__builtin_bit_cast(unsigned, a), __builtin_bit_cast(unsigned, c), false, false); unsigned r0 = r[0], r1 = r[1]; return f32x2{__builtin_bit_cast(float, r0), __builtin_bit_cast(float, r1)}; }

enum { OP_A, OP_W, OP_ST, OP_UV, OP_STORE, OP_PEEK, OP_A5, OP_MW, OP_MC };
struct Sched {
  int n, tot; int ty[128], ix[128], cn[128], pos[128], start[27], before[27];
  constexpr void put(int t, int i, int c) { ty[n] = t; ix[n] = i; cn[n] = c; pos[n] = tot; tot += c; ++n; }
  constexpr void puts(int t, int i, int c) { for (int k = 0; k < c; ++k) put(t, i + k, 1); }
  constexpr void putw(int r0, int r1) {
    while (r0 < r1) { int c = r1 - r0; if (c > 2) c = 2; if (r0 % 15 + c > 15) c = 15 - r0 % 15; put(OP_W, r0, c); r0 += c; } }
  constexpr void seg(int q) { start[q] = n; before[q] = tot; }
  constexpr int slot(int q, int K, int ns) const {
    int c = start[q + 1] - start[q]; if (c && ty[start[q + 1] - 1] == OP_STORE) --c;
    for (int k = 0; k < c; ++k) if ((k * ns) / c == K) return start[q] + k;
    return -1; }
  constexpr int at(int t, int i) const { for (int k = 0; k < n; ++k) if (ty[k] == t && i >= ix[k] && i < ix[k] + cn[k]) return pos[k] + i - ix[k]; return -1; }
};
constexpr int cmax(int a, int b) { return a > b ? a : b; }
constexpr Sched make_sched() {
  Sched s{};
  s.putw(0, R4); s.puts(OP_A, 0, 10); s.puts(OP_ST, 0, 4);
  const int wto[25] = {R4, R4 + 2, R4 + 5 > 16 ? 16 : R4 + 5, 19, 23, 27, 30, 32, 35, 37, 40, 42, 45, 45, 47, 49, 51, 53, 55, 57, 58, 60, 60, 60, 60};
  const int a5to[25] = {0, 0, 0, 0, 0, 0, 0, 0, 0, 0, 0, 4, 8, 11, 13, 15, 16, 16, 18, 20, 23, 24, 24, 24, 24};
  for (int q = 0; q < 25; ++q) {
    s.seg(q);
    if (q == 10 || q == 17) s.put(OP_PEEK, q == 10 ? 0 : 2, 0);
    if (q < 2) s.puts(OP_A, 10 + 5 * q, 5);
    if (q) s.puts(OP_A5, a5to[q - 1], a5to[q] - a5to[q - 1]);
    if (q) s.putw(wto[q - 1], wto[q]);
    if (q == 12) s.put(OP_PEEK, 1, 0);
    if (q == 6 || q == 9 || q == 12) s.put(OP_STORE, q == 6 ? 0 : q == 9 ? 1 : 2, 1);
  }
  s.seg(25);
  return s;
}
constexpr Sched SC = make_sched();
constexpr int sc_peek(int i) { for (int k = 0; k < SC.n; ++k) if (SC.ty[k] == OP_PEEK && SC.ix[k] == i) return SC.pos[k]; return 0; }
constexpr int sc_wait(int q) {
  int p = -1;
  if (q <= 11) p = SC.at(OP_W, (10 * (q + 1) + (q == 11 ? 0 : q % 4 == 2 ? 10 : 6) + 3) / 4 - 1);
  if (q >= 13) p = cmax(SC.at(OP_W, (10 * q + (q < 24 ? 10 : 0) + 3) / 4 - 1), SC.at(OP_A5, 2 * (q - 13) + 1));
  if (q < 4) p = cmax(p, SC.at(OP_A, 5 * q + 4));
  if (q == 8 || q == 11 || q == 14) p = cmax(p, SC.at(OP_STORE, q == 8 ? 0 : q == 11 ? 1 : 2));
  if (p < 0) return 63;
  return SC.before[q] - 1 - p;
}
constexpr Sched make_sched1() {
  Sched s{}; int a = 5, r = 7;
  s.putw(0, 4); s.puts(OP_A, 0, 3); s.putw(4, 7); s.puts(OP_A, 3, 2);
  for (int q = 0; q < 12; ++q) {
    s.seg(q);
    int na = q < 3 ? 3 : q < 6 ? 2 : 0, nw = q < 5 ? 2 : q < 8 ? 1 : 0;
    s.puts(OP_A, a, na); a += na; s.putw(r, r + nw); r += nw;
  }
  s.seg(12);
  return s;
}
constexpr Sched S1 = make_sched1();
constexpr int s1_first(int q) { return q <= 10 ? 6 * q : q == 11 ? 64 : 80; }
constexpr int s1_wait(int q) {
  int pc = s1_first(q + 1) + 6; if (pc > 80 || q == 11) pc = 80;
  int p = S1.at(OP_W, (pc + 3) / 4 - 1);
  if (q < 10) p = cmax(p, S1.at(OP_A, q < 9 ? 2 * q + 2 : 19));
  return S1.before[q] - 1 - p;
}

#define STAMP(i)

DI void poll(const GAS unsigned* addr, unsigned base, unsigned want, bool active) {
#pragma nounroll
  for (unsigned s = 0; s < SPIN_CAP; ++s) {
    unsigned v = base + want;
    if (active) v = __hip_atomic_load(addr, RLX_AGENT);
    if (all64((v - base) >= want)) break;
    __builtin_amdgcn_s_sleep(2);
  }
  __builtin_amdgcn_fence(__ATOMIC_ACQUIRE, "wavefront");
}
DI unsigned lds_arrive(LAS char* L, int off, int lane) {
  unsigned o = 0;
  asm volatile("s_waitcnt lgkmcnt(0)" ::: "memory");
  if (lane == 0) o = __hip_atomic_fetch_add((LAS unsigned*)(L + off), 1u, __ATOMIC_RELAXED, __HIP_MEMORY_SCOPE_WORKGROUP);
  o = __builtin_amdgcn_readfirstlane(o);
  asm volatile("" ::: "memory");
  return o;
}
template <int N> DI void mod_rows(const v4u* wm, const unsigned* cv, LAS char* red, int lane) {
  float acc[8] = {0, 0, 0, 0, 0, 0, 0, 0};
#pragma unroll
  for (int i = 0; i < N; ++i) {
    float cf = bflo(cv[i]);
    float sv = bfr(cf * __builtin_amdgcn_rcpf(1.f + __builtin_amdgcn_exp2f(-1.44269504f * cf)));
#pragma unroll
    for (int e = 0; e < 4; ++e) { acc[2 * e] += sv * bflo(wm[i][e]); acc[2 * e + 1] += sv * bfhi(wm[i][e]); }
  }
  float u[4], v[2];
#pragma unroll
  for (int e = 0; e < 4; ++e) { f32x2 r = xswp32(acc[e], acc[e + 4]); u[e] = r.x + r.y; }
#pragma unroll
  for (int e = 0; e < 2; ++e) { f32x2 r = xswp16(u[e], u[e + 2]); v[e] = ror8_add(r.x + r.y); }
  if (!(lane & 8)) *(LAS f32x2*)(red + (lane & 7) * 32 + (lane >> 4) * 8) = f32x2{v[0], v[1]};
}
DI void lds_wait(LAS char* L, int off, unsigned want) {
#pragma nounroll
  for (unsigned s = 0; s < SPIN_CAP && *(volatile LAS unsigned*)(L + off) < want; ++s) __builtin_amdgcn_s_sleep(1);
  asm volatile("" ::: "memory");
}

__global__ __launch_bounds__(512, 2) void fused_block(Params P) {
  __shared__ __attribute__((aligned(16))) char lds_raw[163840];
  LAS char* L = (LAS char*)lds_raw;
  const unsigned LB = (unsigned)(size_t)L;
  const int tid = threadIdx.x, lane = tid & 63;
  const int w = __builtin_amdgcn_readfirstlane(tid >> 6);
  const int b = blockIdx.x;
  const int p = 4 * ((b & 7) >> 1) + ((b >> 3) & 3), j = 8 * (b & 1) + (b >> 5);
  const int m = w & 3, kh = w >> 2, h = lane >> 5, r32 = lane & 31;
  const int g4 = lane >> 4, i16 = lane & 15, q4 = (lane & 15) >> 2, p4 = lane & 3, blk = (lane >> 4) & 1;
  const rsrc_t rws = __builtin_amdgcn_make_buffer_rsrc((void*)P.ws, 0, WS_END, 0x00020000);
  const rsrc_t rx = __builtin_amdgcn_make_buffer_rsrc((void*)P.x, 0, 2048 * 1280, 0x00020000);
  const unsigned l16 = lane * 16;
  const unsigned ringL = kh * 61440 + l16, ringD = LB + kh * 61440 + m * 15360;
  STAMP(0);
  unsigned long long pc0; asm volatile("s_getpc_b64 %0" : "=s"(pc0));
  unsigned cbase = 0;
  const GAS unsigned* caddr = (const GAS unsigned*)P.ws;
  const int c40 = b % 40, c20 = (b - 160) % 20;
  const int cc = b < 160 ? (c40 < 20 ? c40 : c40 + 10) : (c20 < 10 ? 20 + c20 : 40 + c20), kq = b < 160 ? b / 40 : ((b - 160) / 20) & 3;
  const char* wm0 = (const char*)P.wmod + (size_t)(160 * kq) * 7680 + cc * 128;
  {
    const int bj = b < 240 ? b : 0, s = bj / 6, q = bj % 6;
    const int lr = (lane * 205) >> 11, lc = lane - 10 * lr;
    const bool lo = w < 4;
    v4u t[8]; unsigned cv[5], dst[2];
    {
      const char* px = (const char*)P.x + (size_t)b * 10240;
      const rsrc_t r1 = __builtin_amdgcn_make_buffer_rsrc((void*)(lo ? wm0 : px), 0, lo && b >= 160 ? 0 : 0x200000, 0x00020000);
      const rsrc_t r2 = __builtin_amdgcn_make_buffer_rsrc((void*)((const char*)P.wqkv + (size_t)(16 * s) * 3840 + q * 640 + m * 160), 0, lo ? 0x10000 : 0, 0x00020000);
      const rsrc_t rc = __builtin_amdgcn_make_buffer_rsrc((void*)(P.c + 160 * kq), 0, lo && b < 160 ? 320 : 0, 0x00020000);
      const unsigned v1 = lo ? (tid >> 3) * 7680 + (tid & 7) * 16 : (lane & 7) * 1280 + (lane >> 3) * 16 + m * 128, st1 = lo ? 245760 : 512;
      const unsigned v2 = (lr < 6 ? lr : 5) * 3840 + lc * 16;
      ld16p(t[0], v1, r1, 0); ld16p(t[1], v1, r1, st1); ld16p(t[2], v1, r1, w < 6 ? 2 * st1 : 0x7ff00000u);
      if (lo) {
        ld16p(t[3], v1, r1, 3 * st1); ld16p(t[4], v1, r1, 4 * st1);
        ld16p(t[5], v2, r2, 0); ld16p(t[6], v2, r2, 23040); ld16p(t[7], lr >= 4 ? v2 - 7680 : v2, r2, 46080);
        sfor<5>([&](auto I) { ld2p(cv[I], (tid >> 3) * 2, rc, I * 64); });
      } else if (w >= 6) {
        const rsrc_t ro = __builtin_amdgcn_make_buffer_rsrc((void*)P.wo, 0, 640 * 1280, 0x00020000);
#pragma unroll
        for (int u = 0; u < 2; ++u) {
          const int sl = 2 * (w - 6) + u, f = sl < 3 ? 3 * b + sl : 768 + (b & 31), cw = f / 100, st = (f % 100) / 5, tl = f % 5;
          const int e = (lane >> 1) & 7;
          const int ch = st < 16 ? 40 * st + 20 * (g4 >> 1) + 4 * (g4 & 1) + (e < 4 ? e : e + 4) : 40 * (4 * (st - 16) + g4) + 16 + (e < 4 ? e : e + 16);
          dma<false>(LB + 16384 + sl * 1024, ch * 1280 + (80 * cw + 16 * tl) * 2 + (lane & 1) * 16, ro, 0);
          dst[u] = WS_WT3 + cw * 106496 + (st & 1) * 53248 + strm_off((st >> 1) * 5 + tl, 13) + l16;
        }
      } else {
        ld16(t[3], l16, rws, b < 8 && w == 5 ? CT_FLAG + b * 1024 : 0x7ff00000);
        ld16p(t[4], l16, __builtin_amdgcn_make_buffer_rsrc((void*)(pc0 & ~255ull), 0, 34816, 0x00020000), (w == 5 ? 0 : 32768) + (b >> 3) * 1024);
      }
    }
    if (w == 4) {
      int off = CT_E;
      if (lane == 1) off = CT_C1 + (((p >= 1 ? p - 1 : 0) * 16 + j) << 7);
      if (lane == 2) off = CT_C1 + (((p >= 2 ? p - 2 : 0) * 16 + j) << 7);
      if (lane == 3) off = CT_C2 + (p << 7);
      if (lane == 4) off = CT_C3 + (p << 7);
      if (lane == 5) off = CT_C4 + p * 384;
      if (lane == 16 || lane == 17) off = CT_C4 + p * 384 + (lane - 15) * 128;
      if (lane == 7) off = CT_CM;
      if (lane >= 8 && lane < 16) off = CT_CB + ((lane - 8) << 7);
      caddr = (const GAS unsigned*)(P.ws + off);
    }
    peek(cbase, caddr);
    if (tid < 2) *(LAS v4u*)(L + L_CNT + tid * 16) = v4u{0u, 0u, 0u, 0u};
    bar();
    waitv<0>();
    sfor<8>([&](auto I) { tie(t[I]); });
    sfor<5>([&](auto I) { tie(cv[I]); });
    tie(cbase);
    if (lo) {
      const unsigned wql = L_WQ + w * 2560;
      if (lr < 6) { *(LAS v4u*)(L + wql + l16) = t[5]; *(LAS v4u*)(L + wql + 960 + l16) = t[6]; }
      if (lr < 4) *(LAS v4u*)(L + wql + 1920 + l16) = t[7];
      if (b < 160) {
        mod_rows<5>(t, cv, L + L_RED + w * 256, lane);
        if (lds_arrive(L, L_CNT, lane) == 3 && lane < 16) {
          f32x4 u = (ldsf4(L, L_RED + l16) + ldsf4(L, L_RED + 256 + l16)) + (ldsf4(L, L_RED + 512 + l16) + ldsf4(L, L_RED + 768 + l16));
          st16(__builtin_bit_cast(v4u, u), rws, WS_MODP + (kq * 3840 + cc * 64 + lane * 4) * 4);
        }
      }
      if (b < 240) {
        asm volatile("s_waitcnt lgkmcnt(0)" ::: "memory");
        const int part = q >> 1, jb = 8 * (q & 1) + 2 * w;
        const int sg = s >= 20 ? s - 20 : s, pk0 = 3 * (sg & ~1);
        const unsigned dst0 = WS_WT1 + jb * 163840 + (s >= 20 ? 81920 : 0);
        const unsigned dmain = dst0 + strm_off(part == 0 ? 60 + sg : pk0 + 2 * (sg & 1) + part - 1, 20), dmix = dst0 + strm_off(pk0 + 4 + (sg & 1), 20);
        const unsigned rowa = wql + (8 * h + q4) * 160;
        const unsigned a = rowa + (20 * blk + 4 * p4) * 2, a2 = rowa + (40 * blk + 16 + 20 * (p4 & 1)) * 2;
        v4u f0 = trpair(L, a, a + 640), f1 = trpair(L, a + 80, a + 720), f2 = trpair(L, a2, a2 + 640);
        st16(f0, rws, dmain + l16);
        st16(f1, rws, dmain + 163840 + l16);
        if (i16 >= 8) f2 = v4u{0, 0, 0, 0};
        const int r = (part == 0 ? 16 : part == 1 ? 0 : 8) + i16;
        if (part == 0 || i16 < 8) st16(f2, rws, dmix + blk * 163840 + (h * 32 + r) * 16);
      }
    } else {
      float s1 = 0.f, s2 = 0.f, s3 = 0.f, s4 = 0.f;
#pragma unroll
      for (int i = 0; i < 3; ++i) {
        s1 = dot2(t[i][0], 0x3f803f80u, s1); s2 = dot2(t[i][0], t[i][0], s2); s3 = dot2(t[i][1], 0x3f803f80u, s3); s4 = dot2(t[i][1], t[i][1], s4);
        s1 = dot2(t[i][2], 0x3f803f80u, s1); s2 = dot2(t[i][2], t[i][2], s2); s3 = dot2(t[i][3], 0x3f803f80u, s3); s4 = dot2(t[i][3], t[i][3], s4);
      }
      s1 = swap32_add(swap16_add(ror8_add(s1 + s3))); s2 = swap32_add(swap16_add(ror8_add(s2 + s4)));
      if (lane < 8) *(LAS f32x2*)(L + L_RED + 1024 + lane * 32 + m * 8) = f32x2{s1, s2};
      lds_arrive(L, L_CNT + 8, lane);
      if (w >= 6) {
#pragma unroll
        for (int u = 0; u < 2; ++u) {
          const unsigned a = 16384 + (2 * (w - 6) + u) * 1024 + (8 * g4 + q4) * 32 + p4 * 8;
          st16(trpair(L, a, a + 128), rws, dst[u]);
        }
      }
      lds_wait(L, L_CNT + 8, 4u);
      const f32x4 o0 = ldsf4(L, L_RED + 1024 + (lane & 7) * 32), o1 = ldsf4(L, L_RED + 1040 + (lane & 7) * 32);
      const float mean = ((o0[0] + o0[2]) + (o1[0] + o1[2])) * (1.f / 640.f);
      const float rstd = __builtin_amdgcn_rsqf(fmaxf(((o0[1] + o0[3]) + (o1[1] + o1[3])) * (1.f / 640.f) - mean * mean, 0.f) + 1e-5f), nm = -mean * rstd;
      const int tok = 8 * b + (lane & 7);
      const unsigned xo = WS_XH + (tok >> 5) * 40960 + m * 4096 + (lane >> 3) * 512 + (tok & 31) * 16;
#pragma unroll
      for (int i = 0; i < 3; ++i) {
        if (i == 2 && w >= 6) break;
        v4u o;
#pragma unroll
        for (int e = 0; e < 4; ++e) { f32x2 v = {fmaf(bflo(t[i][e]), rstd, nm), fmaf(bfhi(t[i][e]), rstd, nm)}; o[e] = __builtin_bit_cast(unsigned, __builtin_convertvector(v, f16x2)); }
        st16(o, rws, xo + i * 16384);
      }
    }
  }
  waitv<0>();
  if (lds_arrive(L, L_CNT + 4, lane) == 7 && lane < 8) __hip_atomic_store((GAS unsigned*)(P.ws + CT_FLAG + lane * 1024 + b * 4), __builtin_amdgcn_readfirstlane(cbase) + 1u, RLX_AGENT);
  STAMP(1);
  const int c8 = __builtin_amdgcn_readfirstlane(4 * (b & 1) + ((b >> 5) & 3)), rh = b >> 7;
  const int kp = (b >> 4) & 7;
  int col = tid < 320 ? 16 * tid : tid < 340 ? (2560 + 80 * kp + 4 * (tid - 320)) * 4 : tid < 360 ? (1920 + 80 * kp + 4 * (tid - 340)) * 4 : 0;
  unsigned mdst = L_MODV + (tid < 160 ? tid * 32 + 16 : tid < 320 ? (tid - 160) * 32 : 5760 + (tid - 320) * 16);
  const rsrc_t rb = __builtin_amdgcn_make_buffer_rsrc((void*)((const char*)((b >> 7) ? P.wu : P.wg) + (size_t)(80 * kp) * 3072 + (b & 15) * 192), 0, 80 * 3072, 0x00020000);
  tie(col); tie(mdst);
  const bool mod2 = b >= 160 && b < 240;
  v4u m2w[3]; unsigned m2c[3];
  __builtin_amdgcn_s_sleep(DELAY0);
  const int v6 = w < 4 ? w : w - 1;
  if (w == 7) {
    const rsrc_t rd = __builtin_amdgcn_make_buffer_rsrc((void*)((const char*)P.wd + (b / 5) * 40960 + (b % 5) * 256), 0, b < 240 ? 40960 : 0, 0x00020000);
    const int e = (lane >> 1) & 7;
    const unsigned vo = (16 * (g4 >> 1) + 4 * (g4 & 1) + (e < 4 ? e : e + 4)) * 1280 + (lane & 1) * 16;
    sfor<8>([&](auto U) { dma<false>(LB + L_GSRC + (18 + U) * 1024, vo, rd, U * 32); });
  } else if (w != 4) {
    const int e = (lane >> 2) & 7;
#pragma unroll
    for (int u = 0; u < 3; ++u) {
      const int sg = (v6 & 1) * 3 + u;
      const int k = sg < 4 ? 32 * (sg >> 1) + 4 * (2 * (sg & 1) + h) + (e < 4 ? e : e + 12) : 64 + 8 * h + e;
      if (sg < 5) dma<false>(LB + L_GSRC + (3 * v6 + u) * 1024, k * 3072 + (v6 >> 1) * 64 + p4 * 16, rb, 0);
    }
  }
  {
    const rsrc_t rm = __builtin_amdgcn_make_buffer_rsrc((void*)wm0, 0, mod2 ? 160 * 7680 : 0, 0x00020000);
    const rsrc_t rc = __builtin_amdgcn_make_buffer_rsrc((void*)(P.c + 160 * kq), 0, mod2 ? 320 : 0, 0x00020000);
    sfor<3>([&](auto I) { ld16p(m2w[I], (tid >> 3) * 7680 + (tid & 7) * 16, rm, I * 491520); ld2p(m2c[I], (tid >> 3) * 2, rc, I * 128); });
  }
  if (w == 4) {
    const unsigned tag = __builtin_amdgcn_readfirstlane(cbase) + 1u;
    GAS unsigned* go = (GAS unsigned*)(P.ws + CT_GO + (b & 7) * 128);
    if (b < 8) {
#pragma nounroll
      for (unsigned s = 0; s < SPIN_CAP; ++s) {
        v4u f; ld16(f, l16, rws, CT_FLAG + b * 1024); waitv<0>(); tie(f);
        if (all64(f.x == tag && f.y == tag && f.z == tag && f.w == tag)) break;
      }
      if (lane == 0) __hip_atomic_store(go, tag, RLX_AGENT);
    } else {
#pragma nounroll
      for (unsigned s = 0; s < SPIN_CAP && __hip_atomic_load(go, RLX_AGENT) != tag; ++s) __builtin_amdgcn_s_sleep(1);
    }
    __builtin_amdgcn_fence(__ATOMIC_ACQUIRE, "wavefront");
  }
  bar();
  STAMP(4);

  auto mod_put = [&](v4u* mp, bool isc, bool on, unsigned dst) {
    sfor<4>([&](auto I) { tie(mp[I]); });
    f32x4 v = (__builtin_bit_cast(f32x4, mp[0]) + __builtin_bit_cast(f32x4, mp[1])) + (__builtin_bit_cast(f32x4, mp[2]) + __builtin_bit_cast(f32x4, mp[3]));
#pragma unroll
    for (int e = 0; e < 4; ++e) { float t = bfr(v[e]); v[e] = isc ? bfr(1.f + t) : t; }
    if (on) *(LAS f32x4*)(L + dst) = v;
  };
  {
    v4u mp[4], xf[20];
    sfor<4>([&](auto I) { ld16(mp[I], col, rws, WS_MODP + I * 15360); });
    const unsigned w1 = WS_WT1 + j * 163840 + kh * 81920 + m * 20480;
    const unsigned ab = WS_XH + ((4 * p + m) * 40 + 20 * kh) * 1024;
    rbar();
    dman<4, 0, 0>(ringD, l16, rws, w1);
    sfor<3>([&](auto I) { ldf<I>(xf[I], l16, rws, ab); });
    waitv<7>();
    mod_put(mp, tid >= 160 && tid < 340, tid < 360, mdst);
    dman<3, 4096, 4096>(ringD, l16, rws, w1);
    ldf<3>(xf[3], l16, rws, ab); ldf<4>(xf[4], l16, rws, ab);
    STAMP(5);
    f32x16 acc[4] = {};
    const unsigned wbK = ringL + kh * 15360, wbS = ringL + 15360 - kh * 15360;
    unsigned cb = L_MODV + (80 * kh + 2 * h) * 32; tie(cb);
    unsigned xw = kh * 61440 + m * 15360 + 5120 + l16, xr = (kh ^ 1) * 61440 + m * 15360 + 5120 + l16;
    v4u wf[80], hf[20], c0, c1, c2; f32x4 ca[40], cs[40], eo[6];
    const unsigned kvg = WS_KV + ((4 * p + m) * 16 + j) * 6144, kvl = (8 + m) * KVT;
    auto rdw = [&](auto N) {
      constexpr int n = N < 60 ? 3 * (N / 3) + 2 - N % 3 : (N ^ 1), i = n / 3, pk0 = 3 * (i & ~1);
      if constexpr (n >= 60) wf[n] = lds16(L, ringL + ring_off(n));
      else if constexpr (n % 3 == 1) wf[n] = lds16(L, ringL + ring_off(pk0 + 4 + (i & 1)));
      else wf[n] = lds16(L, (n % 3 == 0 ? wbS : wbK) + ring_off(pk0 + 2 * (i & 1)));
    };
    auto rdc = [&](auto R) { ca[R] = ldsf4(L, cb + (R / 2) * 128 + (R & 1) * 32); cs[R] = ldsf4(L, cb + (R / 2) * 128 + (R & 1) * 32 + 16); };
    auto aff = [&](auto I, auto E) { constexpr int e = 2 * E; unsigned o0, o1; affine4(xf[I][e], xf[I][e + 1], ca[2 * I + E], cs[2 * I + E], o0, o1); hf[I][e] = o0; hf[I][e + 1] = o1; };
    auto quad = [&](const f32x16& a, int q) { return f32x4{a[4 * q], a[4 * q + 1], a[4 * q + 2], a[4 * q + 3]}; };
    sfor<12>([&](auto Q) {
      constexpr int q = Q, n0 = s1_first(q);
      waitv<s1_wait(q)>();
      if constexpr (q == 0) tie(xf[0]);
      if constexpr (q < 10) { tie(xf[2 * q + 1]); if constexpr (q < 9) tie(xf[2 * q + 2]); }
      if constexpr (q == 0 || q == 11) bar(); else rbar();
      if constexpr (q == 0) { sfor<6>(rdc); sfor<6>(rdw); aff(IC<0>{}, IC<0>{}); aff(IC<0>{}, IC<1>{}); asm volatile("s_nop 1"); SB(); }
      sfor<s1_first(q + 1) - n0>([&](auto K) {
        constexpr int n = n0 + K, i = n < 60 ? n / 3 : n - 60, t = n < 60 ? n % 3 : 3, k = S1.slot(q, K, 6);
        acc[t] = mfma32(wf[n], hf[i], acc[t]); tie(acc[t]);
        SB();
        if constexpr (t < 2 && i + 1 < 20) aff(IC<i + 1>{}, IC<t>{});
        if constexpr (t < 2 && i + 3 < 20) rdc(IC<2 * (i + 3) + t>{});
        if constexpr (n + 6 < 80) rdw(IC<n + 6>{});
        if constexpr (k >= 0) {
          constexpr int ty = S1.ty[k], ix = S1.ix[k];
          if constexpr (ty == OP_A) ldf<ix>(xf[ix], l16, rws, ab);
          else if constexpr (ty == OP_W) dman<S1.cn[k], (ix % 15) * 1024, ix * 1024>(ringD, l16, rws, w1);
        }
        if constexpr (n >= 59 && n < 63) *(LAS f32x4*)(L + xw + (n - 59) * 1024) = quad(acc[0], n - 59);
        if constexpr (n == 63) { if (kh) { *(LAS f32x4*)(L + xw + 4096) = quad(acc[1], 0); *(LAS f32x4*)(L + xw + 5120) = quad(acc[1], 1); } }
        if constexpr (n == 64) sfor<6>([&](auto I) { eo[I] = ldsf4(L, xr + I * 1024); });
        if constexpr (n == 65 || n == 66) {
          constexpr int o = 8 * (n - 65);
          v4u c = {pk2(acc[2][o] + eo[o / 4][0], acc[2][o + 1] + eo[o / 4][1]), pk2(acc[2][o + 2] + eo[o / 4][2], acc[2][o + 3] + eo[o / 4][3]),
                   pk2(acc[2][o + 4] + eo[o / 4 + 1][0], acc[2][o + 5] + eo[o / 4 + 1][1]), pk2(acc[2][o + 6] + eo[o / 4 + 1][2], acc[2][o + 7] + eo[o / 4 + 1][3])};
          st16(c, rws, kvg + (kh ? 3072 + r32 * 32 + h * 16 : l16) + (n - 65) * 1024);
          if constexpr (n == 65) c0 = c; else c1 = c;
        }
        if constexpr (n == 67) {
          c2 = v4u{pk2(acc[1][0] + eo[4][0], acc[1][1] + eo[4][1]), pk2(acc[1][2] + eo[4][2], acc[1][3] + eo[4][3]),
                   pk2(acc[1][4] + eo[5][0], acc[1][5] + eo[5][1]), pk2(acc[1][6] + eo[5][2], acc[1][7] + eo[5][3])};
          if (!kh) { st16(v4u{c2.x, c2.y, 0u, 0u}, rws, kvg + 2048 + l16); st16(v4u{c2.z, c2.w, 0x3f80u, 0u}, rws, kvg + 5120 + l16); }
        }
        SB();
      });
    });
    STAMP(6);
    const unsigned xq = 5120 + (m >> 1) * 15360 + (m & 1) * 5120 + l16;
    if (!kh) {
#pragma unroll
      for (int qd = 0; qd < 4; ++qd) *(LAS f32x4*)(L + xq + qd * 1024) = quad(acc[3], qd);
      *(LAS f32x4*)(L + xq + 4096) = quad(acc[1], 2);
    }
    if (mod2) {
      sfor<3>([&](auto I) { tie(m2w[I]); tie(m2c[I]); });
      mod_rows<3>(m2w, m2c, L + L_RED2 + w * 256, lane);
    }
    waitv<0>();
    bar();
    if (tid == 0) __hip_atomic_fetch_add((GAS unsigned*)(P.ws + CT_C1 + ((p * 16 + j) << 7)), 1u, RLX_AGENT);
    {
      const unsigned lo = kh ? 3072 + r32 * 48 + h * 16 : l16, ls = kh ? 1536 : 1024;
      *(LAS v4u*)(L + kvl + lo) = c0; *(LAS v4u*)(L + kvl + lo + ls) = c1;
    }
    if (kh == 0) {
      *(LAS v4u*)(L + kvl + 2048 + l16) = v4u{c2.x, c2.y, 0u, 0u};
      *(LAS v4u*)(L + kvl + 3072 + h * 1536 + r32 * 48 + 32) = v4u{c2.z, c2.w, 0x3f80u, 0u};
    } else {
      const float qs = 0.22360679775f * 1.44269504089f;
      float qv[20];
#pragma unroll
      for (int qd = 0; qd < 5; ++qd) {
        const f32x4 u = ldsf4(L, xq + qd * 1024);
#pragma unroll
        for (int e = 0; e < 4; ++e) qv[4 * qd + e] = ((qd < 4 ? acc[3][4 * qd + e] : acc[1][8 + e]) + u[e]) * qs;
      }
      *(LAS v4u*)(L + L_Q + m * 2048 + l16) = v4u{pk2(qv[0], qv[1]), pk2(qv[2], qv[3]), pk2(qv[4], qv[5]), pk2(qv[6], qv[7])};
      *(LAS v4u*)(L + L_Q + m * 2048 + 1024 + l16) = v4u{pk2(qv[8], qv[9]), pk2(qv[10], qv[11]), pk2(qv[12], qv[13]), pk2(qv[14], qv[15])};
      *(LAS v2u*)(L + L_QHI + m * 512 + lane * 8) = v2u{pk2(qv[16], qv[17]), pk2(qv[18], qv[19])};
    }
  if (mod2 && w == 3 && lane < 16) {
    f32x4 u = ldsf4(L, L_RED2 + l16);
#pragma unroll
    for (int i = 1; i < 8; ++i) u += ldsf4(L, L_RED2 + i * 256 + l16);
    st16(__builtin_bit_cast(v4u, u), rws, WS_MODP + (kq * 3840 + cc * 64 + lane * 4) * 4);
  }
    STAMP(7);
  }
  unsigned vg[3];
#pragma unroll
  for (int i = 0; i < 3; ++i) { const int c = 64 * i + lane, rw = (c * 171) >> 9, pt = c - 3 * rw; vg[i] = pt < 2 ? 3072 + rw * 32 + pt * 16 : 5120 + rw * 16; tie(vg[i]); }
  if (w == 4) poll(caddr, cbase, 1u, (lane == 1 && p >= 1) || (lane == 2 && p >= 2));
  else if (w == 7) {
    if (b < 240) {
      const int st0 = b / 5, st = 16 * (st0 % 3) + st0 / 3, ct0 = 8 * (b % 5);
      const unsigned a = L_GSRC + 18432 + (8 * g4 + q4) * 32 + p4 * 8, d0 = WS_WT5 + (st & 1) * 122880 + l16;
#pragma unroll
      for (int u = 0; u < 8; ++u) { const int ct = ct0 + u; st16(trpair(L, a + u * 1024, a + u * 1024 + 128), rws, d0 + (ct / 5) * 245760 + strm_off((st >> 1) * 5 + ct % 5, 30)); }
    }
  } else {
    const int mat = b >> 7, jd = b & 15, t = 2 * (v6 >> 1) + blk;
    const unsigned d0 = WS_WT4 + jd * 245760 + (kp >> 2) * 122880 + blk * 30720 + (h * 32 + mat * 16 + i16) * 16;
    float us = 0.f, vs = 0.f;
#pragma unroll
    for (int u = 0; u < 3; ++u) {
      const int sg = (v6 & 1) * 3 + u;
      if (sg == 5) break;
      const int k0 = sg < 4 ? 32 * (sg >> 1) + 4 * (2 * (sg & 1) + h) : 64 + 8 * h, k1 = sg < 4 ? k0 + 16 : k0 + 4;
      const unsigned ca = L_GSRC + (3 * v6 + u) * 1024 + (8 * h + q4) * 64 + blk * 32 + p4 * 8;
      v4u f = trpair(L, ca, ca + 256);
      f32x4 a0 = ldsf4(L, L_MODV + 5760 + k0 * 4), a1 = ldsf4(L, L_MODV + 5760 + k1 * 4);
      f32x4 b0 = ldsf4(L, L_MODV + 6080 + k0 * 4), b1 = ldsf4(L, L_MODV + 6080 + k1 * 4);
      v4u o;
      o[0] = pk2(bflo(f[0]) * a0[0], bfhi(f[0]) * a0[1]); o[1] = pk2(bflo(f[1]) * a0[2], bfhi(f[1]) * a0[3]);
      o[2] = pk2(bflo(f[2]) * a1[0], bfhi(f[2]) * a1[1]); o[3] = pk2(bflo(f[3]) * a1[2], bfhi(f[3]) * a1[3]);
      vs += bflo(f[0]) * b0[0] + bfhi(f[0]) * b0[1] + bflo(f[1]) * b0[2] + bfhi(f[1]) * b0[3] + bflo(f[2]) * b1[0] + bfhi(f[2]) * b1[1] + bflo(f[3]) * b1[2] + bfhi(f[3]) * b1[3];
      us = dot2(o[0], 0x3f803f80u, us); us = dot2(o[1], 0x3f803f80u, us); us = dot2(o[2], 0x3f803f80u, us); us = dot2(o[3], 0x3f803f80u, us);
      st16(o, rws, d0 + strm_off((v6 >> 1) * 40 + (5 * (kp & 3) + sg) * 2, 30));
    }
    us = swap32_add(us); vs = swap32_add(vs);
    const int row = (jd * 6 + t) * 32 + mat * 16 + i16;
    __builtin_amdgcn_raw_buffer_store_b32(__builtin_bit_cast(unsigned, h ? vs : us), rws, WS_UV + (((kp * 2 + (v6 & 1)) * 2 + h) * 3072 + row) * 4, 0, 16);
  }
  bar();
  STAMP(8);
  const int gt = 4 * p - 8 + w;
  {
    const unsigned ko = gt >= 0 ? WS_KV + (gt * 16 + j) * 6144 : 0x7ff00000u, lo = LB + w * KVT;
    dman<3, 0, 0>(lo, l16, rws, ko);
    sfor<3>([&](auto I) { dman<1, 3072 + I * 1024, 0>(lo, vg[I], rws, ko); });
  }
  STAMP(9);
  const unsigned w3 = WS_WT3 + c8 * 106496 + kh * 53248 + m * 13312;
  unsigned cbv;
  {
    waitv<0>();
    if (gt < 0) *(LAS unsigned*)(L + w * KVT + 2056 + l16) = 0xfe96u;
    bar();
    if (tid == 0 && mod2) __hip_atomic_fetch_add((GAS unsigned*)(P.ws + CT_CM), 1u, RLX_AGENT);
    if (tid == 64) __hip_atomic_fetch_add((GAS unsigned*)(P.ws + CT_CB + ((b & 7) << 7)), 1u, RLX_AGENT);
    STAMP(10);
    const int hd = kh;
    const v4u q0 = lds16(L, L_Q + m * 2048 + hd * 1024 + l16);
    v2u qh = *(LAS v2u*)(L + L_QHI + m * 512 + lane * 8);
    if (h != hd) qh = v2u{0, 0};
    const v4u q1 = {qh.x, qh.y, 0x3f80u, 0};
    const f32x16 zero = {};
    const unsigned ka = m * KVT + hd * 1024 + l16, kb = m * KVT + 2048 + l16;
    v4u k0[9], k1[9];
    sfor<9>([&](auto I) { constexpr int i = I == 0 ? 0 : I == 1 ? 8 : I - 1; k0[i] = lds16(L, ka + i * KVT); k1[i] = lds16(L, kb + i * KVT); });
    dman<4, L_W3A, 0>(LB + kh * 32768 + m * 8192, l16, rws, w3);
    dman<4, L_W3A + 4096, 4096>(LB + kh * 32768 + m * 8192, l16, rws, w3);
    auto qk = [&](auto I, f32x16 c) { f32x16 r = mfma32(k1[I], q1, mfma32(k0[I], q0, c)); tie(r); return r; };
    auto vmax = [](float a, float c) { return __builtin_elementwise_maximum(a, c); };
    auto tmax = [&](const f32x16& v, float t) {
#pragma unroll
      for (int r = 0; r < 16; r += 2) t = vmax(vmax(v[r], v[r + 1]), t);
      tie(t); return t; };
    f32x16 Sm, S[8];
    float mx = -3.0e38f;
    {
      const f32x16 s0 = qk(IC<0>{}, zero), s8 = qk(IC<8>{}, zero);
      S[1] = qk(IC<1>{}, zero); SB();
#pragma unroll
      for (int r = 0; r < 16; ++r) Sm[r] = (r & 3) + 8 * (r >> 2) + 4 * h <= r32 ? s8[r] : s0[r];
      tie(Sm); SB();
    }
    sfor<7>([&](auto I) {
      if constexpr (I < 6) S[I + 2] = qk(IC<I + 2>{}, zero);
      SB();
      if constexpr (I == 0) mx = tmax(Sm, mx); else mx = tmax(S[I], mx);
      SB();
    });
    const unsigned va = m * KVT + 3072 + hd * 1536 + (4 * h + q4) * 48 + (blk == 0 ? (p4 & 1) * 16 + (p4 >> 1) * 8 : (p4 ? 40 : 32));
    v4u vf[18];
    auto rdv = [&](auto I) { vf[2 * I + 1] = trpair(L, va + I * KVT + 768, va + I * KVT + 1152); vf[2 * I] = trpair(L, va + I * KVT, va + I * KVT + 384); };
    rdv(IC<8>{}); rdv(IC<0>{}); SB();
    mx = tmax(S[7], mx);
    { f32x2 t = swp32(mx); mx = vmax(t.x, t.y); }
    f32x16 negm;
#pragma unroll
    for (int r = 0; r < 16; ++r) negm[r] = -mx;
    tie(negm);
    f32x16 O = {};
    S[1] = qk(IC<1>{}, negm); rdv(IC<1>{}); SB();
#pragma unroll
    for (int s = 0; s < 2; ++s) {
      v4u p0, p8;
#pragma unroll
      for (int e = 0; e < 4; ++e) {
        const int r = 8 * s + 2 * e, key = (r & 3) + 8 * (r >> 2) + 4 * h;
        const float a = __builtin_amdgcn_exp2f(Sm[r] - mx), c = __builtin_amdgcn_exp2f(Sm[r + 1] - mx);
        const float a8 = key <= r32 ? a : 0.f, c8_ = key + 1 <= r32 ? c : 0.f;
        p8[e] = pk2(a8, c8_); p0[e] = pk2(a - a8, c - c8_);
      }
      O = mfma32(vf[s], p0, O); O = mfma32(vf[16 + s], p8, O); tie(O); SB();
    }
    sfor<7>([&](auto I) {
      constexpr int i = I + 1;
      if constexpr (i < 7) { S[i + 1] = qk(IC<i + 1>{}, negm); rdv(IC<i + 1>{}); SB(); }
      v4u pf[2];
#pragma unroll
      for (int e = 0; e < 8; ++e) pf[e / 4][e % 4] = pk2(__builtin_amdgcn_exp2f(S[i][2 * e]), __builtin_amdgcn_exp2f(S[i][2 * e + 1]));
      O = mfma32(vf[2 * i], pf[0], O); O = mfma32(vf[2 * i + 1], pf[1], O); tie(O); SB();
    });
    const float inv = __builtin_amdgcn_rcpf(swap32_hi(O[8]));
    const int tok = 128 * p + 32 * m + r32;
    const unsigned ob = WS_OF + (tok >> 4) * 20480;
    STAMP(11);
    st16(v4u{pk2(O[0] * inv, O[1] * inv), pk2(O[2] * inv, O[3] * inv), pk2(O[4] * inv, O[5] * inv), pk2(O[6] * inv, O[7] * inv)}, rws, ob + ((j & 1) * 10 + (j >> 1)) * 1024 + (2 * hd + h) * 256 + (tok & 15) * 16);
    if (h == 0) st8(v2u{pk2(O[8] * inv, O[9] * inv), pk2(O[10] * inv, O[11] * inv)}, rws, ob + (((j >> 2) & 1) * 10 + 8 + (j >> 3)) * 1024 + hd * 512 + (j & 3) * 128 + (tok & 15) * 8);
  }
  waitv<0>();
  bar();
  const int tt = 8 * p + 4 * rh + m;
  unsigned x2k[6];
  {
    v2u xr[3];
    const unsigned xo = (16 * tt + i16) * 1280 + (80 * c8 + 4 * g4) * 2;
    if (tid == 0) __hip_atomic_fetch_add((GAS unsigned*)(P.ws + CT_C2 + (p << 7)), 1u, RLX_AGENT);
    STAMP(12);
    v4u mp[4];
    ld8p(xr[0], xo, rx, kh * 64); ld8p(xr[1], xo, rx, kh * 64 + 32); ld8p(xr[2], xo, rx, 128);
    dman<4, 0, 8192>(LB + kh * 20480 + m * 5120, l16, rws, w3); dman<1, 4096, 12288>(LB + kh * 20480 + m * 5120, l16, rws, w3);
    if (w == 4) poll(caddr, cbase, lane == 7 ? 80u : 16u, lane == 3 || lane == 7);
    bar();
    STAMP(13);
    v4u af[10], wf[50];
    const unsigned ab = WS_OF + (tt * 20 + kh * 10) * 1024;
    v2u ah[4];
    sfor<4>([&](auto I) { ld16(mp[I], w == 5 ? ((lane < 20 ? 1280 : 3200 - 80) + 80 * c8 + 4 * (lane < 40 ? lane : 20)) * 4 : 0x7ff00000, rws, WS_MODP + I * 15360); });
    sfor<8>([&](auto I) { ldf<I>(af[I], l16, rws, ab); });
    sfor<4>([&](auto I) { ldh<I * 512>(ah[I], lane * 8, rws, ab + 8192); });
    peek(cbv, lane >= 8 && lane < 16 ? caddr : (const GAS unsigned*)P.ws);
    f32x4 acc[5] = {};
    unsigned wa = L_W3A + kh * 32768 + l16; tie(wa);
    const unsigned wb = kh * 20480 + l16;
    auto rdw = [&](auto N) {
      constexpr int n = 5 * (N / 5) + 4 - N % 5;
      if constexpr (n < 32) wf[n] = lds16(L, wa + (n & 3) * 8192 + (n >> 2) * 1024); else wf[n] = lds16(L, wb + (n & 3) * 5120 + ((n >> 2) - 8) * 1024);
    };
    sfor<10>([&](auto E) {
      constexpr int e = E;
      if constexpr (!(e & 1) && e < 8) { waitv<11 - e>(); tie(af[e]); tie(af[e + 1]); }
      if constexpr (e == 8) { waitv<1>(); sfor<2>([&](auto I) { tie(ah[2 * I]); tie(ah[2 * I + 1]); af[8 + I] = v4u{ah[2 * I].x, ah[2 * I].y, ah[2 * I + 1].x, ah[2 * I + 1].y}; }); }
      if constexpr (e == 0) { rbar(); sfor<8>(rdw); }
      if constexpr (e == 1) { if (w == 5) mod_put(mp, false, lane < 40, L_MODV + 5120 + l16); }
      sfor<5>([&](auto K) {
        constexpr int n = 5 * e + K;
        acc[K] = mfma16(wf[n], af[e], acc[K]); tie(acc[K]);
        if constexpr (n + 8 < 50) rdw(IC<n + 8>{});
        SB();
      });
    });
    STAMP(14);
    const unsigned xs = 57344 + m * 5120;
    if (kh == 0) { *(LAS f32x4*)(L + xs + l16) = acc[2]; *(LAS f32x4*)(L + xs + 1024 + l16) = acc[3]; }
    else { *(LAS f32x4*)(L + xs + 2048 + l16) = acc[0]; *(LAS f32x4*)(L + xs + 3072 + l16) = acc[1]; *(LAS f32x4*)(L + xs + 4096 + l16) = acc[4]; }
    bar();
    tie(xr[0]); tie(xr[1]); tie(xr[2]);
    f32x4 r0, r1, r2 = {};
    if (kh == 0) { r0 = acc[0] + ldsf4(L, xs + 2048 + l16); r1 = acc[1] + ldsf4(L, xs + 3072 + l16); r2 = acc[4] + ldsf4(L, xs + 4096 + l16); }
    else { r0 = acc[2] + ldsf4(L, xs + l16); r1 = acc[3] + ldsf4(L, xs + 1024 + l16); }
    const f32x4 ga0 = ldsf4(L, L_MODV + 5120 + (32 * kh + 4 * g4) * 4), ga1 = ldsf4(L, L_MODV + 5120 + (32 * kh + 16 + 4 * g4) * 4), ga2 = ldsf4(L, L_MODV + 5120 + (64 + 4 * g4) * 4);
    x2k[0] = pk2(bflo(xr[0].x) + ga0[0] * r0[0], bfhi(xr[0].x) + ga0[1] * r0[1]); x2k[1] = pk2(bflo(xr[0].y) + ga0[2] * r0[2], bfhi(xr[0].y) + ga0[3] * r0[3]);
    x2k[2] = pk2(bflo(xr[1].x) + ga1[0] * r1[0], bfhi(xr[1].x) + ga1[1] * r1[1]); x2k[3] = pk2(bflo(xr[1].y) + ga1[2] * r1[2], bfhi(xr[1].y) + ga1[3] * r1[3]);
    x2k[4] = pk2(bflo(xr[2].x) + ga2[0] * r2[0], bfhi(xr[2].x) + ga2[1] * r2[1]); x2k[5] = pk2(bflo(xr[2].y) + ga2[2] * r2[2], bfhi(xr[2].y) + ga2[3] * r2[3]);
    if (kh) { x2k[4] = 0; x2k[5] = 0; }
    const unsigned xb = WS_X2F + ((tt >> 1) * 40 + 5 * c8) * 1024 + ((tt & 1) * 16 + i16) * 16;
    st16(v4u{x2k[0], x2k[1], x2k[2], x2k[3]}, rws, xb + (2 * kh + (g4 >> 1)) * 1024 + (g4 & 1) * 512);
    if (kh == 0) st8(v2u{x2k[4], x2k[5]}, rws, xb + 4096 + (g4 >> 1) * 512 + (g4 & 1) * 8);
    float s1 = 0.f, s2 = 0.f;
#pragma unroll
    for (int e = 0; e < 6; ++e) { s1 = dot2(x2k[e], 0x3f803f80u, s1); s2 = dot2(x2k[e], x2k[e], s2); }
    s1 = swap32_add(swap16_add(s1)); s2 = swap32_add(swap16_add(s2));
    if (g4 == 0) st8(v2u{__builtin_bit_cast(unsigned, s1), __builtin_bit_cast(unsigned, s2)}, rws, WS_ST + ((tt >> 1) * 16 + 2 * c8 + kh) * 256 + ((tt & 1) * 16 + i16) * 8);
  }
  waitv<0>();
  tie(cbv);
  if (w == 4) *(LAS unsigned*)(L + L_CNT) = all64(lane < 8 || lane >= 16 || cbv - cbase >= 32u);
  bar();
  const bool early = __builtin_amdgcn_readfirstlane(*(LAS unsigned*)(L + L_CNT)) != 0;
  {
    const unsigned w4 = WS_WT4 + j * 245760 + kh * 122880 + m * 30720, w5 = WS_WT5 + c8 * 245760 + kh * 122880 + m * 30720 - 30720;
    if (tid == 0) __hip_atomic_fetch_add((GAS unsigned*)(P.ws + CT_C3 + (p << 7)), 1u, RLX_AGENT);
    STAMP(15);
    if (!early) { if (w == 4) poll(caddr, cbase, 32u, lane >= 8 && lane < 16); bar(); }
    auto pre4 = [&]() { dman<4, 0, 0>(ringD, l16, rws, w4); dman<4, 4096, 4096>(ringD, l16, rws, w4); dman<R4 - 8, 8192, 8192>(ringD, l16, rws, w4); };
    if (w == 4) { pre4(); poll(caddr, cbase, 16u, lane == 4); }
    else {
      const int rq = (v6 < 6 ? v6 : 0) * 16 + i16;
      v4u uv[4];
      sfor<4>([&](auto I) { ld16(uv[I], (g4 * 8 + rq / 48) * 12288 + (j * 192 + 4 * (rq % 48)) * 4, rws, WS_UV + I * 24576); });
      pre4();
      waitv<R4>();
      sfor<4>([&](auto I) { tie(uv[I]); });
      f32x4 u = (__builtin_bit_cast(f32x4, uv[0]) + __builtin_bit_cast(f32x4, uv[1])) + (__builtin_bit_cast(f32x4, uv[2]) + __builtin_bit_cast(f32x4, uv[3]));
#pragma unroll
      for (int e = 0; e < 4; ++e) { float t = u[e]; u[e] = swap32_add(swap16_add(t)); }
      if (v6 < 6 && lane < 16) *(LAS f32x4*)(L + L_UVS + rq * 16) = u;
    }
    bar();
    STAMP(16);
    v4u af[20], a5[24], stv[4], wf[240]; unsigned pk;
    const unsigned ab = WS_X2F + ((4 * p + m) * 40 + 20 * kh) * 1024, ab5 = WS_ACT + (tt * 48 + 24 * kh) * 1024;
    sfor<10>([&](auto I) { ldf<I>(af[I], l16, rws, ab); });
    sfor<4>([&](auto I) { ldf<I>(stv[I], l16, rws, WS_ST + (4 * p + m) * 4096); });
    const int tok = 128 * p + 32 * m + r32;
    const unsigned ao = WS_ACT + ((tok >> 4) * 48 + (j & 1) * 24 + (j >> 1)) * 1024 + kh * 512 + h * 256 + (tok & 15) * 16;
    f32x16 aK[3] = {}, aS[3] = {};
    f32x4 acc[5] = {};
    const unsigned wbK = ringL + kh * 15360, wbS = ringL + 15360 - kh * 15360;
    unsigned xw = L_BSRC + w * 4096 + l16, xr_ = L_BSRC + (w ^ 4) * 4096 + l16, ub = L_UVS + kh * 128 + h * 16; tie(xw); tie(xr_); tie(ub);
    float rs2, nm2, av[8]; f32x4 eo[4], eu[4], ev[4];
    constexpr auto isS = [](int n) { return (n / 10) % 4 == 3 ? n % 10 < 5 : (n & 1) != 0; };
    constexpr auto stp = [](int n) { return (n / 10) % 4 == 3 ? (n / 10) * 5 + n % 5 : n / 2; };
    auto rdw = [&](auto N) {
      constexpr int n = N < 120 ? (N ^ 1) : 5 * (N / 5) + 4 - N % 5;
      if constexpr (n < 120) wf[n] = lds16(L, (isS(n) ? wbS : wbK) + ring_off(2 * stp(n))); else wf[n] = lds16(L, ringL + ring_off(n));
    };
    auto vmem = [&](auto Q, auto K) {
      constexpr int k = SC.slot(Q, K, 10);
      if constexpr (k >= 0) {
        constexpr int ty = SC.ty[k], ix = SC.ix[k];
        if constexpr (ty == OP_A) ldf<ix>(af[ix], l16, rws, ab);
        else if constexpr (ty == OP_W) dman<SC.cn[k], (ix % 15) * 1024, ix * 1024>(ringD, l16, rws, ix < 30 ? w4 : w5);
        else if constexpr (ty == OP_ST) ldf<ix>(stv[ix], l16, rws, WS_ST + (4 * p + m) * 4096);
        else if constexpr (ty == OP_PEEK) { if (w == 4) ld4(pk, lane == (ix ? 15 + ix : 5) ? CT_C4 + p * 384 + ix * 128 : 0x7ff00000, rws, 0); }
        else if constexpr (ty == OP_A5) ldf<ix>(a5[ix], l16, rws, ab5);
      }
    };
    auto epiA = [&](auto G, auto QD) { const f32x16 sd = aS[G]; *(LAS f32x4*)(L + xw + QD * 1024) = f32x4{sd[4 * QD], sd[4 * QD + 1], sd[4 * QD + 2], sd[4 * QD + 3]}; };
    auto epi = [&](auto G, auto K) {
      if constexpr (K < 2) sfor<2>([&](auto I) { constexpr int qd = 2 * K + I; eo[qd] = ldsf4(L, xr_ + qd * 1024); eu[qd] = ldsf4(L, ub + G * 256 + qd * 32); ev[qd] = ldsf4(L, ub + 768 + G * 256 + qd * 32); });
      else if constexpr (K < 18) { constexpr int r = K - 2; float t = fmaf(aK[G][r] + eo[r / 4][r % 4], rs2, fmaf(nm2, eu[r / 4][r % 4], ev[r / 4][r % 4])); tie(t); aK[G][r] = t; }
      else if constexpr (K < 26) { constexpr int k = K - 18; const float gv = aK[G][k]; float t = gv * __builtin_amdgcn_rcpf(1.f + __builtin_amdgcn_exp2f(-1.44269504f * gv)) * aK[G][8 + k]; tie(t); av[k] = t; }
      else if constexpr (K == 29) st16(v4u{pk2(av[0], av[1]), pk2(av[2], av[3]), pk2(av[4], av[5]), pk2(av[6], av[7])}, rws, ao + G * 8192);
    };
    auto top = [&](auto Q) {
      constexpr int q = Q;
      waitv<sc_wait(q)>();
      if constexpr (q < 4) sfor<5>([&](auto I) { tie(af[5 * q + I]); });
      if constexpr (q >= 13) { tie(a5[2 * (q - 13)]); tie(a5[2 * (q - 13) + 1]); }
      if constexpr (q == 11 || q == 13 || q == 18) {
        constexpr int ln = q == 11 ? 5 : q == 13 ? 16 : 17;
        if (w == 4) { waitv<SC.before[q] - sc_peek(q == 11 ? 0 : q == 13 ? 1 : 2)>(); tie(pk); if (!all64(lane != ln || pk - cbase >= 16u)) poll(caddr, cbase, 16u, lane == ln); }
      }
      if constexpr (q == 4 || q == 8 || q == 12) bar(); else rbar();
      if constexpr (q == 8 || q == 11 || q == 14) { if (tid == 64) __hip_atomic_fetch_add((GAS unsigned*)(P.ws + CT_C4 + p * 384 + (q == 8 ? 0 : q == 11 ? 128 : 256)), 1u, RLX_AGENT); }
    };
    sfor<12>([&](auto Q) {
      constexpr int q = Q, g = q / 4;
      top(Q);
      if constexpr (q == 0) sfor<6>(rdw);
      sfor<10>([&](auto K) {
        constexpr int n = 10 * q + K, i = stp(n) % 20;
        if constexpr (isS(n)) { aS[g] = mfma32(wf[n], af[i], aS[g]); tie(aS[g]); } else { aK[g] = mfma32(wf[n], af[i], aK[g]); tie(aK[g]); }
        if constexpr (n + 6 < 120) rdw(IC<n + 6>{});
        vmem(Q, K);
        if constexpr (q == 2 && K == 4) {
          sfor<4>([&](auto I) { tie(stv[I]); });
          f32x4 v = (__builtin_bit_cast(f32x4, stv[0]) + __builtin_bit_cast(f32x4, stv[1])) + (__builtin_bit_cast(f32x4, stv[2]) + __builtin_bit_cast(f32x4, stv[3]));
          float u[4];
#pragma unroll
          for (int e = 0; e < 4; ++e) { float a = v[e]; a = swap32_add(swap16_add(a)); u[e] = __builtin_bit_cast(float, __builtin_amdgcn_ds_bpermute((r32 >> 1) * 4, __builtin_bit_cast(int, a))); }
          const float s1 = (lane & 1) ? u[2] : u[0], s2 = (lane & 1) ? u[3] : u[1];
          const float mean = s1 * (1.f / 640.f);
          rs2 = __builtin_amdgcn_rsqf(fmaxf(s2 * (1.f / 640.f) - mean * mean, 0.f) + 1e-5f); nm2 = -mean * rs2; tie(rs2); tie(nm2);
        }
        if constexpr (q % 4 == 3 && K >= 6) epiA(IC<g>{}, IC<K - 6>{});
        if constexpr (g == 1 && q % 4 < 3) epi(IC<0>{}, IC<10 * (q % 4) + K>{});
        if constexpr (q == 8) { if constexpr (K < 2) epi(IC<1>{}, K); else { epi(IC<1>{}, IC<2 * K - 2>{}); epi(IC<1>{}, IC<2 * K - 1>{}); } }
        if constexpr (q == 9) epi(IC<1>{}, IC<(K < 9 ? 18 + K : 29)>{});
        SB();
      });
    });
    STAMP(17);
    top(IC<12>{});
    sfor<30>([&](auto K) { if constexpr (K % 3 == 0) vmem(IC<12>{}, IC<K / 3>{}); epi(IC<2>{}, K); SB(); });
    STAMP(19);
    sfor<12>([&](auto E) {
      constexpr int e = E;
      top(IC<13 + e>{});
      if constexpr (e == 0) sfor<8>([&](auto N) { rdw(IC<120 + N>{}); });
      sfor<10>([&](auto K) {
        constexpr int n = 10 * e + K;
        acc[n % 5] = mfma16(wf[120 + n], a5[n / 5], acc[n % 5]); tie(acc[n % 5]);
        if constexpr (n + 8 < 120) rdw(IC<128 + n>{});
        vmem(IC<13 + e>{}, K);
        SB();
      });
    });
    STAMP(20);
    const unsigned xs = L_BSRC + m * 5120;
    if (kh == 0) { *(LAS f32x4*)(L + xs + l16) = acc[2]; *(LAS f32x4*)(L + xs + 1024 + l16) = acc[3]; }
    else { *(LAS f32x4*)(L + xs + 2048 + l16) = acc[0]; *(LAS f32x4*)(L + xs + 3072 + l16) = acc[1]; *(LAS f32x4*)(L + xs + 4096 + l16) = acc[4]; }
    bar();
    f32x4 r0, r1, r2 = {};
    if (kh == 0) { r0 = acc[0] + ldsf4(L, xs + 2048 + l16); r1 = acc[1] + ldsf4(L, xs + 3072 + l16); r2 = acc[4] + ldsf4(L, xs + 4096 + l16); }
    else { r0 = acc[2] + ldsf4(L, xs + l16); r1 = acc[3] + ldsf4(L, xs + 1024 + l16); }
    const f32x4 g0 = ldsf4(L, L_MODV + 5440 + (32 * kh + 4 * g4) * 4), g1 = ldsf4(L, L_MODV + 5440 + (32 * kh + 16 + 4 * g4) * 4), g2 = ldsf4(L, L_MODV + 5440 + (64 + 4 * g4) * 4);
    GAS char* o = (GAS char*)P.out + (size_t)(16 * tt + i16) * 1280 + (80 * c8 + 4 * g4) * 2;
    *(GAS v2u*)(o + kh * 64) = v2u{pk2(bflo(x2k[0]) + g0[0] * r0[0], bfhi(x2k[0]) + g0[1] * r0[1]), pk2(bflo(x2k[1]) + g0[2] * r0[2], bfhi(x2k[1]) + g0[3] * r0[3])};
    *(GAS v2u*)(o + kh * 64 + 32) = v2u{pk2(bflo(x2k[2]) + g1[0] * r1[0], bfhi(x2k[2]) + g1[1] * r1[1]), pk2(bflo(x2k[3]) + g1[2] * r1[2], bfhi(x2k[3]) + g1[3] * r1[3])};
    if (kh == 0) *(GAS v2u*)(o + 128) = v2u{pk2(bflo(x2k[4]) + g2[0] * r2[0], bfhi(x2k[4]) + g2[1] * r2[1]), pk2(bflo(x2k[5]) + g2[2] * r2[2], bfhi(x2k[5]) + g2[3] * r2[3])};
    if (b == 0 && tid == 0) __hip_atomic_store((GAS unsigned*)(P.ws + CT_E), cbase + 1u, RLX_AGENT);
    STAMP(21);
  }
}

#if defined(__HIP_DEVICE_COMPILE__)
#pragma clang attribute pop
#endif
extern "C" void kernel_launch(void* const* d_in, const int* in_sizes, int n_in, void* d_out, int out_size, void* d_ws, size_t ws_size,
                              hipStream_t stream) {
  Params P;
  P.x = (const u16*)d_in[0]; P.wqkv = (const u16*)d_in[1]; P.wo = (const u16*)d_in[2];
  P.wg = (const u16*)d_in[4]; P.wu = (const u16*)d_in[5]; P.wd = (const u16*)d_in[6];
  P.c = (const u16*)d_in[7]; P.wmod = (const u16*)d_in[8];
  P.out = (u16*)d_out; P.ws = (char*)d_ws;
  fused_block<<<256, 512, 0, stream>>>(P);
}
```

```cpp
#include <hip/hip_runtime.h>
#include <hip/hip_bf16.h>
#include <utility>

typedef unsigned short u16;
typedef unsigned v4u __attribute__((ext_vector_type(4)));
typedef unsigned v2u __attribute__((ext_vector_type(2)));
typedef short v4s __attribute__((ext_vector_type(4)));
typedef float f32x2 __attribute__((ext_vector_type(2)));
typedef float f32x4 __attribute__((ext_vector_type(4)));
typedef float f32x16 __attribute__((ext_vector_type(16)));
typedef __bf16 bf16x2 __attribute__((ext_vector_type(2)));
typedef __bf16 bf16x8 __attribute__((ext_vector_type(8)));
typedef _Float16 f16x2 __attribute__((ext_vector_type(2)));
typedef __amdgpu_buffer_rsrc_t rsrc_t;
#define LAS __attribute__((address_space(3)))
#define GAS __attribute__((address_space(1)))
#define DI __device__ __forceinline__
#define RLX_AGENT __ATOMIC_RELAXED, __HIP_MEMORY_SCOPE_AGENT

#define DELAY0 20
#define R4 10
#define NTM 3
#define STAMPS 0

constexpr int WS_CTRL = 0;
constexpr int WS_MODP = 65536;
constexpr int WS_ST   = 131072;
constexpr int WS_UV   = 393216;
constexpr int WS_WT1  = 786432;
constexpr int WS_WT3  = WS_WT1 + 16 * 163840;
constexpr int WS_WT4  = WS_WT3 + 8 * 106496;
constexpr int WS_WT5  = WS_WT4 + 16 * 245760;
constexpr int WS_KV   = WS_WT5 + 8 * 245760;
constexpr int WS_OF   = WS_KV + 64 * 16 * 6144;
constexpr int WS_X2F  = WS_OF + 128 * 20 * 1024;
constexpr int WS_ACT  = WS_X2F + 64 * 40 * 1024;
constexpr int WS_XH   = WS_ACT + 128 * 48 * 1024;
constexpr int WS_END  = WS_XH + 64 * 40 * 1024;
constexpr int WS_DBG  = WS_END;
constexpr int CT_E = 0, CT_FLAG = 4096  , CT_C1 = 16384  , CT_C2 = 49152, CT_C3 = CT_C2 + 2048,
              CT_C4 = CT_C3 + 2048  , CT_CB = CT_C4 + 6144  , CT_CD = CT_CB + 1024  , CT_CM = CT_CD + 1024, CT_GO = 12288  ;
constexpr int L_WQ = 0, L_RED = 10240;
constexpr int L_BSRC = 122880;
constexpr int L_MODV = 155648;
constexpr int L_CNT = 162816;
constexpr int L_UVS = 155648;
constexpr int KVT = 6144;
constexpr int L_Q = 73728, L_QHI = 81920, L_RED2 = 112640  , L_W3A = 86016, L_GSRC = 122880  ;
constexpr unsigned SPIN_CAP = 1u << 20;

#if defined(__HIP_DEVICE_COMPILE__)
#pragma clang attribute push (__attribute__((target("no-packed-fp32-ops"))), apply_to = function)
#endif
struct Params { const u16 *x, *wqkv, *wo, *wg, *wu, *wd, *c, *wmod; u16* out; char* ws; };

DI f32x16 mfma32(v4u a, v4u b, f32x16 c) { return __builtin_amdgcn_mfma_f32_32x32x16_bf16(__builtin_bit_cast(bf16x8, a), __builtin_bit_cast(bf16x8, b), c, 0, 0, 0); }
DI f32x4 mfma16(v4u a, v4u b, f32x4 c) { return __builtin_amdgcn_mfma_f32_16x16x32_bf16(__builtin_bit_cast(bf16x8, a), __builtin_bit_cast(bf16x8, b), c, 0, 0, 0); }
DI unsigned pk2(float a, float b) { f32x2 v = {a, b}; return __builtin_bit_cast(unsigned, __builtin_convertvector(v, bf16x2)); }
DI float bflo(unsigned x) { return __builtin_bit_cast(float, x << 16); }
DI float bfhi(unsigned x) { return __builtin_bit_cast(float, x & 0xffff0000u); }
DI float bfr(float a) { return bflo(pk2(a, a)); }
DI f32x2 swp32(float v) {
  unsigned a = __builtin_bit_cast(unsigned, v), c = a; asm("" : "+v"(c));
  auto r = __builtin_amdgcn_permlane32_swap(a, c, false, false);
  unsigned r0 = r[0], r1 = r[1];
  return f32x2{__builtin_bit_cast(float, r0), __builtin_bit_cast(float, r1)};
}
DI f32x2 swp16(float v) {
  unsigned a = __builtin_bit_cast(unsigned, v), c = a; asm("" : "+v"(c));
  auto r = __builtin_amdgcn_permlane16_swap(a, c, false, false);
  unsigned r0 = r[0], r1 = r[1];
  return f32x2{__builtin_bit_cast(float, r0), __builtin_bit_cast(float, r1)};
}
DI float swap32_add(float v) { f32x2 r = swp32(v); return r.x + r.y; }
DI float swap16_add(float v) { f32x2 r = swp16(v); return r.x + r.y; }
DI float swap32_max(float v) { f32x2 r = swp32(v); return fmaxf(r.x, r.y); }
DI float swap32_hi(float v) { return swp32(v).y; }
DI bool all64(bool c) { return __builtin_amdgcn_ballot_w64(!c) == 0ull; }
DI float ror8_add(float v) { return v + __builtin_bit_cast(float, __builtin_amdgcn_update_dpp(0, __builtin_bit_cast(int, v), 0x128, 0xf, 0xf, true)); }

template <bool SC1> DI void dma(unsigned lds_addr, unsigned voff, rsrc_t rs, unsigned soff) {
  if (SC1) asm volatile("s_mov_b32 m0, %0\n\ts_nop 0\n\tbuffer_load_dwordx4 %1, %2, %3 offen sc1 lds" ::"s"(lds_addr), "v"(voff), "s"(rs), "s"(soff) : "memory");
  else     asm volatile("s_mov_b32 m0, %0\n\ts_nop 0\n\tbuffer_load_dwordx4 %1, %2, %3 offen nt lds" ::"s"(lds_addr), "v"(voff), "s"(rs), "s"(soff) : "memory");
}
template <int N, int LO, int GO> DI void dman(unsigned lb, unsigned voff, rsrc_t rs, unsigned gb) {
  unsigned t;
#define DMA_HEAD "s_add_u32 m0, %1, %5\n\ts_add_u32 %0, %4, %6\n\tbuffer_load_dwordx4 %2, %3, %0 offen sc1 lds"
#define DMA_MORE(o) "\n\tbuffer_load_dwordx4 %2, %3, %0 offen offset:" #o " sc1 lds"
#define DMA_ARGS : "=&s"(t) : "s"(lb), "v"(voff), "s"(rs), "s"(gb), "n"(LO), "n"(GO) : "memory", "scc"
  if constexpr (N == 1) asm volatile(DMA_HEAD DMA_ARGS);
  if constexpr (N == 2) asm volatile(DMA_HEAD DMA_MORE(1024) DMA_ARGS);
  if constexpr (N == 3) asm volatile(DMA_HEAD DMA_MORE(1024) DMA_MORE(2048) DMA_ARGS);
  if constexpr (N == 4) asm volatile(DMA_HEAD DMA_MORE(1024) DMA_MORE(2048) DMA_MORE(3072) DMA_ARGS);
}
template <int I> DI void ldf(v4u& d, unsigned voff, rsrc_t rs, unsigned base) {
  asm volatile("buffer_load_dwordx4 %0, %1, %2, %3 offen offset:%4 sc1" : "=v"(d) : "v"(voff), "s"(rs), "s"(base + (I / 4) * 4096), "n"((I % 4) * 1024) : "memory");
}
template <int O> DI void ldh(v2u& d, unsigned voff, rsrc_t rs, unsigned base) {
  asm volatile("buffer_load_dwordx2 %0, %1, %2, %3 offen offset:%4 sc1" : "=v"(d) : "v"(voff), "s"(rs), "s"(base), "n"(O) : "memory");
}
DI void signal(rsrc_t rs, unsigned off) {
  asm volatile("buffer_atomic_add %0, %1, %2, 0 offen" ::"v"(1u), "v"(off), "s"(rs) : "memory");
}
DI void peek(unsigned& d, const GAS unsigned* a) { asm volatile("global_load_dword %0, %1, off sc1" : "=v"(d) : "v"(a) : "memory"); }
DI void ld16(v4u& d, unsigned voff, rsrc_t rs, unsigned soff) { asm volatile("buffer_load_dwordx4 %0, %1, %2, %3 offen sc1" : "=v"(d) : "v"(voff), "s"(rs), "s"(soff) : "memory"); }
DI void ld16p(v4u& d, unsigned voff, rsrc_t rs, unsigned soff) { asm volatile("buffer_load_dwordx4 %0, %1, %2, %3 offen nt" : "=v"(d) : "v"(voff), "s"(rs), "s"(soff) : "memory"); }
DI void ld16c(v4u& d, unsigned voff, rsrc_t rs, unsigned soff) { asm volatile("buffer_load_dwordx4 %0, %1, %2, %3 offen" : "=v"(d) : "v"(voff), "s"(rs), "s"(soff) : "memory"); }
DI void ld8p(v2u& d, unsigned voff, rsrc_t rs, unsigned soff) { asm volatile("buffer_load_dwordx2 %0, %1, %2, %3 offen" : "=v"(d) : "v"(voff), "s"(rs), "s"(soff) : "memory"); }
DI void ld2p(unsigned& d, unsigned voff, rsrc_t rs, unsigned soff) { asm volatile("buffer_load_ushort %0, %1, %2, %3 offen" : "=v"(d) : "v"(voff), "s"(rs), "s"(soff) : "memory"); }
DI void ld4(unsigned& d, unsigned voff, rsrc_t rs, unsigned soff) { asm volatile("buffer_load_dword %0, %1, %2, %3 offen sc1" : "=v"(d) : "v"(voff), "s"(rs), "s"(soff) : "memory"); }
template <int N> DI void waitv() { asm volatile("s_waitcnt vmcnt(%0)" ::"n"(N) : "memory"); }
template <class T> DI void tie(T& a) { asm volatile("" : "+v"(a)); }
DI void rbar() { asm volatile("s_barrier" ::: "memory"); }
DI void bar() { asm volatile("s_waitcnt lgkmcnt(0)\n\ts_barrier" ::: "memory"); }
DI void st16(v4u v, rsrc_t rs, unsigned off) { __builtin_amdgcn_raw_buffer_store_b128(v, rs, off, 0, 16); }
DI void st8(v2u v, rsrc_t rs, unsigned off) { __builtin_amdgcn_raw_buffer_store_b64(v, rs, off, 0, 16); }
DI v4u trpair(LAS char* L, unsigned a0, unsigned a1) {
  v4s lo = __builtin_amdgcn_ds_read_tr16_b64_v4i16((LAS v4s*)(L + a0));
  v4s hi = __builtin_amdgcn_ds_read_tr16_b64_v4i16((LAS v4s*)(L + a1));
  v2u l = __builtin_bit_cast(v2u, lo), h = __builtin_bit_cast(v2u, hi);
  return v4u{l.x, l.y, h.x, h.y};
}
DI v4u lds16(LAS char* L, unsigned a) { return *(LAS v4u*)(L + a); }
DI f32x4 ldsf4(LAS char* L, unsigned a) { return *(LAS f32x4*)(L + a); }

template <int... I, class F> DI void sfor_impl(std::integer_sequence<int, I...>, F&& f) { (f(std::integral_constant<int, I>{}), ...); }
template <int N, class F> DI void sfor(F&& f) { sfor_impl(std::make_integer_sequence<int, N>{}, f); }
#define SB() __builtin_amdgcn_sched_barrier(0)

template <int N> using IC = std::integral_constant<int, N>;
DI void affine4(unsigned x0, unsigned x1, f32x4 a, f32x4 s, unsigned& o0, unsigned& o1) {
  float t0, t1;
  const float a0 = a[0], a1 = a[1], a2 = a[2], a3 = a[3], s0 = s[0], s1 = s[1], s2 = s[2], s3 = s[3];
  asm volatile("v_fma_mix_f32 %0, %4, %6, %10 op_sel_hi:[1,0,0]\n\tv_fma_mix_f32 %2, %4, %7, %11 op_sel:[1,0,0] op_sel_hi:[1,0,0]\n\t"
      "v_fma_mix_f32 %1, %5, %8, %12 op_sel_hi:[1,0,0]\n\tv_fma_mix_f32 %3, %5, %9, %13 op_sel:[1,0,0] op_sel_hi:[1,0,0]\n\t"
      "v_cvt_pk_bf16_f32 %0, %0, %2\n\tv_cvt_pk_bf16_f32 %1, %1, %3"
      : "=&v"(o0), "=&v"(o1), "=&v"(t0), "=&v"(t1) : "v"(x0), "v"(x1), "v"(a0), "v"(a1), "v"(a2), "v"(a3), "v"(s0), "v"(s1), "v"(s2), "v"(s3));
}
constexpr int ring_off(int pc) { return (pc & 3) * 15360 + ((pc >> 2) % 15) * 1024; }
DI unsigned strm_off(int pc, int rounds) { return ((pc & 3) * rounds + (pc >> 2)) * 1024; }
DI float dot2(unsigned a, unsigned b, float c) { return __builtin_amdgcn_fdot2_f32_bf16(__builtin_bit_cast(bf16x2, a), __builtin_bit_cast(bf16x2, b), c, false); }
DI f32x2 xswp32(float a, float c) { auto r = __builtin_amdgcn_permlane32_swap(__builtin_bit_cast(unsigned, a), __builtin_bit_cast(unsigned, c), false, false); unsigned r0 = r[0], r1 = r[1]; return f32x2{__builtin_bit_cast(float, r0), __builtin_bit_cast(float, r1)}; }
DI f32x2 xswp16(float a, float c) { auto r = __builtin_amdgcn_permlane16_swap(__builtin_bit_cast(unsigned, a), __builtin_bit_cast(unsigned, c), false, false); unsigned r0 = r[0], r1 = r[1]; return f32x2{__builtin_bit_cast(float, r0), __builtin_bit_cast(float, r1)}; }

enum { OP_A, OP_W, OP_ST, OP_UV, OP_STORE, OP_PEEK, OP_A5, OP_MW, OP_MC };
struct Sched {
  int n, tot; int ty[128], ix[128], cn[128], pos[128], start[27], before[27];
  constexpr void put(int t, int i, int c) { ty[n] = t; ix[n] = i; cn[n] = c; pos[n] = tot; tot += c; ++n; }
  constexpr void puts(int t, int i, int c) { for (int k = 0; k < c; ++k) put(t, i + k, 1); }
  constexpr void putw(int r0, int r1) {
    while (r0 < r1) { int c = r1 - r0; if (c > 2) c = 2; if (r0 % 15 + c > 15) c = 15 - r0 % 15; put(OP_W, r0, c); r0 += c; } }
  constexpr void seg(int q) { start[q] = n; before[q] = tot; }
  constexpr int slot(int q, int K, int ns) const {
    int c = start[q + 1] - start[q]; if (c && ty[start[q + 1] - 1] == OP_STORE) --c;
    for (int k = 0; k < c; ++k) if ((k * ns) / c == K) return start[q] + k;
    return -1; }
  constexpr int at(int t, int i) const { for (int k = 0; k < n; ++k) if (ty[k] == t && i >= ix[k] && i < ix[k] + cn[k]) return pos[k] + i - ix[k]; return -1; }
};
constexpr int cmax(int a, int b) { return a > b ? a : b; }
constexpr Sched make_sched() {
  Sched s{};
  s.putw(0, R4); s.puts(OP_A, 0, 10); s.puts(OP_ST, 0, 4);
  const int wto[25] = {R4, R4 + 2, R4 + 5 > 16 ? 16 : R4 + 5, 19, 23, 27, 30, 32, 35, 37, 40, 42, 45, 45, 47, 49, 51, 53, 55, 57, 58, 60, 60, 60, 60};
  const int a5to[25] = {0, 0, 0, 0, 0, 0, 0, 0, 0, 0, 0, 4, 8, 11, 13, 15, 16, 16, 18, 20, 23, 24, 24, 24, 24};
  for (int q = 0; q < 25; ++q) {
    s.seg(q);
    if (q == 10 || q == 17) s.put(OP_PEEK, q == 10 ? 0 : 2, 0);
    if (q < 2) s.puts(OP_A, 10 + 5 * q, 5);
    if (q) s.puts(OP_A5, a5to[q - 1], a5to[q] - a5to[q - 1]);
    if (q) s.putw(wto[q - 1], wto[q]);
    if (q == 12) s.put(OP_PEEK, 1, 0);
    if (q == 6 || q == 9 || q == 12) s.put(OP_STORE, q == 6 ? 0 : q == 9 ? 1 : 2, 1);
  }
  s.seg(25);
  return s;
}
constexpr Sched SC = make_sched();
constexpr int sc_peek(int i) { for (int k = 0; k < SC.n; ++k) if (SC.ty[k] == OP_PEEK && SC.ix[k] == i) return SC.pos[k]; return 0; }
constexpr int sc_wait(int q) {
  int p = -1;
  if (q <= 11) p = SC.at(OP_W, (10 * (q + 1) + (q == 11 ? 0 : q % 4 == 2 ? 10 : 6) + 3) / 4 - 1);
  if (q >= 13) p = cmax(SC.at(OP_W, (10 * q + (q < 24 ? 10 : 0) + 3) / 4 - 1), SC.at(OP_A5, 2 * (q - 13) + 1));
  if (q < 4) p = cmax(p, SC.at(OP_A, 5 * q + 4));
  if (q == 8 || q == 11 || q == 14) p = cmax(p, SC.at(OP_STORE, q == 8 ? 0 : q == 11 ? 1 : 2));
  if (p < 0) return 63;
  return SC.before[q] - 1 - p;
}
constexpr Sched make_sched1() {
  Sched s{}; int a = 5, r = 7;
  s.putw(0, 4); s.puts(OP_A, 0, 3); s.putw(4, 7); s.puts(OP_A, 3, 2);
  for (int q = 0; q < 12; ++q) {
    s.seg(q);
    int na = q < 3 ? 3 : q < 6 ? 2 : 0, nw = q < 5 ? 2 : q < 8 ? 1 : 0;
    s.puts(OP_A, a, na); a += na; s.putw(r, r + nw); r += nw;
  }
  s.seg(12);
  return s;
}
constexpr Sched S1 = make_sched1();
constexpr int s1_first(int q) { return q <= 10 ? 6 * q : q == 11 ? 64 : 80; }
constexpr int s1_wait(int q) {
  int pc = s1_first(q + 1) + 6; if (pc > 80 || q == 11) pc = 80;
  int p = S1.at(OP_W, (pc + 3) / 4 - 1);
  if (q < 10) p = cmax(p, S1.at(OP_A, q < 9 ? 2 * q + 2 : 19));
  return S1.before[q] - 1 - p;
}

#define STAMP(i)

DI void poll(const GAS unsigned* addr, unsigned base, unsigned want, bool active) {
#pragma nounroll
  for (unsigned s = 0; s < SPIN_CAP; ++s) {
    unsigned v = base + want;
    if (active) v = __hip_atomic_load(addr, RLX_AGENT);
    if (all64((v - base) >= want)) break;
    __builtin_amdgcn_s_sleep(2);
  }
  __builtin_amdgcn_fence(__ATOMIC_ACQUIRE, "wavefront");
}
DI unsigned lds_arrive(LAS char* L, int off, int lane) {
  unsigned o = 0;
  asm volatile("s_waitcnt lgkmcnt(0)" ::: "memory");
  if (lane == 0) o = __hip_atomic_fetch_add((LAS unsigned*)(L + off), 1u, __ATOMIC_RELAXED, __HIP_MEMORY_SCOPE_WORKGROUP);
  o = __builtin_amdgcn_readfirstlane(o);
  asm volatile("" ::: "memory");
  return o;
}
template <int N> DI void mod_rows(const v4u* wm, const unsigned* cv, LAS char* red, int lane) {
  float acc[8] = {0, 0, 0, 0, 0, 0, 0, 0};
#pragma unroll
  for (int i = 0; i < N; ++i) {
    float cf = bflo(cv[i]);
    float sv = bfr(cf * __builtin_amdgcn_rcpf(1.f + __builtin_amdgcn_exp2f(-1.44269504f * cf)));
#pragma unroll
    for (int e = 0; e < 4; ++e) { acc[2 * e] += sv * bflo(wm[i][e]); acc[2 * e + 1] += sv * bfhi(wm[i][e]); }
  }
  float u[4], v[2];
#pragma unroll
  for (int e = 0; e < 4; ++e) { f32x2 r = xswp32(acc[e], acc[e + 4]); u[e] = r.x + r.y; }
#pragma unroll
  for (int e = 0; e < 2; ++e) { f32x2 r = xswp16(u[e], u[e + 2]); v[e] = ror8_add(r.x + r.y); }
  if (!(lane & 8)) *(LAS f32x2*)(red + (lane & 7) * 32 + (lane >> 4) * 8) = f32x2{v[0], v[1]};
}
DI void lds_wait(LAS char* L, int off, unsigned want) {
#pragma nounroll
  for (unsigned s = 0; s < SPIN_CAP && *(volatile LAS unsigned*)(L + off) < want; ++s) __builtin_amdgcn_s_sleep(1);
  asm volatile("" ::: "memory");
}

__global__ __launch_bounds__(512, 2) void fused_block(Params P) {
  __shared__ __attribute__((aligned(16))) char lds_raw[163840];
  LAS char* L = (LAS char*)lds_raw;
  const unsigned LB = (unsigned)(size_t)L;
  const int tid = threadIdx.x, lane = tid & 63;
  const int w = __builtin_amdgcn_readfirstlane(tid >> 6);
  const int b = blockIdx.x;
  const int p = 4 * ((b & 7) >> 1) + ((b >> 3) & 3), j = 8 * (b & 1) + (b >> 5);
  const int m = w & 3, kh = w >> 2, h = lane >> 5, r32 = lane & 31;
  const int g4 = lane >> 4, i16 = lane & 15, q4 = (lane & 15) >> 2, p4 = lane & 3, blk = (lane >> 4) & 1;
  const rsrc_t rws = __builtin_amdgcn_make_buffer_rsrc((void*)P.ws, 0, WS_END, 0x00020000);
  const rsrc_t rx = __builtin_amdgcn_make_buffer_rsrc((void*)P.x, 0, 2048 * 1280, 0x00020000);
  const unsigned l16 = lane * 16;
  const unsigned ringL = kh * 61440 + l16, ringD = LB + kh * 61440 + m * 15360;
  STAMP(0);
  unsigned long long pc0; asm volatile("s_getpc_b64 %0" : "=s"(pc0));
  unsigned cbase = 0;
  const GAS unsigned* caddr = (const GAS unsigned*)P.ws;
  const int c40 = b % 40, c20 = (b - 160) % 20;
  const int cc = b < 160 ? (c40 < 20 ? c40 : c40 + 10) : (c20 < 10 ? 20 + c20 : 40 + c20), kq = b < 160 ? b / 40 : ((b - 160) / 20) & 3;
  const char* wm0 = (const char*)P.wmod + (size_t)(160 * kq) * 7680 + cc * 128;
  {
    const int bj = b < 240 ? b : 0, s = bj / 6, q = bj % 6;
    const int lr = (lane * 205) >> 11, lc = lane - 10 * lr;
    const bool lo = w < 4;
    v4u t[8], xv[3], z[2]; unsigned cv[5], dst[2];
    {
      const rsrc_t r0 = __builtin_amdgcn_make_buffer_rsrc((void*)((const char*)P.x + (size_t)b * 10240), 0, lo ? 0 : 10240, 0x00020000);
      const rsrc_t r1 = __builtin_amdgcn_make_buffer_rsrc((void*)wm0, 0, lo && b < 160 ? 0x200000 : 0, 0x00020000);
      const rsrc_t r2 = __builtin_amdgcn_make_buffer_rsrc((void*)((const char*)P.wqkv + (size_t)(16 * s) * 3840 + q * 640 + m * 160), 0, lo ? 0x10000 : 0, 0x00020000);
      const rsrc_t rc = __builtin_amdgcn_make_buffer_rsrc((void*)(P.c + 160 * kq), 0, lo && b < 160 ? 320 : 0, 0x00020000);
      const unsigned v0 = (lane & 7) * 1280 + (lane >> 3) * 16 + m * 128, v1 = (tid >> 3) * 7680 + (tid & 7) * 16, v2 = (lr < 6 ? lr : 5) * 3840 + lc * 16;
      if (NTM & 4) { ld16p(xv[0], v0, r0, 0); ld16p(xv[1], v0, r0, 512); ld16p(xv[2], v0, r0, w < 6 ? 1024 : 0x7ff00000u); }
      else { ld16c(xv[0], v0, r0, 0); ld16c(xv[1], v0, r0, 512); ld16c(xv[2], v0, r0, w < 6 ? 1024 : 0x7ff00000u); }
      if (NTM & 1) sfor<5>([&](auto I) { ld16p(t[I], v1, r1, I * 245760); }); else sfor<5>([&](auto I) { ld16c(t[I], v1, r1, I * 245760); });
      if (NTM & 2) { ld16p(t[5], v2, r2, 0); ld16p(t[6], v2, r2, 23040); ld16p(t[7], lr >= 4 ? v2 - 7680 : v2, r2, 46080); }
      else { ld16c(t[5], v2, r2, 0); ld16c(t[6], v2, r2, 23040); ld16c(t[7], lr >= 4 ? v2 - 7680 : v2, r2, 46080); }
      sfor<5>([&](auto I) { ld2p(cv[I], (tid >> 3) * 2, rc, I * 64); });
      ld16(z[0], l16, rws, b < 8 && w == 5 ? CT_FLAG + b * 1024 : 0x7ff00000);
      ld16(z[1], l16, __builtin_amdgcn_make_buffer_rsrc((void*)(pc0 & ~255ull), 0, w == 4 || w == 5 ? 34816 : 0, 0x00020000), (w == 5 ? 0 : 32768) + (b >> 3) * 1024);
      if (w >= 6) {
        const rsrc_t ro = __builtin_amdgcn_make_buffer_rsrc((void*)P.wo, 0, 640 * 1280, 0x00020000);
#pragma unroll
        for (int u = 0; u < 2; ++u) {
          const int sl = 2 * (w - 6) + u, f = sl < 3 ? 3 * b + sl : 768 + (b & 31), cw = f / 100, st = (f % 100) / 5, tl = f % 5;
          const int e = (lane >> 1) & 7;
          const int ch = st < 16 ? 40 * st + 20 * (g4 >> 1) + 4 * (g4 & 1) + (e < 4 ? e : e + 4) : 40 * (4 * (st - 16) + g4) + 16 + (e < 4 ? e : e + 16);
          dma<false>(LB + 16384 + sl * 1024, ch * 1280 + (80 * cw + 16 * tl) * 2 + (lane & 1) * 16, ro, 0);
          dst[u] = WS_WT3 + cw * 106496 + (st & 1) * 53248 + strm_off((st >> 1) * 5 + tl, 13) + l16;
        }
      }
    }
    if (w == 4) {
      int off = CT_E;
      if (lane == 1) off = CT_C1 + (((p >= 1 ? p - 1 : 0) * 16 + j) << 7);
      if (lane == 2) off = CT_C1 + (((p >= 2 ? p - 2 : 0) * 16 + j) << 7);
      if (lane == 3) off = CT_C2 + (p << 7);
      if (lane == 4) off = CT_C3 + (p << 7);
      if (lane == 5) off = CT_C4 + p * 384;
      if (lane == 16 || lane == 17) off = CT_C4 + p * 384 + (lane - 15) * 128;
      if (lane == 7) off = CT_CM;
      if (lane >= 8 && lane < 16) off = CT_CB + ((lane - 8) << 7);
      caddr = (const GAS unsigned*)(P.ws + off);
    }
    peek(cbase, caddr);
    if (tid < 2) *(LAS v4u*)(L + L_CNT + tid * 16) = v4u{0u, 0u, 0u, 0u};
    bar();
    waitv<0>();
    sfor<8>([&](auto I) { tie(t[I]); });
    sfor<5>([&](auto I) { tie(cv[I]); });
    tie(xv[0]); tie(xv[1]); tie(xv[2]); tie(z[0]); tie(z[1]); tie(cbase);
    if (lo) {
      const unsigned wql = L_WQ + w * 2560;
      if (lr < 6) { *(LAS v4u*)(L + wql + l16) = t[5]; *(LAS v4u*)(L + wql + 960 + l16) = t[6]; }
      if (lr < 4) *(LAS v4u*)(L + wql + 1920 + l16) = t[7];
      if (b < 160) {
        mod_rows<5>(t, cv, L + L_RED + w * 256, lane);
        if (lds_arrive(L, L_CNT, lane) == 3 && lane < 16) {
          f32x4 u = (ldsf4(L, L_RED + l16) + ldsf4(L, L_RED + 256 + l16)) + (ldsf4(L, L_RED + 512 + l16) + ldsf4(L, L_RED + 768 + l16));
          st16(__builtin_bit_cast(v4u, u), rws, WS_MODP + (kq * 3840 + cc * 64 + lane * 4) * 4);
        }
      }
      if (b < 240) {
        asm volatile("s_waitcnt lgkmcnt(0)" ::: "memory");
        const int part = q >> 1, jb = 8 * (q & 1) + 2 * w;
        const int sg = s >= 20 ? s - 20 : s, pk0 = 3 * (sg & ~1);
        const unsigned dst0 = WS_WT1 + jb * 163840 + (s >= 20 ? 81920 : 0);
        const unsigned dmain = dst0 + strm_off(part == 0 ? 60 + sg : pk0 + 2 * (sg & 1) + part - 1, 20), dmix = dst0 + strm_off(pk0 + 4 + (sg & 1), 20);
        const unsigned rowa = wql + (8 * h + q4) * 160;
        const unsigned a = rowa + (20 * blk + 4 * p4) * 2, a2 = rowa + (40 * blk + 16 + 20 * (p4 & 1)) * 2;
        v4u f0 = trpair(L, a, a + 640), f1 = trpair(L, a + 80, a + 720), f2 = trpair(L, a2, a2 + 640);
        st16(f0, rws, dmain + l16);
        st16(f1, rws, dmain + 163840 + l16);
        if (i16 >= 8) f2 = v4u{0, 0, 0, 0};
        const int r = (part == 0 ? 16 : part == 1 ? 0 : 8) + i16;
        if (part == 0 || i16 < 8) st16(f2, rws, dmix + blk * 163840 + (h * 32 + r) * 16);
      }
    } else {
      float s1 = 0.f, s2 = 0.f, s3 = 0.f, s4 = 0.f;
#pragma unroll
      for (int i = 0; i < 3; ++i) {
        s1 = dot2(xv[i][0], 0x3f803f80u, s1); s2 = dot2(xv[i][0], xv[i][0], s2); s3 = dot2(xv[i][1], 0x3f803f80u, s3); s4 = dot2(xv[i][1], xv[i][1], s4);
        s1 = dot2(xv[i][2], 0x3f803f80u, s1); s2 = dot2(xv[i][2], xv[i][2], s2); s3 = dot2(xv[i][3], 0x3f803f80u, s3); s4 = dot2(xv[i][3], xv[i][3], s4);
      }
      s1 = swap32_add(swap16_add(ror8_add(s1 + s3))); s2 = swap32_add(swap16_add(ror8_add(s2 + s4)));
      if (lane < 8) *(LAS f32x2*)(L + L_RED + 1024 + lane * 32 + m * 8) = f32x2{s1, s2};
      lds_arrive(L, L_CNT + 8, lane);
      if (w >= 6) {
#pragma unroll
        for (int u = 0; u < 2; ++u) {
          const unsigned a = 16384 + (2 * (w - 6) + u) * 1024 + (8 * g4 + q4) * 32 + p4 * 8;
          st16(trpair(L, a, a + 128), rws, dst[u]);
        }
      }
      lds_wait(L, L_CNT + 8, 4u);
      const f32x4 o0 = ldsf4(L, L_RED + 1024 + (lane & 7) * 32), o1 = ldsf4(L, L_RED + 1040 + (lane & 7) * 32);
      const float mean = ((o0[0] + o0[2]) + (o1[0] + o1[2])) * (1.f / 640.f);
      const float rstd = __builtin_amdgcn_rsqf(fmaxf(((o0[1] + o0[3]) + (o1[1] + o1[3])) * (1.f / 640.f) - mean * mean, 0.f) + 1e-5f), nm = -mean * rstd;
      const int tok = 8 * b + (lane & 7);
      const unsigned xo = WS_XH + (tok >> 5) * 40960 + m * 4096 + (lane >> 3) * 512 + (tok & 31) * 16;
#pragma unroll
      for (int i = 0; i < 3; ++i) {
        if (i == 2 && w >= 6) break;
        v4u o;
#pragma unroll
        for (int e = 0; e < 4; ++e) { f32x2 v = {fmaf(bflo(xv[i][e]), rstd, nm), fmaf(bfhi(xv[i][e]), rstd, nm)}; o[e] = __builtin_bit_cast(unsigned, __builtin_convertvector(v, f16x2)); }
        st16(o, rws, xo + i * 16384);
      }
    }
  }
  waitv<0>();
  if (lds_arrive(L, L_CNT + 4, lane) == 7 && lane < 8) __hip_atomic_store((GAS unsigned*)(P.ws + CT_FLAG + lane * 1024 + b * 4), __builtin_amdgcn_readfirstlane(cbase) + 1u, RLX_AGENT);
  STAMP(1);
  const int c8 = __builtin_amdgcn_readfirstlane(4 * (b & 1) + ((b >> 5) & 3)), rh = b >> 7;
  const int kp = (b >> 4) & 7;
  int col = tid < 320 ? 16 * tid : tid < 340 ? (2560 + 80 * kp + 4 * (tid - 320)) * 4 : tid < 360 ? (1920 + 80 * kp + 4 * (tid - 340)) * 4 : 0;
  unsigned mdst = L_MODV + (tid < 160 ? tid * 32 + 16 : tid < 320 ? (tid - 160) * 32 : 5760 + (tid - 320) * 16);
  const rsrc_t rb = __builtin_amdgcn_make_buffer_rsrc((void*)((const char*)((b >> 7) ? P.wu : P.wg) + (size_t)(80 * kp) * 3072 + (b & 15) * 192), 0, 80 * 3072, 0x00020000);
  tie(col); tie(mdst);
  const bool mod2 = b >= 160 && b < 240;
  v4u m2w[3]; unsigned m2c[3];
  __builtin_amdgcn_s_sleep(DELAY0);
  const int v6 = w < 4 ? w : w - 1;
  if (w == 7) {
    const rsrc_t rd = __builtin_amdgcn_make_buffer_rsrc((void*)((const char*)P.wd + (b / 5) * 40960 + (b % 5) * 256), 0, b < 240 ? 40960 : 0, 0x00020000);
    const int e = (lane >> 1) & 7;
    const unsigned vo = (16 * (g4 >> 1) + 4 * (g4 & 1) + (e < 4 ? e : e + 4)) * 1280 + (lane & 1) * 16;
    sfor<8>([&](auto U) { dma<false>(LB + L_GSRC + (18 + U) * 1024, vo, rd, U * 32); });
  } else if (w != 4) {
    const int e = (lane >> 2) & 7;
#pragma unroll
    for (int u = 0; u < 3; ++u) {
      const int sg = (v6 & 1) * 3 + u;
      const int k = sg < 4 ? 32 * (sg >> 1) + 4 * (2 * (sg & 1) + h) + (e < 4 ? e : e + 12) : 64 + 8 * h + e;
      if (sg < 5) dma<false>(LB + L_GSRC + (3 * v6 + u) * 1024, k * 3072 + (v6 >> 1) * 64 + p4 * 16, rb, 0);
    }
  }
  {
    const rsrc_t rm = __builtin_amdgcn_make_buffer_rsrc((void*)wm0, 0, mod2 ? 160 * 7680 : 0, 0x00020000);
    const rsrc_t rc = __builtin_amdgcn_make_buffer_rsrc((void*)(P.c + 160 * kq), 0, mod2 ? 320 : 0, 0x00020000);
    sfor<3>([&](auto I) { ld16p(m2w[I], (tid >> 3) * 7680 + (tid & 7) * 16, rm, I * 491520); ld2p(m2c[I], (tid >> 3) * 2, rc, I * 128); });
  }
  if (w == 4) {
    const unsigned tag = __builtin_amdgcn_readfirstlane(cbase) + 1u;
    GAS unsigned* go = (GAS unsigned*)(P.ws + CT_GO + (b & 7) * 128);
    if (b < 8) {
#pragma nounroll
      for (unsigned s = 0; s < SPIN_CAP; ++s) {
        v4u f; ld16(f, l16, rws, CT_FLAG + b * 1024); waitv<0>(); tie(f);
        if (all64(f.x == tag && f.y == tag && f.z == tag && f.w == tag)) break;
      }
      if (lane == 0) __hip_atomic_store(go, tag, RLX_AGENT);
    } else {
#pragma nounroll
      for (unsigned s = 0; s < SPIN_CAP && __hip_atomic_load(go, RLX_AGENT) != tag; ++s) __builtin_amdgcn_s_sleep(1);
    }
    __builtin_amdgcn_fence(__ATOMIC_ACQUIRE, "wavefront");
  }
  bar();
  STAMP(4);

  auto mod_put = [&](v4u* mp, bool isc, bool on, unsigned dst) {
    sfor<4>([&](auto I) { tie(mp[I]); });
    f32x4 v = (__builtin_bit_cast(f32x4, mp[0]) + __builtin_bit_cast(f32x4, mp[1])) + (__builtin_bit_cast(f32x4, mp[2]) + __builtin_bit_cast(f32x4, mp[3]));
#pragma unroll
    for (int e = 0; e < 4; ++e) { float t = bfr(v[e]); v[e] = isc ? bfr(1.f + t) : t; }
    if (on) *(LAS f32x4*)(L + dst) = v;
  };
  {
    v4u mp[4], xf[20];
    sfor<4>([&](auto I) { ld16(mp[I], col, rws, WS_MODP + I * 15360); });
    const unsigned w1 = WS_WT1 + j * 163840 + kh * 81920 + m * 20480;
    const unsigned ab = WS_XH + ((4 * p + m) * 40 + 20 * kh) * 1024;
    rbar();
    dman<4, 0, 0>(ringD, l16, rws, w1);
    sfor<3>([&](auto I) { ldf<I>(xf[I], l16, rws, ab); });
    waitv<7>();
    mod_put(mp, tid >= 160 && tid < 340, tid < 360, mdst);
    dman<3, 4096, 4096>(ringD, l16, rws, w1);
    ldf<3>(xf[3], l16, rws, ab); ldf<4>(xf[4], l16, rws, ab);
    STAMP(5);
    f32x16 acc[4] = {};
    const unsigned wbK = ringL + kh * 15360, wbS = ringL + 15360 - kh * 15360;
    unsigned cb = L_MODV + (80 * kh + 2 * h) * 32; tie(cb);
    unsigned xw = kh * 61440 + m * 15360 + 5120 + l16, xr = (kh ^ 1) * 61440 + m * 15360 + 5120 + l16;
    v4u wf[80], hf[20], c0, c1, c2; f32x4 ca[40], cs[40], eo[6];
    const unsigned kvg = WS_KV + ((4 * p + m) * 16 + j) * 6144, kvl = (8 + m) * KVT;
    auto rdw = [&](auto N) {
      constexpr int n = N < 60 ? 3 * (N / 3) + 2 - N % 3 : (N ^ 1), i = n / 3, pk0 = 3 * (i & ~1);
      if constexpr (n >= 60) wf[n] = lds16(L, ringL + ring_off(n));
      else if constexpr (n % 3 == 1) wf[n] = lds16(L, ringL + ring_off(pk0 + 4 + (i & 1)));
      else wf[n] = lds16(L, (n % 3 == 0 ? wbS : wbK) + ring_off(pk0 + 2 * (i & 1)));
    };
    auto rdc = [&](auto R) { ca[R] = ldsf4(L, cb + (R / 2) * 128 + (R & 1) * 32); cs[R] = ldsf4(L, cb + (R / 2) * 128 + (R & 1) * 32 + 16); };
    auto aff = [&](auto I, auto E) { constexpr int e = 2 * E; unsigned o0, o1; affine4(xf[I][e], xf[I][e + 1], ca[2 * I + E], cs[2 * I + E], o0, o1); hf[I][e] = o0; hf[I][e + 1] = o1; };
    auto quad = [&](const f32x16& a, int q) { return f32x4{a[4 * q], a[4 * q + 1], a[4 * q + 2], a[4 * q + 3]}; };
    sfor<12>([&](auto Q) {
      constexpr int q = Q, n0 = s1_first(q);
      waitv<s1_wait(q)>();
      if constexpr (q == 0) tie(xf[0]);
      if constexpr (q < 10) { tie(xf[2 * q + 1]); if constexpr (q < 9) tie(xf[2 * q + 2]); }
      if constexpr (q == 0 || q == 11) bar(); else rbar();
      if constexpr (q == 0) { sfor<6>(rdc); sfor<6>(rdw); aff(IC<0>{}, IC<0>{}); aff(IC<0>{}, IC<1>{}); asm volatile("s_nop 1"); SB(); }
      sfor<s1_first(q + 1) - n0>([&](auto K) {
        constexpr int n = n0 + K, i = n < 60 ? n / 3 : n - 60, t = n < 60 ? n % 3 : 3, k = S1.slot(q, K, 6);
        acc[t] = mfma32(wf[n], hf[i], acc[t]); tie(acc[t]);
        SB();
        if constexpr (t < 2 && i + 1 < 20) aff(IC<i + 1>{}, IC<t>{});
        if constexpr (t < 2 && i + 3 < 20) rdc(IC<2 * (i + 3) + t>{});
        if constexpr (n + 6 < 80) rdw(IC<n + 6>{});
        if constexpr (k >= 0) {
          constexpr int ty = S1.ty[k], ix = S1.ix[k];
          if constexpr (ty == OP_A) ldf<ix>(xf[ix], l16, rws, ab);
          else if constexpr (ty == OP_W) dman<S1.cn[k], (ix % 15) * 1024, ix * 1024>(ringD, l16, rws, w1);
        }
        if constexpr (n >= 59 && n < 63) *(LAS f32x4*)(L + xw + (n - 59) * 1024) = quad(acc[0], n - 59);
        if constexpr (n == 63) { if (kh) { *(LAS f32x4*)(L + xw + 4096) = quad(acc[1], 0); *(LAS f32x4*)(L + xw + 5120) = quad(acc[1], 1); } }
        if constexpr (n == 64) sfor<6>([&](auto I) { eo[I] = ldsf4(L, xr + I * 1024); });
        if constexpr (n == 65 || n == 66) {
          constexpr int o = 8 * (n - 65);
          v4u c = {pk2(acc[2][o] + eo[o / 4][0], acc[2][o + 1] + eo[o / 4][1]), pk2(acc[2][o + 2] + eo[o / 4][2], acc[2][o + 3] + eo[o / 4][3]),
                   pk2(acc[2][o + 4] + eo[o / 4 + 1][0], acc[2][o + 5] + eo[o / 4 + 1][1]), pk2(acc[2][o + 6] + eo[o / 4 + 1][2], acc[2][o + 7] + eo[o / 4 + 1][3])};
          st16(c, rws, kvg + (kh ? 3072 + r32 * 32 + h * 16 : l16) + (n - 65) * 1024);
          if constexpr (n == 65) c0 = c; else c1 = c;
        }
        if constexpr (n == 67) {
          c2 = v4u{pk2(acc[1][0] + eo[4][0], acc[1][1] + eo[4][1]), pk2(acc[1][2] + eo[4][2], acc[1][3] + eo[4][3]),
                   pk2(acc[1][4] + eo[5][0], acc[1][5] + eo[5][1]), pk2(acc[1][6] + eo[5][2], acc[1][7] + eo[5][3])};
          if (!kh) { st16(v4u{c2.x, c2.y, 0u, 0u}, rws, kvg + 2048 + l16); st16(v4u{c2.z, c2.w, 0x3f80u, 0u}, rws, kvg + 5120 + l16); }
        }
        SB();
      });
    });
    STAMP(6);
    const unsigned xq = 5120 + (m >> 1) * 15360 + (m & 1) * 5120 + l16;
    if (!kh) {
#pragma unroll
      for (int qd = 0; qd < 4; ++qd) *(LAS f32x4*)(L + xq + qd * 1024) = quad(acc[3], qd);
      *(LAS f32x4*)(L + xq + 4096) = quad(acc[1], 2);
    }
    if (mod2) {
      sfor<3>([&](auto I) { tie(m2w[I]); tie(m2c[I]); });
      mod_rows<3>(m2w, m2c, L + L_RED2 + w * 256, lane);
    }
    waitv<0>();
    bar();
    if (tid == 0) signal(rws, CT_C1 + ((p * 16 + j) << 7));
    {
      const unsigned lo = kh ? 3072 + r32 * 48 + h * 16 : l16, ls = kh ? 1536 : 1024;
      *(LAS v4u*)(L + kvl + lo) = c0; *(LAS v4u*)(L + kvl + lo + ls) = c1;
    }
    if (kh == 0) {
      *(LAS v4u*)(L + kvl + 2048 + l16) = v4u{c2.x, c2.y, 0u, 0u};
      *(LAS v4u*)(L + kvl + 3072 + h * 1536 + r32 * 48 + 32) = v4u{c2.z, c2.w, 0x3f80u, 0u};
    } else {
      const float qs = 0.22360679775f * 1.44269504089f;
      float qv[20];
#pragma unroll
      for (int qd = 0; qd < 5; ++qd) {
        const f32x4 u = ldsf4(L, xq + qd * 1024);
#pragma unroll
        for (int e = 0; e < 4; ++e) qv[4 * qd + e] = ((qd < 4 ? acc[3][4 * qd + e] : acc[1][8 + e]) + u[e]) * qs;
      }
      *(LAS v4u*)(L + L_Q + m * 2048 + l16) = v4u{pk2(qv[0], qv[1]), pk2(qv[2], qv[3]), pk2(qv[4], qv[5]), pk2(qv[6], qv[7])};
      *(LAS v4u*)(L + L_Q + m * 2048 + 1024 + l16) = v4u{pk2(qv[8], qv[9]), pk2(qv[10], qv[11]), pk2(qv[12], qv[13]), pk2(qv[14], qv[15])};
      *(LAS v2u*)(L + L_QHI + m * 512 + lane * 8) = v2u{pk2(qv[16], qv[17]), pk2(qv[18], qv[19])};
    }
  if (mod2 && w == 3 && lane < 16) {
    f32x4 u = ldsf4(L, L_RED2 + l16);
#pragma unroll
    for (int i = 1; i < 8; ++i) u += ldsf4(L, L_RED2 + i * 256 + l16);
    st16(__builtin_bit_cast(v4u, u), rws, WS_MODP + (kq * 3840 + cc * 64 + lane * 4) * 4);
  }
    STAMP(7);
  }
  unsigned vg[3];
#pragma unroll
  for (int i = 0; i < 3; ++i) { const int c = 64 * i + lane, rw = (c * 171) >> 9, pt = c - 3 * rw; vg[i] = pt < 2 ? 3072 + rw * 32 + pt * 16 : 5120 + rw * 16; tie(vg[i]); }
  if (w == 4) poll(caddr, cbase, 1u, (lane == 1 && p >= 1) || (lane == 2 && p >= 2));
  else if (w == 7) {
    if (b < 240) {
      const int st0 = b / 5, st = 16 * (st0 % 3) + st0 / 3, ct0 = 8 * (b % 5);
      const unsigned a = L_GSRC + 18432 + (8 * g4 + q4) * 32 + p4 * 8, d0 = WS_WT5 + (st & 1) * 122880 + l16;
#pragma unroll
      for (int u = 0; u < 8; ++u) { const int ct = ct0 + u; st16(trpair(L, a + u * 1024, a + u * 1024 + 128), rws, d0 + (ct / 5) * 245760 + strm_off((st >> 1) * 5 + ct % 5, 30)); }
    }
  } else {
    const int mat = b >> 7, jd = b & 15, t = 2 * (v6 >> 1) + blk;
    const unsigned d0 = WS_WT4 + jd * 245760 + (kp >> 2) * 122880 + blk * 30720 + (h * 32 + mat * 16 + i16) * 16;
    float us = 0.f, vs = 0.f;
#pragma unroll
    for (int u = 0; u < 3; ++u) {
      const int sg = (v6 & 1) * 3 + u;
      if (sg == 5) break;
      const int k0 = sg < 4 ? 32 * (sg >> 1) + 4 * (2 * (sg & 1) + h) : 64 + 8 * h, k1 = sg < 4 ? k0 + 16 : k0 + 4;
      const unsigned ca = L_GSRC + (3 * v6 + u) * 1024 + (8 * h + q4) * 64 + blk * 32 + p4 * 8;
      v4u f = trpair(L, ca, ca + 256);
      f32x4 a0 = ldsf4(L, L_MODV + 5760 + k0 * 4), a1 = ldsf4(L, L_MODV + 5760 + k1 * 4);
      f32x4 b0 = ldsf4(L, L_MODV + 6080 + k0 * 4), b1 = ldsf4(L, L_MODV + 6080 + k1 * 4);
      v4u o;
      o[0] = pk2(bflo(f[0]) * a0[0], bfhi(f[0]) * a0[1]); o[1] = pk2(bflo(f[1]) * a0[2], bfhi(f[1]) * a0[3]);
      o[2] = pk2(bflo(f[2]) * a1[0], bfhi(f[2]) * a1[1]); o[3] = pk2(bflo(f[3]) * a1[2], bfhi(f[3]) * a1[3]);
      vs += bflo(f[0]) * b0[0] + bfhi(f[0]) * b0[1] + bflo(f[1]) * b0[2] + bfhi(f[1]) * b0[3] + bflo(f[2]) * b1[0] + bfhi(f[2]) * b1[1] + bflo(f[3]) * b1[2] + bfhi(f[3]) * b1[3];
      us = dot2(o[0], 0x3f803f80u, us); us = dot2(o[1], 0x3f803f80u, us); us = dot2(o[2], 0x3f803f80u, us); us = dot2(o[3], 0x3f803f80u, us);
      st16(o, rws, d0 + strm_off((v6 >> 1) * 40 + (5 * (kp & 3) + sg) * 2, 30));
    }
    us = swap32_add(us); vs = swap32_add(vs);
    const int row = (jd * 6 + t) * 32 + mat * 16 + i16;
    __builtin_amdgcn_raw_buffer_store_b32(__builtin_bit_cast(unsigned, h ? vs : us), rws, WS_UV + (((kp * 2 + (v6 & 1)) * 2 + h) * 3072 + row) * 4, 0, 16);
  }
  bar();
  STAMP(8);
  const int gt = 4 * p - 8 + w;
  {
    const unsigned ko = gt >= 0 ? WS_KV + (gt * 16 + j) * 6144 : 0x7ff00000u, lo = LB + w * KVT;
    dman<3, 0, 0>(lo, l16, rws, ko);
    sfor<3>([&](auto I) { dman<1, 3072 + I * 1024, 0>(lo, vg[I], rws, ko); });
  }
  STAMP(9);
  const unsigned w3 = WS_WT3 + c8 * 106496 + kh * 53248 + m * 13312;
  unsigned cbv;
  {
    waitv<0>();
    if (gt < 0) *(LAS unsigned*)(L + w * KVT + 2056 + l16) = 0xfe96u;
    bar();
    if (tid == 0 && mod2) signal(rws, CT_CM);
    if (tid == 64) signal(rws, CT_CB + ((b & 7) << 7));
    STAMP(10);
    const int hd = kh;
    const v4u q0 = lds16(L, L_Q + m * 2048 + hd * 1024 + l16);
    v2u qh = *(LAS v2u*)(L + L_QHI + m * 512 + lane * 8);
    if (h != hd) qh = v2u{0, 0};
    const v4u q1 = {qh.x, qh.y, 0x3f80u, 0};
    const f32x16 zero = {};
    const unsigned ka = m * KVT + hd * 1024 + l16, kb = m * KVT + 2048 + l16;
    v4u k0[9], k1[9];
    sfor<9>([&](auto I) { constexpr int i = I == 0 ? 0 : I == 1 ? 8 : I - 1; k0[i] = lds16(L, ka + i * KVT); k1[i] = lds16(L, kb + i * KVT); });
    auto qk = [&](auto I, f32x16 c) { f32x16 r = mfma32(k1[I], q1, mfma32(k0[I], q0, c)); tie(r); return r; };
    auto vmax = [](float a, float c) { return __builtin_elementwise_maximum(a, c); };
    auto tmax = [&](const f32x16& v, float t) {
#pragma unroll
      for (int r = 0; r < 16; r += 2) t = vmax(vmax(v[r], v[r + 1]), t);
      tie(t); return t; };
    int dq = r32 - 4 * h; tie(dq);
    f32x16 Sm, S[8];
    float mx = -3.0e38f;
    {
      const f32x16 s0 = qk(IC<0>{}, zero), s8 = qk(IC<8>{}, zero);
      S[1] = qk(IC<1>{}, zero); SB();
#pragma unroll
      for (int r = 0; r < 16; ++r) Sm[r] = (r & 3) + 8 * (r >> 2) <= dq ? s8[r] : s0[r];
      tie(Sm); SB();
    }
    sfor<7>([&](auto I) {
      if constexpr (I < 6) S[I + 2] = qk(IC<I + 2>{}, zero);
      SB();
      if constexpr (I == 0) mx = tmax(Sm, mx); else mx = tmax(S[I], mx);
      SB();
    });
    const unsigned va = m * KVT + 3072 + hd * 1536 + (4 * h + q4) * 48 + (blk == 0 ? (p4 & 1) * 16 + (p4 >> 1) * 8 : (p4 ? 40 : 32));
    v4u vf[18];
    auto rdv = [&](auto I) { vf[2 * I + 1] = trpair(L, va + I * KVT + 768, va + I * KVT + 1152); vf[2 * I] = trpair(L, va + I * KVT, va + I * KVT + 384); };
    rdv(IC<8>{}); rdv(IC<0>{}); SB();
    mx = tmax(S[7], mx);
    { f32x2 t = swp32(mx); mx = vmax(t.x, t.y); }
    f32x16 negm;
#pragma unroll
    for (int r = 0; r < 16; ++r) negm[r] = -mx;
    tie(negm);
    f32x16 O = {};
    S[1] = qk(IC<1>{}, negm); rdv(IC<1>{}); SB();
#pragma unroll
    for (int s = 0; s < 2; ++s) {
      v4u p0, p8;
#pragma unroll
      for (int e = 0; e < 4; ++e) {
        const int r = 8 * s + 2 * e, key = (r & 3) + 8 * (r >> 2);
        const float a = __builtin_amdgcn_exp2f(Sm[r] - mx), c = __builtin_amdgcn_exp2f(Sm[r + 1] - mx);
        const float a8 = key <= dq ? a : 0.f, c8_ = key + 1 <= dq ? c : 0.f;
        p8[e] = pk2(a8, c8_); p0[e] = pk2(a - a8, c - c8_);
      }
      O = mfma32(vf[s], p0, O); O = mfma32(vf[16 + s], p8, O); tie(O); SB();
    }
    sfor<7>([&](auto I) {
      constexpr int i = I + 1;
      if constexpr (i < 7) { S[i + 1] = qk(IC<i + 1>{}, negm); rdv(IC<i + 1>{}); SB(); }
      v4u pf[2];
#pragma unroll
      for (int e = 0; e < 8; ++e) pf[e / 4][e % 4] = pk2(__builtin_amdgcn_exp2f(S[i][2 * e]), __builtin_amdgcn_exp2f(S[i][2 * e + 1]));
      O = mfma32(vf[2 * i], pf[0], O); O = mfma32(vf[2 * i + 1], pf[1], O); tie(O);
      if constexpr (I < 4) dman<2, L_W3A + I * 2048, I * 2048>(LB + kh * 32768 + m * 8192, l16, rws, w3);
      SB();
    });
    const float inv = __builtin_amdgcn_rcpf(swap32_hi(O[8]));
    const int tok = 128 * p + 32 * m + r32;
    const unsigned ob = WS_OF + (tok >> 4) * 20480;
    STAMP(11);
    st16(v4u{pk2(O[0] * inv, O[1] * inv), pk2(O[2] * inv, O[3] * inv), pk2(O[4] * inv, O[5] * inv), pk2(O[6] * inv, O[7] * inv)}, rws, ob + ((j & 1) * 10 + (j >> 1)) * 1024 + (2 * hd + h) * 256 + (tok & 15) * 16);
    if (h == 0) st8(v2u{pk2(O[8] * inv, O[9] * inv), pk2(O[10] * inv, O[11] * inv)}, rws, ob + (((j >> 2) & 1) * 10 + 8 + (j >> 3)) * 1024 + hd * 512 + (j & 3) * 128 + (tok & 15) * 8);
  }
  waitv<0>();
  bar();
  const int tt = 8 * p + 4 * rh + m;
  unsigned x2k[6];
  {
    v2u xr[3];
    const unsigned xo = (16 * tt + i16) * 1280 + (80 * c8 + 4 * g4) * 2;
    if (tid == 0) signal(rws, CT_C2 + (p << 7));
    STAMP(12);
    v4u mp[4];
    ld8p(xr[0], xo, rx, kh * 64); ld8p(xr[1], xo, rx, kh * 64 + 32); ld8p(xr[2], xo, rx, 128);
    dman<4, 0, 8192>(LB + kh * 20480 + m * 5120, l16, rws, w3); dman<1, 4096, 12288>(LB + kh * 20480 + m * 5120, l16, rws, w3);
    if (w == 4) poll(caddr, cbase, lane == 7 ? 80u : 16u, lane == 3 || lane == 7);
    bar();
    STAMP(13);
    v4u af[10], wf[50];
    const unsigned ab = WS_OF + (tt * 20 + kh * 10) * 1024;
    v2u ah[4];
    sfor<4>([&](auto I) { ld16(mp[I], w == 5 ? ((lane < 20 ? 1280 : 3200 - 80) + 80 * c8 + 4 * (lane < 40 ? lane : 20)) * 4 : 0x7ff00000, rws, WS_MODP + I * 15360); });
    sfor<8>([&](auto I) { ldf<I>(af[I], l16, rws, ab); });
    sfor<4>([&](auto I) { ldh<I * 512>(ah[I], lane * 8, rws, ab + 8192); });
    peek(cbv, lane >= 8 && lane < 16 ? caddr : (const GAS unsigned*)P.ws);
    f32x4 acc[5] = {};
    unsigned wa = L_W3A + kh * 32768 + l16; tie(wa);
    const unsigned wb = kh * 20480 + l16;
    auto rdw = [&](auto N) {
      constexpr int n = 5 * (N / 5) + 4 - N % 5;
      if constexpr (n < 32) wf[n] = lds16(L, wa + (n & 3) * 8192 + (n >> 2) * 1024); else wf[n] = lds16(L, wb + (n & 3) * 5120 + ((n >> 2) - 8) * 1024);
    };
    sfor<10>([&](auto E) {
      constexpr int e = E;
      if constexpr (!(e & 1) && e < 8) { waitv<11 - e>(); tie(af[e]); tie(af[e + 1]); }
      if constexpr (e == 8) { waitv<1>(); sfor<2>([&](auto I) { tie(ah[2 * I]); tie(ah[2 * I + 1]); af[8 + I] = v4u{ah[2 * I].x, ah[2 * I].y, ah[2 * I + 1].x, ah[2 * I + 1].y}; }); }
      if constexpr (e == 0) { rbar(); sfor<8>(rdw); }
      if constexpr (e == 1) { if (w == 5) mod_put(mp, false, lane < 40, L_MODV + 5120 + l16); }
      sfor<5>([&](auto K) {
        constexpr int n = 5 * e + K;
        acc[K] = mfma16(wf[n], af[e], acc[K]); tie(acc[K]);
        if constexpr (n + 8 < 50) rdw(IC<n + 8>{});
        SB();
      });
    });
    STAMP(14);
    const unsigned xs = 57344 + m * 5120;
    if (kh == 0) { *(LAS f32x4*)(L + xs + l16) = acc[2]; *(LAS f32x4*)(L + xs + 1024 + l16) = acc[3]; }
    else { *(LAS f32x4*)(L + xs + 2048 + l16) = acc[0]; *(LAS f32x4*)(L + xs + 3072 + l16) = acc[1]; *(LAS f32x4*)(L + xs + 4096 + l16) = acc[4]; }
    bar();
    tie(xr[0]); tie(xr[1]); tie(xr[2]);
    f32x4 r0, r1, r2 = {};
    if (kh == 0) { r0 = acc[0] + ldsf4(L, xs + 2048 + l16); r1 = acc[1] + ldsf4(L, xs + 3072 + l16); r2 = acc[4] + ldsf4(L, xs + 4096 + l16); }
    else { r0 = acc[2] + ldsf4(L, xs + l16); r1 = acc[3] + ldsf4(L, xs + 1024 + l16); }
    const f32x4 ga0 = ldsf4(L, L_MODV + 5120 + (32 * kh + 4 * g4) * 4), ga1 = ldsf4(L, L_MODV + 5120 + (32 * kh + 16 + 4 * g4) * 4), ga2 = ldsf4(L, L_MODV + 5120 + (64 + 4 * g4) * 4);
    x2k[0] = pk2(bflo(xr[0].x) + ga0[0] * r0[0], bfhi(xr[0].x) + ga0[1] * r0[1]); x2k[1] = pk2(bflo(xr[0].y) + ga0[2] * r0[2], bfhi(xr[0].y) + ga0[3] * r0[3]);
    x2k[2] = pk2(bflo(xr[1].x) + ga1[0] * r1[0], bfhi(xr[1].x) + ga1[1] * r1[1]); x2k[3] = pk2(bflo(xr[1].y) + ga1[2] * r1[2], bfhi(xr[1].y) + ga1[3] * r1[3]);
    x2k[4] = pk2(bflo(xr[2].x) + ga2[0] * r2[0], bfhi(xr[2].x) + ga2[1] * r2[1]); x2k[5] = pk2(bflo(xr[2].y) + ga2[2] * r2[2], bfhi(xr[2].y) + ga2[3] * r2[3]);
    if (kh) { x2k[4] = 0; x2k[5] = 0; }
    const unsigned xb = WS_X2F + ((tt >> 1) * 40 + 5 * c8) * 1024 + ((tt & 1) * 16 + i16) * 16;
    st16(v4u{x2k[0], x2k[1], x2k[2], x2k[3]}, rws, xb + (2 * kh + (g4 >> 1)) * 1024 + (g4 & 1) * 512);
    if (kh == 0) st8(v2u{x2k[4], x2k[5]}, rws, xb + 4096 + (g4 >> 1) * 512 + (g4 & 1) * 8);
    float s1 = 0.f, s2 = 0.f;
#pragma unroll
    for (int e = 0; e < 6; ++e) { s1 = dot2(x2k[e], 0x3f803f80u, s1); s2 = dot2(x2k[e], x2k[e], s2); }
    s1 = swap32_add(swap16_add(s1)); s2 = swap32_add(swap16_add(s2));
    if (g4 == 0) st8(v2u{__builtin_bit_cast(unsigned, s1), __builtin_bit_cast(unsigned, s2)}, rws, WS_ST + ((tt >> 1) * 16 + 2 * c8 + kh) * 256 + ((tt & 1) * 16 + i16) * 8);
  }
  waitv<0>();
  tie(cbv);
  if (w == 4) *(LAS unsigned*)(L + L_CNT) = all64(lane < 8 || lane >= 16 || cbv - cbase >= 32u);
  bar();
  const bool early = __builtin_amdgcn_readfirstlane(*(LAS unsigned*)(L + L_CNT)) != 0;
  {
    const unsigned w4 = WS_WT4 + j * 245760 + kh * 122880 + m * 30720, w5 = WS_WT5 + c8 * 245760 + kh * 122880 + m * 30720 - 30720;
    if (tid == 0) signal(rws, CT_C3 + (p << 7));
    STAMP(15);
    if (!early) { if (w == 4) poll(caddr, cbase, 32u, lane >= 8 && lane < 16); bar(); }
    auto pre4 = [&]() { dman<4, 0, 0>(ringD, l16, rws, w4); dman<4, 4096, 4096>(ringD, l16, rws, w4); dman<R4 - 8, 8192, 8192>(ringD, l16, rws, w4); };
    if (w == 4) { pre4(); poll(caddr, cbase, 16u, lane == 4); }
    else {
      const int rq = (v6 < 6 ? v6 : 0) * 16 + i16;
      v4u uv[4];
      sfor<4>([&](auto I) { ld16(uv[I], (g4 * 8 + rq / 48) * 12288 + (j * 192 + 4 * (rq % 48)) * 4, rws, WS_UV + I * 24576); });
      pre4();
      waitv<R4>();
      sfor<4>([&](auto I) { tie(uv[I]); });
      f32x4 u = (__builtin_bit_cast(f32x4, uv[0]) + __builtin_bit_cast(f32x4, uv[1])) + (__builtin_bit_cast(f32x4, uv[2]) + __builtin_bit_cast(f32x4, uv[3]));
#pragma unroll
      for (int e = 0; e < 4; ++e) { float t = u[e]; u[e] = swap32_add(swap16_add(t)); }
      if (v6 < 6 && lane < 16) *(LAS f32x4*)(L + L_UVS + rq * 16) = u;
    }
    bar();
    STAMP(16);
    v4u af[20], a5[24], stv[4], wf[240]; unsigned pk;
    const unsigned ab = WS_X2F + ((4 * p + m) * 40 + 20 * kh) * 1024, ab5 = WS_ACT + (tt * 48 + 24 * kh) * 1024;
    sfor<10>([&](auto I) { ldf<I>(af[I], l16, rws, ab); });
    sfor<4>([&](auto I) { ldf<I>(stv[I], l16, rws, WS_ST + (4 * p + m) * 4096); });
    const int tok = 128 * p + 32 * m + r32;
    const unsigned ao = WS_ACT + ((tok >> 4) * 48 + (j & 1) * 24 + (j >> 1)) * 1024 + kh * 512 + h * 256 + (tok & 15) * 16;
    f32x16 aK[3] = {}, aS[3] = {};
    f32x4 acc[5] = {};
    const unsigned wbK = ringL + kh * 15360, wbS = ringL + 15360 - kh * 15360;
    unsigned xw = L_BSRC + w * 4096 + l16, xr_ = L_BSRC + (w ^ 4) * 4096 + l16, ub = L_UVS + kh * 128 + h * 16; tie(xw); tie(xr_); tie(ub);
    float rs2, nm2, av[8]; f32x4 eo[4], eu[4], ev[4];
    constexpr auto isS = [](int n) { return (n / 10) % 4 == 3 ? n % 10 < 5 : (n & 1) != 0; };
    constexpr auto stp = [](int n) { return (n / 10) % 4 == 3 ? (n / 10) * 5 + n % 5 : n / 2; };
    auto rdw = [&](auto N) {
      constexpr int n = N < 120 ? (N ^ 1) : 5 * (N / 5) + 4 - N % 5;
      if constexpr (n < 120) wf[n] = lds16(L, (isS(n) ? wbS : wbK) + ring_off(2 * stp(n))); else wf[n] = lds16(L, ringL + ring_off(n));
    };
    auto vmem = [&](auto Q, auto K) {
      constexpr int k = SC.slot(Q, K, 10);
      if constexpr (k >= 0) {
        constexpr int ty = SC.ty[k], ix = SC.ix[k];
        if constexpr (ty == OP_A) ldf<ix>(af[ix], l16, rws, ab);
        else if constexpr (ty == OP_W) dman<SC.cn[k], (ix % 15) * 1024, ix * 1024>(ringD, l16, rws, ix < 30 ? w4 : w5);
        else if constexpr (ty == OP_ST) ldf<ix>(stv[ix], l16, rws, WS_ST + (4 * p + m) * 4096);
        else if constexpr (ty == OP_PEEK) { if (w == 4) ld4(pk, lane == (ix ? 15 + ix : 5) ? CT_C4 + p * 384 + ix * 128 : 0x7ff00000, rws, 0); }
        else if constexpr (ty == OP_A5) ldf<ix>(a5[ix], l16, rws, ab5);
      }
    };
    auto epiA = [&](auto G, auto QD) { const f32x16 sd = aS[G]; *(LAS f32x4*)(L + xw + QD * 1024) = f32x4{sd[4 * QD], sd[4 * QD + 1], sd[4 * QD + 2], sd[4 * QD + 3]}; };
    auto epi = [&](auto G, auto K) {
      if constexpr (K < 2) sfor<2>([&](auto I) { constexpr int qd = 2 * K + I; eo[qd] = ldsf4(L, xr_ + qd * 1024); eu[qd] = ldsf4(L, ub + G * 256 + qd * 32); ev[qd] = ldsf4(L, ub + 768 + G * 256 + qd * 32); });
      else if constexpr (K < 18) { constexpr int r = K - 2; float t = fmaf(aK[G][r] + eo[r / 4][r % 4], rs2, fmaf(nm2, eu[r / 4][r % 4], ev[r / 4][r % 4])); tie(t); aK[G][r] = t; }
      else if constexpr (K < 26) { constexpr int k = K - 18; const float gv = aK[G][k]; float t = gv * __builtin_amdgcn_rcpf(1.f + __builtin_amdgcn_exp2f(-1.44269504f * gv)) * aK[G][8 + k]; tie(t); av[k] = t; }
      else if constexpr (K == 29) st16(v4u{pk2(av[0], av[1]), pk2(av[2], av[3]), pk2(av[4], av[5]), pk2(av[6], av[7])}, rws, ao + G * 8192);
    };
    auto top = [&](auto Q) {
      constexpr int q = Q;
      waitv<sc_wait(q)>();
      if constexpr (q < 4) sfor<5>([&](auto I) { tie(af[5 * q + I]); });
      if constexpr (q >= 13) { tie(a5[2 * (q - 13)]); tie(a5[2 * (q - 13) + 1]); }
      if constexpr (q == 11 || q == 13 || q == 18) {
        constexpr int ln = q == 11 ? 5 : q == 13 ? 16 : 17;
        if (w == 4) { waitv<SC.before[q] - sc_peek(q == 11 ? 0 : q == 13 ? 1 : 2)>(); tie(pk); if (!all64(lane != ln || pk - cbase >= 16u)) poll(caddr, cbase, 16u, lane == ln); }
      }
      if constexpr (q == 4 || q == 8 || q == 12) bar(); else rbar();
      if constexpr (q == 8 || q == 11 || q == 14) { if (tid == 64) signal(rws, CT_C4 + p * 384 + (q == 8 ? 0 : q == 11 ? 128 : 256)); }
    };
    sfor<12>([&](auto Q) {
      constexpr int q = Q, g = q / 4;
      top(Q);
      if constexpr (q == 0) sfor<6>(rdw);
      sfor<10>([&](auto K) {
        constexpr int n = 10 * q + K, i = stp(n) % 20;
        if constexpr (isS(n)) { aS[g] = mfma32(wf[n], af[i], aS[g]); tie(aS[g]); } else { aK[g] = mfma32(wf[n], af[i], aK[g]); tie(aK[g]); }
        if constexpr (n + 6 < 120) rdw(IC<n + 6>{});
        vmem(Q, K);
        if constexpr (q == 2 && K == 4) {
          sfor<4>([&](auto I) { tie(stv[I]); });
          f32x4 v = (__builtin_bit_cast(f32x4, stv[0]) + __builtin_bit_cast(f32x4, stv[1])) + (__builtin_bit_cast(f32x4, stv[2]) + __builtin_bit_cast(f32x4, stv[3]));
          float u[4];
#pragma unroll
          for (int e = 0; e < 4; ++e) { float a = v[e]; a = swap32_add(swap16_add(a)); u[e] = __builtin_bit_cast(float, __builtin_amdgcn_ds_bpermute((r32 >> 1) * 4, __builtin_bit_cast(int, a))); }
          const float s1 = (lane & 1) ? u[2] : u[0], s2 = (lane & 1) ? u[3] : u[1];
          const float mean = s1 * (1.f / 640.f);
          rs2 = __builtin_amdgcn_rsqf(fmaxf(s2 * (1.f / 640.f) - mean * mean, 0.f) + 1e-5f); nm2 = -mean * rs2; tie(rs2); tie(nm2);
        }
        if constexpr (q % 4 == 3 && K >= 6) epiA(IC<g>{}, IC<K - 6>{});
        if constexpr (g == 1 && q % 4 < 3) epi(IC<0>{}, IC<10 * (q % 4) + K>{});
        if constexpr (q == 8) { if constexpr (K < 2) epi(IC<1>{}, K); else { epi(IC<1>{}, IC<2 * K - 2>{}); epi(IC<1>{}, IC<2 * K - 1>{}); } }
        if constexpr (q == 9) epi(IC<1>{}, IC<(K < 9 ? 18 + K : 29)>{});
        SB();
      });
    });
    STAMP(17);
    top(IC<12>{});
    sfor<30>([&](auto K) { if constexpr (K % 3 == 0) vmem(IC<12>{}, IC<K / 3>{}); epi(IC<2>{}, K); SB(); });
    STAMP(19);
    sfor<12>([&](auto E) {
      constexpr int e = E;
      top(IC<13 + e>{});
      if constexpr (e == 0) sfor<8>([&](auto N) { rdw(IC<120 + N>{}); });
      sfor<10>([&](auto K) {
        constexpr int n = 10 * e + K;
        acc[n % 5] = mfma16(wf[120 + n], a5[n / 5], acc[n % 5]); tie(acc[n % 5]);
        if constexpr (n + 8 < 120) rdw(IC<128 + n>{});
        vmem(IC<13 + e>{}, K);
        SB();
      });
    });
    STAMP(20);
    const unsigned xs = L_BSRC + m * 5120;
    if (kh == 0) { *(LAS f32x4*)(L + xs + l16) = acc[2]; *(LAS f32x4*)(L + xs + 1024 + l16) = acc[3]; }
    else { *(LAS f32x4*)(L + xs + 2048 + l16) = acc[0]; *(LAS f32x4*)(L + xs + 3072 + l16) = acc[1]; *(LAS f32x4*)(L + xs + 4096 + l16) = acc[4]; }
    bar();
    f32x4 r0, r1, r2 = {};
    if (kh == 0) { r0 = acc[0] + ldsf4(L, xs + 2048 + l16); r1 = acc[1] + ldsf4(L, xs + 3072 + l16); r2 = acc[4] + ldsf4(L, xs + 4096 + l16); }
    else { r0 = acc[2] + ldsf4(L, xs + l16); r1 = acc[3] + ldsf4(L, xs + 1024 + l16); }
    const f32x4 g0 = ldsf4(L, L_MODV + 5440 + (32 * kh + 4 * g4) * 4), g1 = ldsf4(L, L_MODV + 5440 + (32 * kh + 16 + 4 * g4) * 4), g2 = ldsf4(L, L_MODV + 5440 + (64 + 4 * g4) * 4);
    GAS char* o = (GAS char*)P.out + (size_t)(16 * tt + i16) * 1280 + (80 * c8 + 4 * g4) * 2;
    *(GAS v2u*)(o + kh * 64) = v2u{pk2(bflo(x2k[0]) + g0[0] * r0[0], bfhi(x2k[0]) + g0[1] * r0[1]), pk2(bflo(x2k[1]) + g0[2] * r0[2], bfhi(x2k[1]) + g0[3] * r0[3])};
    *(GAS v2u*)(o + kh * 64 + 32) = v2u{pk2(bflo(x2k[2]) + g1[0] * r1[0], bfhi(x2k[2]) + g1[1] * r1[1]), pk2(bflo(x2k[3]) + g1[2] * r1[2], bfhi(x2k[3]) + g1[3] * r1[3])};
    if (kh == 0) *(GAS v2u*)(o + 128) = v2u{pk2(bflo(x2k[4]) + g2[0] * r2[0], bfhi(x2k[4]) + g2[1] * r2[1]), pk2(bflo(x2k[5]) + g2[2] * r2[2], bfhi(x2k[5]) + g2[3] * r2[3])};
    if (b == 0 && tid == 0) __hip_atomic_store((GAS unsigned*)(P.ws + CT_E), cbase + 1u, RLX_AGENT);
    STAMP(21);
  }
}

#if defined(__HIP_DEVICE_COMPILE__)
#pragma clang attribute pop
#endif
extern "C" void kernel_launch(void* const* d_in, const int* in_sizes, int n_in, void* d_out, int out_size, void* d_ws, size_t ws_size,
                              hipStream_t stream) {
  Params P;
  P.x = (const u16*)d_in[0]; P.wqkv = (const u16*)d_in[1]; P.wo = (const u16*)d_in[2];
  P.wg = (const u16*)d_in[4]; P.wu = (const u16*)d_in[5]; P.wd = (const u16*)d_in[6];
  P.c = (const u16*)d_in[7]; P.wmod = (const u16*)d_in[8];
  P.out = (u16*)d_out; P.ws = (char*)d_ws;
  fused_block<<<256, 512, 0, stream>>>(P);
}
```

```cpp
#include <hip/hip_runtime.h>
#include <hip/hip_bf16.h>
#include <utility>

typedef unsigned short u16;
typedef unsigned v4u __attribute__((ext_vector_type(4)));
typedef unsigned v2u __attribute__((ext_vector_type(2)));
typedef short v4s __attribute__((ext_vector_type(4)));
typedef float f32x2 __attribute__((ext_vector_type(2)));
typedef float f32x4 __attribute__((ext_vector_type(4)));
typedef float f32x16 __attribute__((ext_vector_type(16)));
typedef __bf16 bf16x2 __attribute__((ext_vector_type(2)));
typedef __bf16 bf16x8 __attribute__((ext_vector_type(8)));
typedef _Float16 f16x2 __attribute__((ext_vector_type(2)));
typedef __amdgpu_buffer_rsrc_t rsrc_t;
#define LAS __attribute__((address_space(3)))
#define GAS __attribute__((address_space(1)))
#define DI __device__ __forceinline__
#define RLX_AGENT __ATOMIC_RELAXED, __HIP_MEMORY_SCOPE_AGENT

#define DELAY0 20
#define R4 10
#define STAMPS 0

constexpr int WS_CTRL = 0;
constexpr int WS_MODP = 65536;
constexpr int WS_ST   = 131072;
constexpr int WS_UV   = 393216;
constexpr int WS_WT1  = 786432;
constexpr int WS_WT3  = WS_WT1 + 16 * 163840;
constexpr int WS_WT4  = WS_WT3 + 8 * 106496;
constexpr int WS_WT5  = WS_WT4 + 16 * 245760;
constexpr int WS_KV   = WS_WT5 + 8 * 245760;
constexpr int WS_OF   = WS_KV + 64 * 16 * 6144;
constexpr int WS_X2F  = WS_OF + 128 * 20 * 1024;
constexpr int WS_ACT  = WS_X2F + 64 * 40 * 1024;
constexpr int WS_XH   = WS_ACT + 128 * 48 * 1024;
constexpr int WS_END  = WS_XH + 64 * 40 * 1024;
constexpr int WS_DBG  = WS_END;
constexpr int CT_E = 0, CT_FLAG = 4096  , CT_C1 = 16384  , CT_C2 = 49152, CT_C3 = CT_C2 + 2048,
              CT_C4 = CT_C3 + 2048  , CT_CB = CT_C4 + 6144  , CT_CD = CT_CB + 1024  , CT_CM = CT_CD + 1024, CT_GO = 12288  ;
constexpr int L_WQ = 0, L_RED = 10240;
constexpr int L_BSRC = 122880;
constexpr int L_MODV = 155648;
constexpr int L_CNT = 162816;
constexpr int L_UVS = 155648;
constexpr int KVT = 6144;
constexpr int L_Q = 73728, L_QHI = 81920, L_RED2 = 112640  , L_W3A = 86016, L_GSRC = 122880  ;
constexpr unsigned SPIN_CAP = 1u << 20;

#if defined(__HIP_DEVICE_COMPILE__)
#pragma clang attribute push (__attribute__((target("no-packed-fp32-ops"))), apply_to = function)
#endif
struct Params { const u16 *x, *wqkv, *wo, *wg, *wu, *wd, *c, *wmod; u16* out; char* ws; };

DI f32x16 mfma32(v4u a, v4u b, f32x16 c) { return __builtin_amdgcn_mfma_f32_32x32x16_bf16(__builtin_bit_cast(bf16x8, a), __builtin_bit_cast(bf16x8, b), c, 0, 0, 0); }
DI f32x4 mfma16(v4u a, v4u b, f32x4 c) { return __builtin_amdgcn_mfma_f32_16x16x32_bf16(__builtin_bit_cast(bf16x8, a), __builtin_bit_cast(bf16x8, b), c, 0, 0, 0); }
DI unsigned pk2(float a, float b) { f32x2 v = {a, b}; return __builtin_bit_cast(unsigned, __builtin_convertvector(v, bf16x2)); }
DI float bflo(unsigned x) { return __builtin_bit_cast(float, x << 16); }
DI float bfhi(unsigned x) { return __builtin_bit_cast(float, x & 0xffff0000u); }
DI float bfr(float a) { return bflo(pk2(a, a)); }
DI f32x2 swp32(float v) {
  unsigned a = __builtin_bit_cast(unsigned, v), c = a; asm("" : "+v"(c));
  auto r = __builtin_amdgcn_permlane32_swap(a, c, false, false);
  unsigned r0 = r[0], r1 = r[1];
  return f32x2{__builtin_bit_cast(float, r0), __builtin_bit_cast(float, r1)};
}
DI f32x2 swp16(float v) {
  unsigned a = __builtin_bit_cast(unsigned, v), c = a; asm("" : "+v"(c));
  auto r = __builtin_amdgcn_permlane16_swap(a, c, false, false);
  unsigned r0 = r[0], r1 = r[1];
  return f32x2{__builtin_bit_cast(float, r0), __builtin_bit_cast(float, r1)};
}
DI float swap32_add(float v) { f32x2 r = swp32(v); return r.x + r.y; }
DI float swap16_add(float v) { f32x2 r = swp16(v); return r.x + r.y; }
DI float swap32_max(float v) { f32x2 r = swp32(v); return fmaxf(r.x, r.y); }
DI float swap32_hi(float v) { return swp32(v).y; }
DI bool all64(bool c) { return __builtin_amdgcn_ballot_w64(!c) == 0ull; }
DI float ror8_add(float v) { return v + __builtin_bit_cast(float, __builtin_amdgcn_update_dpp(0, __builtin_bit_cast(int, v), 0x128, 0xf, 0xf, true)); }

template <bool SC1> DI void dma(unsigned lds_addr, unsigned voff, rsrc_t rs, unsigned soff) {
  if (SC1) asm volatile("s_mov_b32 m0, %0\n\ts_nop 0\n\tbuffer_load_dwordx4 %1, %2, %3 offen sc1 lds" ::"s"(lds_addr), "v"(voff), "s"(rs), "s"(soff) : "memory");
  else     asm volatile("s_mov_b32 m0, %0\n\ts_nop 0\n\tbuffer_load_dwordx4 %1, %2, %3 offen lds" ::"s"(lds_addr), "v"(voff), "s"(rs), "s"(soff) : "memory");
}
template <int N, int LO, int GO> DI void dman(unsigned lb, unsigned voff, rsrc_t rs, unsigned gb) {
  unsigned t;
#define DMA_HEAD "s_add_u32 m0, %1, %5\n\ts_add_u32 %0, %4, %6\n\tbuffer_load_dwordx4 %2, %3, %0 offen sc1 lds"
#define DMA_MORE(o) "\n\tbuffer_load_dwordx4 %2, %3, %0 offen offset:" #o " sc1 lds"
#define DMA_ARGS : "=&s"(t) : "s"(lb), "v"(voff), "s"(rs), "s"(gb), "n"(LO), "n"(GO) : "memory", "scc"
  if constexpr (N == 1) asm volatile(DMA_HEAD DMA_ARGS);
  if constexpr (N == 2) asm volatile(DMA_HEAD DMA_MORE(1024) DMA_ARGS);
  if constexpr (N == 3) asm volatile(DMA_HEAD DMA_MORE(1024) DMA_MORE(2048) DMA_ARGS);
  if constexpr (N == 4) asm volatile(DMA_HEAD DMA_MORE(1024) DMA_MORE(2048) DMA_MORE(3072) DMA_ARGS);
}
template <int I> DI void ldf(v4u& d, unsigned voff, rsrc_t rs, unsigned base) {
  asm volatile("buffer_load_dwordx4 %0, %1, %2, %3 offen offset:%4 sc1" : "=v"(d) : "v"(voff), "s"(rs), "s"(base + (I / 4) * 4096), "n"((I % 4) * 1024) : "memory");
}
template <int O> DI void ldh(v2u& d, unsigned voff, rsrc_t rs, unsigned base) {
  asm volatile("buffer_load_dwordx2 %0, %1, %2, %3 offen offset:%4 sc1" : "=v"(d) : "v"(voff), "s"(rs), "s"(base), "n"(O) : "memory");
}
DI void signal(rsrc_t rs, unsigned off) {
  asm volatile("buffer_atomic_add %0, %1, %2, 0 offen" ::"v"(1u), "v"(off), "s"(rs) : "memory");
}
DI void peek(unsigned& d, const GAS unsigned* a) { asm volatile("global_load_dword %0, %1, off sc1" : "=v"(d) : "v"(a) : "memory"); }
DI void ld16(v4u& d, unsigned voff, rsrc_t rs, unsigned soff) { asm volatile("buffer_load_dwordx4 %0, %1, %2, %3 offen sc1" : "=v"(d) : "v"(voff), "s"(rs), "s"(soff) : "memory"); }
DI void ld16p(v4u& d, unsigned voff, rsrc_t rs, unsigned soff) { asm volatile("buffer_load_dwordx4 %0, %1, %2, %3 offen" : "=v"(d) : "v"(voff), "s"(rs), "s"(soff) : "memory"); }
DI void ld8p(v2u& d, unsigned voff, rsrc_t rs, unsigned soff) { asm volatile("buffer_load_dwordx2 %0, %1, %2, %3 offen" : "=v"(d) : "v"(voff), "s"(rs), "s"(soff) : "memory"); }
DI void ld2p(unsigned& d, unsigned voff, rsrc_t rs, unsigned soff) { asm volatile("buffer_load_ushort %0, %1, %2, %3 offen" : "=v"(d) : "v"(voff), "s"(rs), "s"(soff) : "memory"); }
DI void ld4(unsigned& d, unsigned voff, rsrc_t rs, unsigned soff) { asm volatile("buffer_load_dword %0, %1, %2, %3 offen sc1" : "=v"(d) : "v"(voff), "s"(rs), "s"(soff) : "memory"); }
template <int N> DI void waitv() { asm volatile("s_waitcnt vmcnt(%0)" ::"n"(N) : "memory"); }
template <class T> DI void tie(T& a) { asm volatile("" : "+v"(a)); }
DI void rbar() { asm volatile("s_barrier" ::: "memory"); }
DI void bar() { asm volatile("s_waitcnt lgkmcnt(0)\n\ts_barrier" ::: "memory"); }
DI void st16(v4u v, rsrc_t rs, unsigned off) { __builtin_amdgcn_raw_buffer_store_b128(v, rs, off, 0, 16); }
DI void st8(v2u v, rsrc_t rs, unsigned off) { __builtin_amdgcn_raw_buffer_store_b64(v, rs, off, 0, 16); }
DI v4u trpair(LAS char* L, unsigned a0, unsigned a1) {
  v4s lo = __builtin_amdgcn_ds_read_tr16_b64_v4i16((LAS v4s*)(L + a0));
  v4s hi = __builtin_amdgcn_ds_read_tr16_b64_v4i16((LAS v4s*)(L + a1));
  v2u l = __builtin_bit_cast(v2u, lo), h = __builtin_bit_cast(v2u, hi);
  return v4u{l.x, l.y, h.x, h.y};
}
DI v4u lds16(LAS char* L, unsigned a) { return *(LAS v4u*)(L + a); }
DI f32x4 ldsf4(LAS char* L, unsigned a) { return *(LAS f32x4*)(L + a); }

template <int... I, class F> DI void sfor_impl(std::integer_sequence<int, I...>, F&& f) { (f(std::integral_constant<int, I>{}), ...); }
template <int N, class F> DI void sfor(F&& f) { sfor_impl(std::make_integer_sequence<int, N>{}, f); }
#define SB() __builtin_amdgcn_sched_barrier(0)

template <int N> using IC = std::integral_constant<int, N>;
DI void affine4(unsigned x0, unsigned x1, f32x4 a, f32x4 s, unsigned& o0, unsigned& o1) {
  float t0, t1;
  const float a0 = a[0], a1 = a[1], a2 = a[2], a3 = a[3], s0 = s[0], s1 = s[1], s2 = s[2], s3 = s[3];
  asm volatile("v_fma_mix_f32 %0, %4, %6, %10 op_sel_hi:[1,0,0]\n\tv_fma_mix_f32 %2, %4, %7, %11 op_sel:[1,0,0] op_sel_hi:[1,0,0]\n\t"
      "v_fma_mix_f32 %1, %5, %8, %12 op_sel_hi:[1,0,0]\n\tv_fma_mix_f32 %3, %5, %9, %13 op_sel:[1,0,0] op_sel_hi:[1,0,0]\n\t"
      "v_cvt_pk_bf16_f32 %0, %0, %2\n\tv_cvt_pk_bf16_f32 %1, %1, %3"
      : "=&v"(o0), "=&v"(o1), "=&v"(t0), "=&v"(t1) : "v"(x0), "v"(x1), "v"(a0), "v"(a1), "v"(a2), "v"(a3), "v"(s0), "v"(s1), "v"(s2), "v"(s3));
}
constexpr int ring_off(int pc) { return (pc & 3) * 15360 + ((pc >> 2) % 15) * 1024; }
DI unsigned strm_off(int pc, int rounds) { return ((pc & 3) * rounds + (pc >> 2)) * 1024; }
DI float dot2(unsigned a, unsigned b, float c) { return __builtin_amdgcn_fdot2_f32_bf16(__builtin_bit_cast(bf16x2, a), __builtin_bit_cast(bf16x2, b), c, false); }
DI f32x2 xswp32(float a, float c) { auto r = __builtin_amdgcn_permlane32_swap(__builtin_bit_cast(unsigned, a), __builtin_bit_cast(unsigned, c), false, false); unsigned r0 = r[0], r1 = r[1]; return f32x2{__builtin_bit_cast(float, r0), __builtin_bit_cast(float, r1)}; }
DI f32x2 xswp16(float a, float c) { auto r = __builtin_amdgcn_permlane16_swap(__builtin_bit_cast(unsigned, a), __builtin_bit_cast(unsigned, c), false, false); unsigned r0 = r[0], r1 = r[1]; return f32x2{__builtin_bit_cast(float, r0), __builtin_bit_cast(float, r1)}; }

enum { OP_A, OP_W, OP_ST, OP_UV, OP_STORE, OP_PEEK, OP_A5, OP_MW, OP_MC };
struct Sched {
  int n, tot; int ty[128], ix[128], cn[128], pos[128], start[27], before[27];
  constexpr void put(int t, int i, int c) { ty[n] = t; ix[n] = i; cn[n] = c; pos[n] = tot; tot += c; ++n; }
  constexpr void puts(int t, int i, int c) { for (int k = 0; k < c; ++k) put(t, i + k, 1); }
  constexpr void putw(int r0, int r1) {
    while (r0 < r1) { int c = r1 - r0; if (c > 2) c = 2; if (r0 % 15 + c > 15) c = 15 - r0 % 15; put(OP_W, r0, c); r0 += c; } }
  constexpr void seg(int q) { start[q] = n; before[q] = tot; }
  constexpr int slot(int q, int K, int ns) const {
    int c = start[q + 1] - start[q]; if (c && ty[start[q + 1] - 1] == OP_STORE) --c;
    for (int k = 0; k < c; ++k) if ((k * ns) / c == K) return start[q] + k;
    return -1; }
  constexpr int at(int t, int i) const { for (int k = 0; k < n; ++k) if (ty[k] == t && i >= ix[k] && i < ix[k] + cn[k]) return pos[k] + i - ix[k]; return -1; }
};
constexpr int cmax(int a, int b) { return a > b ? a : b; }
constexpr Sched make_sched() {
  Sched s{};
  s.putw(0, R4); s.puts(OP_A, 0, 10); s.puts(OP_ST, 0, 4);
  const int wto[25] = {R4, R4 + 2, R4 + 5 > 16 ? 16 : R4 + 5, 19, 23, 27, 30, 32, 35, 37, 40, 42, 45, 45, 47, 49, 51, 53, 55, 57, 58, 60, 60, 60, 60};
  const int a5to[25] = {0, 0, 0, 0, 0, 0, 0, 0, 0, 0, 0, 4, 8, 11, 13, 15, 16, 16, 18, 20, 23, 24, 24, 24, 24};
  for (int q = 0; q < 25; ++q) {
    s.seg(q);
    if (q == 10 || q == 17) s.put(OP_PEEK, q == 10 ? 0 : 2, 0);
    if (q < 2) s.puts(OP_A, 10 + 5 * q, 5);
    if (q) s.puts(OP_A5, a5to[q - 1], a5to[q] - a5to[q - 1]);
    if (q) s.putw(wto[q - 1], wto[q]);
    if (q == 12) s.put(OP_PEEK, 1, 0);
    if (q == 6 || q == 9 || q == 12) s.put(OP_STORE, q == 6 ? 0 : q == 9 ? 1 : 2, 1);
  }
  s.seg(25);
  return s;
}
constexpr Sched SC = make_sched();
constexpr int sc_peek(int i) { for (int k = 0; k < SC.n; ++k) if (SC.ty[k] == OP_PEEK && SC.ix[k] == i) return SC.pos[k]; return 0; }
constexpr int sc_wait(int q) {
  int p = -1;
  if (q <= 11) p = SC.at(OP_W, (10 * (q + 1) + (q == 11 ? 0 : q % 4 == 2 ? 10 : 6) + 3) / 4 - 1);
  if (q >= 13) p = cmax(SC.at(OP_W, (10 * q + (q < 24 ? 10 : 0) + 3) / 4 - 1), SC.at(OP_A5, 2 * (q - 13) + 1));
  if (q < 4) p = cmax(p, SC.at(OP_A, 5 * q + 4));
  if (q == 8 || q == 11 || q == 14) p = cmax(p, SC.at(OP_STORE, q == 8 ? 0 : q == 11 ? 1 : 2));
  if (p < 0) return 63;
  return SC.before[q] - 1 - p;
}
constexpr Sched make_sched1() {
  Sched s{}; int a = 5, r = 7;
  s.putw(0, 4); s.puts(OP_A, 0, 3); s.putw(4, 7); s.puts(OP_A, 3, 2);
  for (int q = 0; q < 12; ++q) {
    s.seg(q);
    int na = q < 3 ? 3 : q < 6 ? 2 : 0, nw = q < 5 ? 2 : q < 8 ? 1 : 0;
    s.puts(OP_A, a, na); a += na; s.putw(r, r + nw); r += nw;
  }
  s.seg(12);
  return s;
}
constexpr Sched S1 = make_sched1();
constexpr int s1_first(int q) { return q <= 10 ? 6 * q : q == 11 ? 64 : 80; }
constexpr int s1_wait(int q) {
  int pc = s1_first(q + 1) + 6; if (pc > 80 || q == 11) pc = 80;
  int p = S1.at(OP_W, (pc + 3) / 4 - 1);
  if (q < 10) p = cmax(p, S1.at(OP_A, q < 9 ? 2 * q + 2 : 19));
  return S1.before[q] - 1 - p;
}

#define STAMP(i)

DI void poll(const GAS unsigned* addr, unsigned base, unsigned want, bool active) {
#pragma nounroll
  for (unsigned s = 0; s < SPIN_CAP; ++s) {
    unsigned v = base + want;
    if (active) v = __hip_atomic_load(addr, RLX_AGENT);
    if (all64((v - base) >= want)) break;
    __builtin_amdgcn_s_sleep(2);
  }
  __builtin_amdgcn_fence(__ATOMIC_ACQUIRE, "wavefront");
}
DI unsigned lds_arrive(LAS char* L, int off, int lane) {
  unsigned o = 0;
  asm volatile("s_waitcnt lgkmcnt(0)" ::: "memory");
  if (lane == 0) o = __hip_atomic_fetch_add((LAS unsigned*)(L + off), 1u, __ATOMIC_RELAXED, __HIP_MEMORY_SCOPE_WORKGROUP);
  o = __builtin_amdgcn_readfirstlane(o);
  asm volatile("" ::: "memory");
  return o;
}
template <int N> DI void mod_rows(const v4u* wm, const unsigned* cv, LAS char* red, int lane) {
  float acc[8] = {0, 0, 0, 0, 0, 0, 0, 0};
#pragma unroll
  for (int i = 0; i < N; ++i) {
    float cf = bflo(cv[i]);
    float sv = bfr(cf * __builtin_amdgcn_rcpf(1.f + __builtin_amdgcn_exp2f(-1.44269504f * cf)));
#pragma unroll
    for (int e = 0; e < 4; ++e) { acc[2 * e] += sv * bflo(wm[i][e]); acc[2 * e + 1] += sv * bfhi(wm[i][e]); }
  }
  float u[4], v[2];
#pragma unroll
  for (int e = 0; e < 4; ++e) { f32x2 r = xswp32(acc[e], acc[e + 4]); u[e] = r.x + r.y; }
#pragma unroll
  for (int e = 0; e < 2; ++e) { f32x2 r = xswp16(u[e], u[e + 2]); v[e] = ror8_add(r.x + r.y); }
  if (!(lane & 8)) *(LAS f32x2*)(red + (lane & 7) * 32 + (lane >> 4) * 8) = f32x2{v[0], v[1]};
}
DI void lds_wait(LAS char* L, int off, unsigned want) {
#pragma nounroll
  for (unsigned s = 0; s < SPIN_CAP && *(volatile LAS unsigned*)(L + off) < want; ++s) __builtin_amdgcn_s_sleep(1);
  asm volatile("" ::: "memory");
}

__global__ __launch_bounds__(512, 2) void fused_block(Params P) {
  __shared__ __attribute__((aligned(16))) char lds_raw[163840];
  LAS char* L = (LAS char*)lds_raw;
  const unsigned LB = (unsigned)(size_t)L;
  const int tid = threadIdx.x, lane = tid & 63;
  const int w = __builtin_amdgcn_readfirstlane(tid >> 6);
  const int b = blockIdx.x;
  const int p = 4 * ((b & 7) >> 1) + ((b >> 3) & 3), j = 8 * (b & 1) + (b >> 5);
  const int m = w & 3, kh = w >> 2, h = lane >> 5, r32 = lane & 31;
  const int g4 = lane >> 4, i16 = lane & 15, q4 = (lane & 15) >> 2, p4 = lane & 3, blk = (lane >> 4) & 1;
  const rsrc_t rws = __builtin_amdgcn_make_buffer_rsrc((void*)P.ws, 0, WS_END, 0x00020000);
  const rsrc_t rx = __builtin_amdgcn_make_buffer_rsrc((void*)P.x, 0, 2048 * 1280, 0x00020000);
  const unsigned l16 = lane * 16;
  const unsigned ringL = kh * 61440 + l16, ringD = LB + kh * 61440 + m * 15360;
  STAMP(0);
  unsigned long long pc0; asm volatile("s_getpc_b64 %0" : "=s"(pc0));
  unsigned cbase = 0;
  const GAS unsigned* caddr = (const GAS unsigned*)P.ws;
  const int c40 = b % 40, c20 = (b - 160) % 20;
  const int cc = b < 160 ? (c40 < 20 ? c40 : c40 + 10) : (c20 < 10 ? 20 + c20 : 40 + c20), kq = b < 160 ? b / 40 : ((b - 160) / 20) & 3;
  const char* wm0 = (const char*)P.wmod + (size_t)(160 * kq) * 7680 + cc * 128;
  {
    const int bj = b < 240 ? b : 0, s = bj / 6, q = bj % 6;
    const int lr = (lane * 205) >> 11, lc = lane - 10 * lr;
    const bool lo = w < 4;
    v4u t[8]; unsigned cv[5], dst[2];
    {
      const char* px = (const char*)P.x + (size_t)b * 10240;
      const rsrc_t r1 = __builtin_amdgcn_make_buffer_rsrc((void*)(lo ? wm0 : px), 0, lo && b >= 160 ? 0 : 0x200000, 0x00020000);
      const rsrc_t r2 = __builtin_amdgcn_make_buffer_rsrc((void*)((const char*)P.wqkv + (size_t)(16 * s) * 3840 + q * 640 + m * 160), 0, lo ? 0x10000 : 0, 0x00020000);
      const rsrc_t rc = __builtin_amdgcn_make_buffer_rsrc((void*)(P.c + 160 * kq), 0, lo && b < 160 ? 320 : 0, 0x00020000);
      const unsigned v1 = lo ? (tid >> 3) * 7680 + (tid & 7) * 16 : (lane & 7) * 1280 + (lane >> 3) * 16 + m * 128, st1 = lo ? 245760 : 512;
      const unsigned v2 = (lr < 6 ? lr : 5) * 3840 + lc * 16;
      ld16p(t[0], v1, r1, 0); ld16p(t[1], v1, r1, st1); ld16p(t[2], v1, r1, w < 6 ? 2 * st1 : 0x7ff00000u);
      if (lo) {
        ld16p(t[3], v1, r1, 3 * st1); ld16p(t[4], v1, r1, 4 * st1);
        ld16p(t[5], v2, r2, 0); ld16p(t[6], v2, r2, 23040); ld16p(t[7], lr >= 4 ? v2 - 7680 : v2, r2, 46080);
        sfor<5>([&](auto I) { ld2p(cv[I], (tid >> 3) * 2, rc, I * 64); });
      } else if (w >= 6) {
        const rsrc_t ro = __builtin_amdgcn_make_buffer_rsrc((void*)P.wo, 0, 640 * 1280, 0x00020000);
#pragma unroll
        for (int u = 0; u < 2; ++u) {
          const int sl = 2 * (w - 6) + u, f = sl < 3 ? 3 * b + sl : 768 + (b & 31), cw = f / 100, st = (f % 100) / 5, tl = f % 5;
          const int e = (lane >> 1) & 7;
          const int ch = st < 16 ? 40 * st + 20 * (g4 >> 1) + 4 * (g4 & 1) + (e < 4 ? e : e + 4) : 40 * (4 * (st - 16) + g4) + 16 + (e < 4 ? e : e + 16);
          dma<false>(LB + 16384 + sl * 1024, ch * 1280 + (80 * cw + 16 * tl) * 2 + (lane & 1) * 16, ro, 0);
          dst[u] = WS_WT3 + cw * 106496 + (st & 1) * 53248 + strm_off((st >> 1) * 5 + tl, 13) + l16;
        }
      } else {
        ld16(t[3], l16, rws, b < 8 && w == 5 ? CT_FLAG + b * 1024 : 0x7ff00000);
        ld16p(t[4], l16, __builtin_amdgcn_make_buffer_rsrc((void*)(pc0 & ~255ull), 0, 34816, 0x00020000), (w == 5 ? 0 : 32768) + (b >> 3) * 1024);
      }
    }
    if (w == 4) {
      int off = CT_E;
      if (lane == 1) off = CT_C1 + (((p >= 1 ? p - 1 : 0) * 16 + j) << 7);
      if (lane == 2) off = CT_C1 + (((p >= 2 ? p - 2 : 0) * 16 + j) << 7);
      if (lane == 3) off = CT_C2 + (p << 7);
      if (lane == 4) off = CT_C3 + (p << 7);
      if (lane == 5) off = CT_C4 + p * 384;
      if (lane == 16 || lane == 17) off = CT_C4 + p * 384 + (lane - 15) * 128;
      if (lane == 7) off = CT_CM;
      if (lane >= 8 && lane < 16) off = CT_CB + ((lane - 8) << 7);
      caddr = (const GAS unsigned*)(P.ws + off);
    }
    peek(cbase, caddr);
    if (tid < 2) *(LAS v4u*)(L + L_CNT + tid * 16) = v4u{0u, 0u, 0u, 0u};
    bar();
    waitv<0>();
    sfor<8>([&](auto I) { tie(t[I]); });
    sfor<5>([&](auto I) { tie(cv[I]); });
    tie(cbase);
    if (lo) {
      const unsigned wql = L_WQ + w * 2560;
      if (lr < 6) { *(LAS v4u*)(L + wql + l16) = t[5]; *(LAS v4u*)(L + wql + 960 + l16) = t[6]; }
      if (lr < 4) *(LAS v4u*)(L + wql + 1920 + l16) = t[7];
      if (b < 160) {
        mod_rows<5>(t, cv, L + L_RED + w * 256, lane);
        if (lds_arrive(L, L_CNT, lane) == 3 && lane < 16) {
          f32x4 u = (ldsf4(L, L_RED + l16) + ldsf4(L, L_RED + 256 + l16)) + (ldsf4(L, L_RED + 512 + l16) + ldsf4(L, L_RED + 768 + l16));
          st16(__builtin_bit_cast(v4u, u), rws, WS_MODP + (kq * 3840 + cc * 64 + lane * 4) * 4);
        }
      }
      if (b < 240) {
        asm volatile("s_waitcnt lgkmcnt(0)" ::: "memory");
        const int part = q >> 1, jb = 8 * (q & 1) + 2 * w;
        const int sg = s >= 20 ? s - 20 : s, pk0 = 3 * (sg & ~1);
        const unsigned dst0 = WS_WT1 + jb * 163840 + (s >= 20 ? 81920 : 0);
        const unsigned dmain = dst0 + strm_off(part == 0 ? 60 + sg : pk0 + 2 * (sg & 1) + part - 1, 20), dmix = dst0 + strm_off(pk0 + 4 + (sg & 1), 20);
        const unsigned rowa = wql + (8 * h + q4) * 160;
        const unsigned a = rowa + (20 * blk + 4 * p4) * 2, a2 = rowa + (40 * blk + 16 + 20 * (p4 & 1)) * 2;
        v4u f0 = trpair(L, a, a + 640), f1 = trpair(L, a + 80, a + 720), f2 = trpair(L, a2, a2 + 640);
        st16(f0, rws, dmain + l16);
        st16(f1, rws, dmain + 163840 + l16);
        if (i16 >= 8) f2 = v4u{0, 0, 0, 0};
        const int r = (part == 0 ? 16 : part == 1 ? 0 : 8) + i16;
        if (part == 0 || i16 < 8) st16(f2, rws, dmix + blk * 163840 + (h * 32 + r) * 16);
      }
    } else {
      float s1 = 0.f, s2 = 0.f, s3 = 0.f, s4 = 0.f;
#pragma unroll
      for (int i = 0; i < 3; ++i) {
        s1 = dot2(t[i][0], 0x3f803f80u, s1); s2 = dot2(t[i][0], t[i][0], s2); s3 = dot2(t[i][1], 0x3f803f80u, s3); s4 = dot2(t[i][1], t[i][1], s4);
        s1 = dot2(t[i][2], 0x3f803f80u, s1); s2 = dot2(t[i][2], t[i][2], s2); s3 = dot2(t[i][3], 0x3f803f80u, s3); s4 = dot2(t[i][3], t[i][3], s4);
      }
      s1 = swap32_add(swap16_add(ror8_add(s1 + s3))); s2 = swap32_add(swap16_add(ror8_add(s2 + s4)));
      if (lane < 8) *(LAS f32x2*)(L + L_RED + 1024 + lane * 32 + m * 8) = f32x2{s1, s2};
      lds_arrive(L, L_CNT + 8, lane);
      if (w >= 6) {
#pragma unroll
        for (int u = 0; u < 2; ++u) {
          const unsigned a = 16384 + (2 * (w - 6) + u) * 1024 + (8 * g4 + q4) * 32 + p4 * 8;
          st16(trpair(L, a, a + 128), rws, dst[u]);
        }
      }
      lds_wait(L, L_CNT + 8, 4u);
      const f32x4 o0 = ldsf4(L, L_RED + 1024 + (lane & 7) * 32), o1 = ldsf4(L, L_RED + 1040 + (lane & 7) * 32);
      const float mean = ((o0[0] + o0[2]) + (o1[0] + o1[2])) * (1.f / 640.f);
      const float rstd = __builtin_amdgcn_rsqf(fmaxf(((o0[1] + o0[3]) + (o1[1] + o1[3])) * (1.f / 640.f) - mean * mean, 0.f) + 1e-5f), nm = -mean * rstd;
      const int tok = 8 * b + (lane & 7);
      const unsigned xo = WS_XH + (tok >> 5) * 40960 + m * 4096 + (lane >> 3) * 512 + (tok & 31) * 16;
#pragma unroll
      for (int i = 0; i < 3; ++i) {
        if (i == 2 && w >= 6) break;
        v4u o;
#pragma unroll
        for (int e = 0; e < 4; ++e) { f32x2 v = {fmaf(bflo(t[i][e]), rstd, nm), fmaf(bfhi(t[i][e]), rstd, nm)}; o[e] = __builtin_bit_cast(unsigned, __builtin_convertvector(v, f16x2)); }
        st16(o, rws, xo + i * 16384);
      }
    }
  }
  waitv<0>();
  if (lds_arrive(L, L_CNT + 4, lane) == 7 && lane < 8) __hip_atomic_store((GAS unsigned*)(P.ws + CT_FLAG + lane * 1024 + b * 4), __builtin_amdgcn_readfirstlane(cbase) + 1u, RLX_AGENT);
  STAMP(1);
  const int c8 = __builtin_amdgcn_readfirstlane(4 * (b & 1) + ((b >> 5) & 3)), rh = b >> 7;
  const int kp = (b >> 4) & 7;
  int col = tid < 320 ? 16 * tid : tid < 340 ? (2560 + 80 * kp + 4 * (tid - 320)) * 4 : tid < 360 ? (1920 + 80 * kp + 4 * (tid - 340)) * 4 : 0;
  unsigned mdst = L_MODV + (tid < 160 ? tid * 32 + 16 : tid < 320 ? (tid - 160) * 32 : 5760 + (tid - 320) * 16);
  const rsrc_t rb = __builtin_amdgcn_make_buffer_rsrc((void*)((const char*)((b >> 7) ? P.wu : P.wg) + (size_t)(80 * kp) * 3072 + (b & 15) * 192), 0, 80 * 3072, 0x00020000);
  tie(col); tie(mdst);
  const bool mod2 = b >= 160 && b < 240;
  v4u m2w[3]; unsigned m2c[3];
  __builtin_amdgcn_s_sleep(DELAY0);
  const int v6 = w < 4 ? w : w - 1;
  if (w == 7) {
    const rsrc_t rd = __builtin_amdgcn_make_buffer_rsrc((void*)((const char*)P.wd + (b / 5) * 40960 + (b % 5) * 256), 0, b < 240 ? 40960 : 0, 0x00020000);
    const int e = (lane >> 1) & 7;
    const unsigned vo = (16 * (g4 >> 1) + 4 * (g4 & 1) + (e < 4 ? e : e + 4)) * 1280 + (lane & 1) * 16;
    sfor<8>([&](auto U) { dma<false>(LB + L_GSRC + (18 + U) * 1024, vo, rd, U * 32); });
  } else if (w != 4) {
    const int e = (lane >> 2) & 7;
#pragma unroll
    for (int u = 0; u < 3; ++u) {
      const int sg = (v6 & 1) * 3 + u;
      const int k = sg < 4 ? 32 * (sg >> 1) + 4 * (2 * (sg & 1) + h) + (e < 4 ? e : e + 12) : 64 + 8 * h + e;
      if (sg < 5) dma<false>(LB + L_GSRC + (3 * v6 + u) * 1024, k * 3072 + (v6 >> 1) * 64 + p4 * 16, rb, 0);
    }
  }
  {
    const rsrc_t rm = __builtin_amdgcn_make_buffer_rsrc((void*)wm0, 0, mod2 ? 160 * 7680 : 0, 0x00020000);
    const rsrc_t rc = __builtin_amdgcn_make_buffer_rsrc((void*)(P.c + 160 * kq), 0, mod2 ? 320 : 0, 0x00020000);
    sfor<3>([&](auto I) { ld16p(m2w[I], (tid >> 3) * 7680 + (tid & 7) * 16, rm, I * 491520); ld2p(m2c[I], (tid >> 3) * 2, rc, I * 128); });
  }
  if (w == 4) {
    const unsigned tag = __builtin_amdgcn_readfirstlane(cbase) + 1u;
    GAS unsigned* go = (GAS unsigned*)(P.ws + CT_GO + (b & 7) * 128);
    if (b < 8) {
#pragma nounroll
      for (unsigned s = 0; s < SPIN_CAP; ++s) {
        v4u f; ld16(f, l16, rws, CT_FLAG + b * 1024); waitv<0>(); tie(f);
        if (all64(f.x == tag && f.y == tag && f.z == tag && f.w == tag)) break;
      }
      if (lane == 0) __hip_atomic_store(go, tag, RLX_AGENT);
    } else {
#pragma nounroll
      for (unsigned s = 0; s < SPIN_CAP && __hip_atomic_load(go, RLX_AGENT) != tag; ++s) __builtin_amdgcn_s_sleep(1);
    }
    __builtin_amdgcn_fence(__ATOMIC_ACQUIRE, "wavefront");
  }
  bar();
  STAMP(4);

  auto mod_put = [&](v4u* mp, bool isc, bool on, unsigned dst) {
    sfor<4>([&](auto I) { tie(mp[I]); });
    f32x4 v = (__builtin_bit_cast(f32x4, mp[0]) + __builtin_bit_cast(f32x4, mp[1])) + (__builtin_bit_cast(f32x4, mp[2]) + __builtin_bit_cast(f32x4, mp[3]));
#pragma unroll
    for (int e = 0; e < 4; ++e) { float t = bfr(v[e]); v[e] = isc ? bfr(1.f + t) : t; }
    if (on) *(LAS f32x4*)(L + dst) = v;
  };
  {
    v4u mp[4], xf[20];
    sfor<4>([&](auto I) { ld16(mp[I], col, rws, WS_MODP + I * 15360); });
    const unsigned w1 = WS_WT1 + j * 163840 + kh * 81920 + m * 20480;
    const unsigned ab = WS_XH + ((4 * p + m) * 40 + 20 * kh) * 1024;
    rbar();
    dman<4, 0, 0>(ringD, l16, rws, w1);
    sfor<3>([&](auto I) { ldf<I>(xf[I], l16, rws, ab); });
    waitv<7>();
    mod_put(mp, tid >= 160 && tid < 340, tid < 360, mdst);
    dman<3, 4096, 4096>(ringD, l16, rws, w1);
    ldf<3>(xf[3], l16, rws, ab); ldf<4>(xf[4], l16, rws, ab);
    STAMP(5);
    f32x16 acc[4] = {};
    const unsigned wbK = ringL + kh * 15360, wbS = ringL + 15360 - kh * 15360;
    unsigned cb = L_MODV + (80 * kh + 2 * h) * 32; tie(cb);
    unsigned xw = kh * 61440 + m * 15360 + 5120 + l16, xr = (kh ^ 1) * 61440 + m * 15360 + 5120 + l16;
    v4u wf[80], hf[20], c0, c1, c2; f32x4 ca[40], cs[40], eo[6];
    const unsigned kvg = WS_KV + ((4 * p + m) * 16 + j) * 6144, kvl = (8 + m) * KVT;
    auto rdw = [&](auto N) {
      constexpr int n = N < 60 ? 3 * (N / 3) + 2 - N % 3 : (N ^ 1), i = n / 3, pk0 = 3 * (i & ~1);
      if constexpr (n >= 60) wf[n] = lds16(L, ringL + ring_off(n));
      else if constexpr (n % 3 == 1) wf[n] = lds16(L, ringL + ring_off(pk0 + 4 + (i & 1)));
      else wf[n] = lds16(L, (n % 3 == 0 ? wbS : wbK) + ring_off(pk0 + 2 * (i & 1)));
    };
    auto rdc = [&](auto R) { ca[R] = ldsf4(L, cb + (R / 2) * 128 + (R & 1) * 32); cs[R] = ldsf4(L, cb + (R / 2) * 128 + (R & 1) * 32 + 16); };
    auto aff = [&](auto I, auto E) { constexpr int e = 2 * E; unsigned o0, o1; affine4(xf[I][e], xf[I][e + 1], ca[2 * I + E], cs[2 * I + E], o0, o1); hf[I][e] = o0; hf[I][e + 1] = o1; };
    auto quad = [&](const f32x16& a, int q) { return f32x4{a[4 * q], a[4 * q + 1], a[4 * q + 2], a[4 * q + 3]}; };
    sfor<12>([&](auto Q) {
      constexpr int q = Q, n0 = s1_first(q);
      waitv<s1_wait(q)>();
      if constexpr (q == 0) tie(xf[0]);
      if constexpr (q < 10) { tie(xf[2 * q + 1]); if constexpr (q < 9) tie(xf[2 * q + 2]); }
      if constexpr (q == 0 || q == 11) bar(); else rbar();
      if constexpr (q == 0) { sfor<6>(rdc); sfor<6>(rdw); aff(IC<0>{}, IC<0>{}); aff(IC<0>{}, IC<1>{}); asm volatile("s_nop 1"); SB(); }
      sfor<s1_first(q + 1) - n0>([&](auto K) {
        constexpr int n = n0 + K, i = n < 60 ? n / 3 : n - 60, t = n < 60 ? n % 3 : 3, k = S1.slot(q, K, 6);
        acc[t] = mfma32(wf[n], hf[i], acc[t]); tie(acc[t]);
        SB();
        if constexpr (t < 2 && i + 1 < 20) aff(IC<i + 1>{}, IC<t>{});
        if constexpr (t < 2 && i + 3 < 20) rdc(IC<2 * (i + 3) + t>{});
        if constexpr (n + 6 < 80) rdw(IC<n + 6>{});
        if constexpr (k >= 0) {
          constexpr int ty = S1.ty[k], ix = S1.ix[k];
          if constexpr (ty == OP_A) ldf<ix>(xf[ix], l16, rws, ab);
          else if constexpr (ty == OP_W) dman<S1.cn[k], (ix % 15) * 1024, ix * 1024>(ringD, l16, rws, w1);
        }
        if constexpr (n >= 59 && n < 63) *(LAS f32x4*)(L + xw + (n - 59) * 1024) = quad(acc[0], n - 59);
        if constexpr (n == 63) { if (kh) { *(LAS f32x4*)(L + xw + 4096) = quad(acc[1], 0); *(LAS f32x4*)(L + xw + 5120) = quad(acc[1], 1); } }
        if constexpr (n == 64) sfor<6>([&](auto I) { eo[I] = ldsf4(L, xr + I * 1024); });
        if constexpr (n == 65 || n == 66) {
          constexpr int o = 8 * (n - 65);
          v4u c = {pk2(acc[2][o] + eo[o / 4][0], acc[2][o + 1] + eo[o / 4][1]), pk2(acc[2][o + 2] + eo[o / 4][2], acc[2][o + 3] + eo[o / 4][3]),
                   pk2(acc[2][o + 4] + eo[o / 4 + 1][0], acc[2][o + 5] + eo[o / 4 + 1][1]), pk2(acc[2][o + 6] + eo[o / 4 + 1][2], acc[2][o + 7] + eo[o / 4 + 1][3])};
          st16(c, rws, kvg + (kh ? 3072 + r32 * 32 + h * 16 : l16) + (n - 65) * 1024);
          if constexpr (n == 65) c0 = c; else c1 = c;
        }
        if constexpr (n == 67) {
          c2 = v4u{pk2(acc[1][0] + eo[4][0], acc[1][1] + eo[4][1]), pk2(acc[1][2] + eo[4][2], acc[1][3] + eo[4][3]),
                   pk2(acc[1][4] + eo[5][0], acc[1][5] + eo[5][1]), pk2(acc[1][6] + eo[5][2], acc[1][7] + eo[5][3])};
          if (!kh) { st16(v4u{c2.x, c2.y, 0u, 0u}, rws, kvg + 2048 + l16); st16(v4u{c2.z, c2.w, 0x3f80u, 0u}, rws, kvg + 5120 + l16); }
        }
        SB();
      });
    });
    STAMP(6);
    const unsigned xq = 5120 + (m >> 1) * 15360 + (m & 1) * 5120 + l16;
    if (!kh) {
#pragma unroll
      for (int qd = 0; qd < 4; ++qd) *(LAS f32x4*)(L + xq + qd * 1024) = quad(acc[3], qd);
      *(LAS f32x4*)(L + xq + 4096) = quad(acc[1], 2);
    }
    if (mod2) {
      sfor<3>([&](auto I) { tie(m2w[I]); tie(m2c[I]); });
      mod_rows<3>(m2w, m2c, L + L_RED2 + w * 256, lane);
    }
    waitv<0>();
    bar();
    if (tid == 0) signal(rws, CT_C1 + ((p * 16 + j) << 7));
    {
      const unsigned lo = kh ? 3072 + r32 * 48 + h * 16 : l16, ls = kh ? 1536 : 1024;
      *(LAS v4u*)(L + kvl + lo) = c0; *(LAS v4u*)(L + kvl + lo + ls) = c1;
    }
    if (kh == 0) {
      *(LAS v4u*)(L + kvl + 2048 + l16) = v4u{c2.x, c2.y, 0u, 0u};
      *(LAS v4u*)(L + kvl + 3072 + h * 1536 + r32 * 48 + 32) = v4u{c2.z, c2.w, 0x3f80u, 0u};
    } else {
      const float qs = 0.22360679775f * 1.44269504089f;
      float qv[20];
#pragma unroll
      for (int qd = 0; qd < 5; ++qd) {
        const f32x4 u = ldsf4(L, xq + qd * 1024);
#pragma unroll
        for (int e = 0; e < 4; ++e) qv[4 * qd + e] = ((qd < 4 ? acc[3][4 * qd + e] : acc[1][8 + e]) + u[e]) * qs;
      }
      *(LAS v4u*)(L + L_Q + m * 2048 + l16) = v4u{pk2(qv[0], qv[1]), pk2(qv[2], qv[3]), pk2(qv[4], qv[5]), pk2(qv[6], qv[7])};
      *(LAS v4u*)(L + L_Q + m * 2048 + 1024 + l16) = v4u{pk2(qv[8], qv[9]), pk2(qv[10], qv[11]), pk2(qv[12], qv[13]), pk2(qv[14], qv[15])};
      *(LAS v2u*)(L + L_QHI + m * 512 + lane * 8) = v2u{pk2(qv[16], qv[17]), pk2(qv[18], qv[19])};
    }
  if (mod2 && w == 3 && lane < 16) {
    f32x4 u = ldsf4(L, L_RED2 + l16);
#pragma unroll
    for (int i = 1; i < 8; ++i) u += ldsf4(L, L_RED2 + i * 256 + l16);
    st16(__builtin_bit_cast(v4u, u), rws, WS_MODP + (kq * 3840 + cc * 64 + lane * 4) * 4);
  }
    STAMP(7);
  }
  unsigned vg[3];
#pragma unroll
  for (int i = 0; i < 3; ++i) { const int c = 64 * i + lane, rw = (c * 171) >> 9, pt = c - 3 * rw; vg[i] = pt < 2 ? 3072 + rw * 32 + pt * 16 : 5120 + rw * 16; tie(vg[i]); }
  if (w == 4) poll(caddr, cbase, 1u, (lane == 1 && p >= 1) || (lane == 2 && p >= 2));
  else if (w == 7) {
    if (b < 240) {
      const int st0 = b / 5, st = 16 * (st0 % 3) + st0 / 3, ct0 = 8 * (b % 5);
      const unsigned a = L_GSRC + 18432 + (8 * g4 + q4) * 32 + p4 * 8, d0 = WS_WT5 + (st & 1) * 122880 + l16;
#pragma unroll
      for (int u = 0; u < 8; ++u) { const int ct = ct0 + u; st16(trpair(L, a + u * 1024, a + u * 1024 + 128), rws, d0 + (ct / 5) * 245760 + strm_off((st >> 1) * 5 + ct % 5, 30)); }
    }
  } else {
    const int mat = b >> 7, jd = b & 15, t = 2 * (v6 >> 1) + blk;
    const unsigned d0 = WS_WT4 + jd * 245760 + (kp >> 2) * 122880 + blk * 30720 + (h * 32 + mat * 16 + i16) * 16;
    float us = 0.f, vs = 0.f;
#pragma unroll
    for (int u = 0; u < 3; ++u) {
      const int sg = (v6 & 1) * 3 + u;
      if (sg == 5) break;
      const int k0 = sg < 4 ? 32 * (sg >> 1) + 4 * (2 * (sg & 1) + h) : 64 + 8 * h, k1 = sg < 4 ? k0 + 16 : k0 + 4;
      const unsigned ca = L_GSRC + (3 * v6 + u) * 1024 + (8 * h + q4) * 64 + blk * 32 + p4 * 8;
      v4u f = trpair(L, ca, ca + 256);
      f32x4 a0 = ldsf4(L, L_MODV + 5760 + k0 * 4), a1 = ldsf4(L, L_MODV + 5760 + k1 * 4);
      f32x4 b0 = ldsf4(L, L_MODV + 6080 + k0 * 4), b1 = ldsf4(L, L_MODV + 6080 + k1 * 4);
      v4u o;
      o[0] = pk2(bflo(f[0]) * a0[0], bfhi(f[0]) * a0[1]); o[1] = pk2(bflo(f[1]) * a0[2], bfhi(f[1]) * a0[3]);
      o[2] = pk2(bflo(f[2]) * a1[0], bfhi(f[2]) * a1[1]); o[3] = pk2(bflo(f[3]) * a1[2], bfhi(f[3]) * a1[3]);
      vs += bflo(f[0]) * b0[0] + bfhi(f[0]) * b0[1] + bflo(f[1]) * b0[2] + bfhi(f[1]) * b0[3] + bflo(f[2]) * b1[0] + bfhi(f[2]) * b1[1] + bflo(f[3]) * b1[2] + bfhi(f[3]) * b1[3];
      us = dot2(o[0], 0x3f803f80u, us); us = dot2(o[1], 0x3f803f80u, us); us = dot2(o[2], 0x3f803f80u, us); us = dot2(o[3], 0x3f803f80u, us);
      st16(o, rws, d0 + strm_off((v6 >> 1) * 40 + (5 * (kp & 3) + sg) * 2, 30));
    }
    us = swap32_add(us); vs = swap32_add(vs);
    const int row = (jd * 6 + t) * 32 + mat * 16 + i16;
    __builtin_amdgcn_raw_buffer_store_b32(__builtin_bit_cast(unsigned, h ? vs : us), rws, WS_UV + (((kp * 2 + (v6 & 1)) * 2 + h) * 3072 + row) * 4, 0, 16);
  }
  bar();
  STAMP(8);
  const int gt = 4 * p - 8 + w;
  {
    const unsigned ko = gt >= 0 ? WS_KV + (gt * 16 + j) * 6144 : 0x7ff00000u, lo = LB + w * KVT;
    dman<3, 0, 0>(lo, l16, rws, ko);
    sfor<3>([&](auto I) { dman<1, 3072 + I * 1024, 0>(lo, vg[I], rws, ko); });
  }
  STAMP(9);
  const unsigned w3 = WS_WT3 + c8 * 106496 + kh * 53248 + m * 13312;
  unsigned cbv;
  {
    waitv<0>();
    if (gt < 0) *(LAS unsigned*)(L + w * KVT + 2056 + l16) = 0xfe96u;
    bar();
    if (tid == 0 && mod2) signal(rws, CT_CM);
    if (tid == 64) signal(rws, CT_CB + ((b & 7) << 7));
    STAMP(10);
    const int hd = kh;
    const v4u q0 = lds16(L, L_Q + m * 2048 + hd * 1024 + l16);
    v2u qh = *(LAS v2u*)(L + L_QHI + m * 512 + lane * 8);
    if (h != hd) qh = v2u{0, 0};
    const v4u q1 = {qh.x, qh.y, 0x3f80u, 0};
    const f32x16 zero = {};
    const unsigned ka = m * KVT + hd * 1024 + l16, kb = m * KVT + 2048 + l16;
    v4u k0[9], k1[9];
    sfor<9>([&](auto I) { constexpr int i = I == 0 ? 0 : I == 1 ? 8 : I - 1; k0[i] = lds16(L, ka + i * KVT); k1[i] = lds16(L, kb + i * KVT); });
    auto qk = [&](auto I, f32x16 c) { f32x16 r = mfma32(k1[I], q1, mfma32(k0[I], q0, c)); tie(r); return r; };
    auto vmax = [](float a, float c) { return __builtin_elementwise_maximum(a, c); };
    auto tmax = [&](const f32x16& v, float t) {
#pragma unroll
      for (int r = 0; r < 16; r += 2) t = vmax(vmax(v[r], v[r + 1]), t);
      tie(t); return t; };
    int dq = r32 - 4 * h; tie(dq);
    f32x16 Sm, S[8];
    float mx = -3.0e38f;
    {
      const f32x16 s0 = qk(IC<0>{}, zero), s8 = qk(IC<8>{}, zero);
      S[1] = qk(IC<1>{}, zero); SB();
#pragma unroll
      for (int r = 0; r < 16; ++r) Sm[r] = (r & 3) + 8 * (r >> 2) <= dq ? s8[r] : s0[r];
      tie(Sm); SB();
    }
    sfor<7>([&](auto I) {
      if constexpr (I < 6) S[I + 2] = qk(IC<I + 2>{}, zero);
      SB();
      if constexpr (I == 0) mx = tmax(Sm, mx); else mx = tmax(S[I], mx);
      SB();
    });
    const unsigned va = m * KVT + 3072 + hd * 1536 + (4 * h + q4) * 48 + (blk == 0 ? (p4 & 1) * 16 + (p4 >> 1) * 8 : (p4 ? 40 : 32));
    v4u vf[18];
    auto rdv = [&](auto I) { vf[2 * I + 1] = trpair(L, va + I * KVT + 768, va + I * KVT + 1152); vf[2 * I] = trpair(L, va + I * KVT, va + I * KVT + 384); };
    rdv(IC<8>{}); rdv(IC<0>{}); SB();
    mx = tmax(S[7], mx);
    { f32x2 t = swp32(mx); mx = vmax(t.x, t.y); }
    f32x16 negm;
#pragma unroll
    for (int r = 0; r < 16; ++r) negm[r] = -mx;
    tie(negm);
    f32x16 O = {};
    S[1] = qk(IC<1>{}, negm); rdv(IC<1>{}); SB();
#pragma unroll
    for (int s = 0; s < 2; ++s) {
      v4u p0, p8;
#pragma unroll
      for (int e = 0; e < 4; ++e) {
        const int r = 8 * s + 2 * e, key = (r & 3) + 8 * (r >> 2);
        const float a = __builtin_amdgcn_exp2f(Sm[r] - mx), c = __builtin_amdgcn_exp2f(Sm[r + 1] - mx);
        const float a8 = key <= dq ? a : 0.f, c8_ = key + 1 <= dq ? c : 0.f;
        p8[e] = pk2(a8, c8_); p0[e] = pk2(a - a8, c - c8_);
      }
      O = mfma32(vf[s], p0, O); O = mfma32(vf[16 + s], p8, O); tie(O); SB();
    }
    sfor<7>([&](auto I) {
      constexpr int i = I + 1;
      if constexpr (i < 7) { S[i + 1] = qk(IC<i + 1>{}, negm); rdv(IC<i + 1>{}); SB(); }
      v4u pf[2];
#pragma unroll
      for (int e = 0; e < 8; ++e) pf[e / 4][e % 4] = pk2(__builtin_amdgcn_exp2f(S[i][2 * e]), __builtin_amdgcn_exp2f(S[i][2 * e + 1]));
      O = mfma32(vf[2 * i], pf[0], O); O = mfma32(vf[2 * i + 1], pf[1], O); tie(O);
      if constexpr (I < 4) dman<2, L_W3A + I * 2048, I * 2048>(LB + kh * 32768 + m * 8192, l16, rws, w3);
      SB();
    });
    const float inv = __builtin_amdgcn_rcpf(swap32_hi(O[8]));
    const int tok = 128 * p + 32 * m + r32;
    const unsigned ob = WS_OF + (tok >> 4) * 20480;
    STAMP(11);
    st16(v4u{pk2(O[0] * inv, O[1] * inv), pk2(O[2] * inv, O[3] * inv), pk2(O[4] * inv, O[5] * inv), pk2(O[6] * inv, O[7] * inv)}, rws, ob + ((j & 1) * 10 + (j >> 1)) * 1024 + (2 * hd + h) * 256 + (tok & 15) * 16);
    if (h == 0) st8(v2u{pk2(O[8] * inv, O[9] * inv), pk2(O[10] * inv, O[11] * inv)}, rws, ob + (((j >> 2) & 1) * 10 + 8 + (j >> 3)) * 1024 + hd * 512 + (j & 3) * 128 + (tok & 15) * 8);
  }
  waitv<0>();
  bar();
  const int tt = 8 * p + 4 * rh + m;
  unsigned x2k[6];
  {
    v2u xr[3];
    const unsigned xo = (16 * tt + i16) * 1280 + (80 * c8 + 4 * g4) * 2;
    if (tid == 0) signal(rws, CT_C2 + (p << 7));
    STAMP(12);
    v4u mp[4];
    ld8p(xr[0], xo, rx, kh * 64); ld8p(xr[1], xo, rx, kh * 64 + 32); ld8p(xr[2], xo, rx, 128);
    dman<4, 0, 8192>(LB + kh * 20480 + m * 5120, l16, rws, w3); dman<1, 4096, 12288>(LB + kh * 20480 + m * 5120, l16, rws, w3);
    if (w == 4) poll(caddr, cbase, lane == 7 ? 80u : 16u, lane == 3 || lane == 7);
    bar();
    STAMP(13);
    v4u af[10], wf[50];
    const unsigned ab = WS_OF + (tt * 20 + kh * 10) * 1024;
    v2u ah[4];
    sfor<4>([&](auto I) { ld16(mp[I], w == 5 ? ((lane < 20 ? 1280 : 3200 - 80) + 80 * c8 + 4 * (lane < 40 ? lane : 20)) * 4 : 0x7ff00000, rws, WS_MODP + I * 15360); });
    sfor<8>([&](auto I) { ldf<I>(af[I], l16, rws, ab); });
    sfor<4>([&](auto I) { ldh<I * 512>(ah[I], lane * 8, rws, ab + 8192); });
    peek(cbv, lane >= 8 && lane < 16 ? caddr : (const GAS unsigned*)P.ws);
    f32x4 acc[5] = {};
    unsigned wa = L_W3A + kh * 32768 + l16; tie(wa);
    const unsigned wb = kh * 20480 + l16;
    auto rdw = [&](auto N) {
      constexpr int n = 5 * (N / 5) + 4 - N % 5;
      if constexpr (n < 32) wf[n] = lds16(L, wa + (n & 3) * 8192 + (n >> 2) * 1024); else wf[n] = lds16(L, wb + (n & 3) * 5120 + ((n >> 2) - 8) * 1024);
    };
    sfor<10>([&](auto E) {
      constexpr int e = E;
      if constexpr (!(e & 1) && e < 8) { waitv<11 - e>(); tie(af[e]); tie(af[e + 1]); }
      if constexpr (e == 8) { waitv<1>(); sfor<2>([&](auto I) { tie(ah[2 * I]); tie(ah[2 * I + 1]); af[8 + I] = v4u{ah[2 * I].x, ah[2 * I].y, ah[2 * I + 1].x, ah[2 * I + 1].y}; }); }
      if constexpr (e == 0) { rbar(); sfor<8>(rdw); }
      if constexpr (e == 1) { if (w == 5) mod_put(mp, false, lane < 40, L_MODV + 5120 + l16); }
      sfor<5>([&](auto K) {
        constexpr int n = 5 * e + K;
        acc[K] = mfma16(wf[n], af[e], acc[K]); tie(acc[K]);
        if constexpr (n + 8 < 50) rdw(IC<n + 8>{});
        SB();
      });
    });
    STAMP(14);
    const unsigned xs = 57344 + m * 5120;
    if (kh == 0) { *(LAS f32x4*)(L + xs + l16) = acc[2]; *(LAS f32x4*)(L + xs + 1024 + l16) = acc[3]; }
    else { *(LAS f32x4*)(L + xs + 2048 + l16) = acc[0]; *(LAS f32x4*)(L + xs + 3072 + l16) = acc[1]; *(LAS f32x4*)(L + xs + 4096 + l16) = acc[4]; }
    bar();
    tie(xr[0]); tie(xr[1]); tie(xr[2]);
    f32x4 r0, r1, r2 = {};
    if (kh == 0) { r0 = acc[0] + ldsf4(L, xs + 2048 + l16); r1 = acc[1] + ldsf4(L, xs + 3072 + l16); r2 = acc[4] + ldsf4(L, xs + 4096 + l16); }
    else { r0 = acc[2] + ldsf4(L, xs + l16); r1 = acc[3] + ldsf4(L, xs + 1024 + l16); }
    const f32x4 ga0 = ldsf4(L, L_MODV + 5120 + (32 * kh + 4 * g4) * 4), ga1 = ldsf4(L, L_MODV + 5120 + (32 * kh + 16 + 4 * g4) * 4), ga2 = ldsf4(L, L_MODV + 5120 + (64 + 4 * g4) * 4);
    x2k[0] = pk2(bflo(xr[0].x) + ga0[0] * r0[0], bfhi(xr[0].x) + ga0[1] * r0[1]); x2k[1] = pk2(bflo(xr[0].y) + ga0[2] * r0[2], bfhi(xr[0].y) + ga0[3] * r0[3]);
    x2k[2] = pk2(bflo(xr[1].x) + ga1[0] * r1[0], bfhi(xr[1].x) + ga1[1] * r1[1]); x2k[3] = pk2(bflo(xr[1].y) + ga1[2] * r1[2], bfhi(xr[1].y) + ga1[3] * r1[3]);
    x2k[4] = pk2(bflo(xr[2].x) + ga2[0] * r2[0], bfhi(xr[2].x) + ga2[1] * r2[1]); x2k[5] = pk2(bflo(xr[2].y) + ga2[2] * r2[2], bfhi(xr[2].y) + ga2[3] * r2[3]);
    if (kh) { x2k[4] = 0; x2k[5] = 0; }
    const unsigned xb = WS_X2F + ((tt >> 1) * 40 + 5 * c8) * 1024 + ((tt & 1) * 16 + i16) * 16;
    st16(v4u{x2k[0], x2k[1], x2k[2], x2k[3]}, rws, xb + (2 * kh + (g4 >> 1)) * 1024 + (g4 & 1) * 512);
    if (kh == 0) st8(v2u{x2k[4], x2k[5]}, rws, xb + 4096 + (g4 >> 1) * 512 + (g4 & 1) * 8);
    float s1 = 0.f, s2 = 0.f;
#pragma unroll
    for (int e = 0; e < 6; ++e) { s1 = dot2(x2k[e], 0x3f803f80u, s1); s2 = dot2(x2k[e], x2k[e], s2); }
    s1 = swap32_add(swap16_add(s1)); s2 = swap32_add(swap16_add(s2));
    if (g4 == 0) st8(v2u{__builtin_bit_cast(unsigned, s1), __builtin_bit_cast(unsigned, s2)}, rws, WS_ST + ((tt >> 1) * 16 + 2 * c8 + kh) * 256 + ((tt & 1) * 16 + i16) * 8);
  }
  waitv<0>();
  tie(cbv);
  if (w == 4) *(LAS unsigned*)(L + L_CNT) = all64(lane < 8 || lane >= 16 || cbv - cbase >= 32u);
  bar();
  const bool early = __builtin_amdgcn_readfirstlane(*(LAS unsigned*)(L + L_CNT)) != 0;
  {
    const unsigned w4 = WS_WT4 + j * 245760 + kh * 122880 + m * 30720, w5 = WS_WT5 + c8 * 245760 + kh * 122880 + m * 30720 - 30720;
    if (tid == 0) signal(rws, CT_C3 + (p << 7));
    STAMP(15);
    if (!early) { if (w == 4) poll(caddr, cbase, 32u, lane >= 8 && lane < 16); bar(); }
    auto pre4 = [&]() { dman<4, 0, 0>(ringD, l16, rws, w4); dman<4, 4096, 4096>(ringD, l16, rws, w4); dman<R4 - 8, 8192, 8192>(ringD, l16, rws, w4); };
    if (w == 4) { pre4(); poll(caddr, cbase, 16u, lane == 4); }
    else {
      const int rq = (v6 < 6 ? v6 : 0) * 16 + i16;
      v4u uv[4];
      sfor<4>([&](auto I) { ld16(uv[I], (g4 * 8 + rq / 48) * 12288 + (j * 192 + 4 * (rq % 48)) * 4, rws, WS_UV + I * 24576); });
      pre4();
      waitv<R4>();
      sfor<4>([&](auto I) { tie(uv[I]); });
      f32x4 u = (__builtin_bit_cast(f32x4, uv[0]) + __builtin_bit_cast(f32x4, uv[1])) + (__builtin_bit_cast(f32x4, uv[2]) + __builtin_bit_cast(f32x4, uv[3]));
#pragma unroll
      for (int e = 0; e < 4; ++e) { float t = u[e]; u[e] = swap32_add(swap16_add(t)); }
      if (v6 < 6 && lane < 16) *(LAS f32x4*)(L + L_UVS + rq * 16) = u;
    }
    bar();
    STAMP(16);
    v4u af[20], a5[24], stv[4], wf[240]; unsigned pk;
    const unsigned ab = WS_X2F + ((4 * p + m) * 40 + 20 * kh) * 1024, ab5 = WS_ACT + (tt * 48 + 24 * kh) * 1024;
    sfor<10>([&](auto I) { ldf<I>(af[I], l16, rws, ab); });
    sfor<4>([&](auto I) { ldf<I>(stv[I], l16, rws, WS_ST + (4 * p + m) * 4096); });
    const int tok = 128 * p + 32 * m + r32;
    const unsigned ao = WS_ACT + ((tok >> 4) * 48 + (j & 1) * 24 + (j >> 1)) * 1024 + kh * 512 + h * 256 + (tok & 15) * 16;
    f32x16 aK[3] = {}, aS[3] = {};
    f32x4 acc[5] = {};
    const unsigned wbK = ringL + kh * 15360, wbS = ringL + 15360 - kh * 15360;
    unsigned xw = L_BSRC + w * 4096 + l16, xr_ = L_BSRC + (w ^ 4) * 4096 + l16, ub = L_UVS + kh * 128 + h * 16; tie(xw); tie(xr_); tie(ub);
    float rs2, nm2, av[8]; f32x4 eo[4], eu[4], ev[4];
    constexpr auto isS = [](int n) { return (n / 10) % 4 == 3 ? n % 10 < 5 : (n & 1) != 0; };
    constexpr auto stp = [](int n) { return (n / 10) % 4 == 3 ? (n / 10) * 5 + n % 5 : n / 2; };
    auto rdw = [&](auto N) {
      constexpr int n = N < 120 ? (N ^ 1) : 5 * (N / 5) + 4 - N % 5;
      if constexpr (n < 120) wf[n] = lds16(L, (isS(n) ? wbS : wbK) + ring_off(2 * stp(n))); else wf[n] = lds16(L, ringL + ring_off(n));
    };
    auto vmem = [&](auto Q, auto K) {
      constexpr int k = SC.slot(Q, K, 10);
      if constexpr (k >= 0) {
        constexpr int ty = SC.ty[k], ix = SC.ix[k];
        if constexpr (ty == OP_A) ldf<ix>(af[ix], l16, rws, ab);
        else if constexpr (ty == OP_W) dman<SC.cn[k], (ix % 15) * 1024, ix * 1024>(ringD, l16, rws, ix < 30 ? w4 : w5);
        else if constexpr (ty == OP_ST) ldf<ix>(stv[ix], l16, rws, WS_ST + (4 * p + m) * 4096);
        else if constexpr (ty == OP_PEEK) { if (w == 4) ld4(pk, lane == (ix ? 15 + ix : 5) ? CT_C4 + p * 384 + ix * 128 : 0x7ff00000, rws, 0); }
        else if constexpr (ty == OP_A5) ldf<ix>(a5[ix], l16, rws, ab5);
      }
    };
    auto epiA = [&](auto G, auto QD) { const f32x16 sd = aS[G]; *(LAS f32x4*)(L + xw + QD * 1024) = f32x4{sd[4 * QD], sd[4 * QD + 1], sd[4 * QD + 2], sd[4 * QD + 3]}; };
    auto epi = [&](auto G, auto K) {
      if constexpr (K < 2) sfor<2>([&](auto I) { constexpr int qd = 2 * K + I; eo[qd] = ldsf4(L, xr_ + qd * 1024); eu[qd] = ldsf4(L, ub + G * 256 + qd * 32); ev[qd] = ldsf4(L, ub + 768 + G * 256 + qd * 32); });
      else if constexpr (K < 18) { constexpr int r = K - 2; float t = fmaf(aK[G][r] + eo[r / 4][r % 4], rs2, fmaf(nm2, eu[r / 4][r % 4], ev[r / 4][r % 4])); tie(t); aK[G][r] = t; }
      else if constexpr (K < 26) { constexpr int k = K - 18; const float gv = aK[G][k]; float t = gv * __builtin_amdgcn_rcpf(1.f + __builtin_amdgcn_exp2f(-1.44269504f * gv)) * aK[G][8 + k]; tie(t); av[k] = t; }
      else if constexpr (K == 29) st16(v4u{pk2(av[0], av[1]), pk2(av[2], av[3]), pk2(av[4], av[5]), pk2(av[6], av[7])}, rws, ao + G * 8192);
    };
    auto top = [&](auto Q) {
      constexpr int q = Q;
      waitv<sc_wait(q)>();
      if constexpr (q < 4) sfor<5>([&](auto I) { tie(af[5 * q + I]); });
      if constexpr (q >= 13) { tie(a5[2 * (q - 13)]); tie(a5[2 * (q - 13) + 1]); }
      if constexpr (q == 11 || q == 13 || q == 18) {
        constexpr int ln = q == 11 ? 5 : q == 13 ? 16 : 17;
        if (w == 4) { waitv<SC.before[q] - sc_peek(q == 11 ? 0 : q == 13 ? 1 : 2)>(); tie(pk); if (!all64(lane != ln || pk - cbase >= 16u)) poll(caddr, cbase, 16u, lane == ln); }
      }
      if constexpr (q == 4 || q == 8 || q == 12) bar(); else rbar();
      if constexpr (q == 8 || q == 11 || q == 14) { if (tid == 64) signal(rws, CT_C4 + p * 384 + (q == 8 ? 0 : q == 11 ? 128 : 256)); }
    };
    sfor<12>([&](auto Q) {
      constexpr int q = Q, g = q / 4;
      top(Q);
      if constexpr (q == 0) sfor<6>(rdw);
      sfor<10>([&](auto K) {
        constexpr int n = 10 * q + K, i = stp(n) % 20;
        if constexpr (isS(n)) { aS[g] = mfma32(wf[n], af[i], aS[g]); tie(aS[g]); } else { aK[g] = mfma32(wf[n], af[i], aK[g]); tie(aK[g]); }
        if constexpr (n + 6 < 120) rdw(IC<n + 6>{});
        vmem(Q, K);
        if constexpr (q == 2 && K == 4) {
          sfor<4>([&](auto I) { tie(stv[I]); });
          f32x4 v = (__builtin_bit_cast(f32x4, stv[0]) + __builtin_bit_cast(f32x4, stv[1])) + (__builtin_bit_cast(f32x4, stv[2]) + __builtin_bit_cast(f32x4, stv[3]));
          float u[4];
#pragma unroll
          for (int e = 0; e < 4; ++e) { float a = v[e]; a = swap32_add(swap16_add(a)); u[e] = __builtin_bit_cast(float, __builtin_amdgcn_ds_bpermute((r32 >> 1) * 4, __builtin_bit_cast(int, a))); }
          const float s1 = (lane & 1) ? u[2] : u[0], s2 = (lane & 1) ? u[3] : u[1];
          const float mean = s1 * (1.f / 640.f);
          rs2 = __builtin_amdgcn_rsqf(fmaxf(s2 * (1.f / 640.f) - mean * mean, 0.f) + 1e-5f); nm2 = -mean * rs2; tie(rs2); tie(nm2);
        }
        if constexpr (q % 4 == 3 && K >= 6) epiA(IC<g>{}, IC<K - 6>{});
        if constexpr (g == 1 && q % 4 < 3) epi(IC<0>{}, IC<10 * (q % 4) + K>{});
        if constexpr (q == 8) { if constexpr (K < 2) epi(IC<1>{}, K); else { epi(IC<1>{}, IC<2 * K - 2>{}); epi(IC<1>{}, IC<2 * K - 1>{}); } }
        if constexpr (q == 9) epi(IC<1>{}, IC<(K < 9 ? 18 + K : 29)>{});
        SB();
      });
    });
    STAMP(17);
    top(IC<12>{});
    sfor<30>([&](auto K) { if constexpr (K % 3 == 0) vmem(IC<12>{}, IC<K / 3>{}); epi(IC<2>{}, K); SB(); });
    STAMP(19);
    sfor<12>([&](auto E) {
      constexpr int e = E;
      top(IC<13 + e>{});
      if constexpr (e == 0) sfor<8>([&](auto N) { rdw(IC<120 + N>{}); });
      sfor<10>([&](auto K) {
        constexpr int n = 10 * e + K;
        acc[n % 5] = mfma16(wf[120 + n], a5[n / 5], acc[n % 5]); tie(acc[n % 5]);
        if constexpr (n + 8 < 120) rdw(IC<128 + n>{});
        vmem(IC<13 + e>{}, K);
        SB();
      });
    });
    STAMP(20);
    const unsigned xs = L_BSRC + m * 5120;
    if (kh == 0) { *(LAS f32x4*)(L + xs + l16) = acc[2]; *(LAS f32x4*)(L + xs + 1024 + l16) = acc[3]; }
    else { *(LAS f32x4*)(L + xs + 2048 + l16) = acc[0]; *(LAS f32x4*)(L + xs + 3072 + l16) = acc[1]; *(LAS f32x4*)(L + xs + 4096 + l16) = acc[4]; }
    bar();
    f32x4 r0, r1, r2 = {};
    if (kh == 0) { r0 = acc[0] + ldsf4(L, xs + 2048 + l16); r1 = acc[1] + ldsf4(L, xs + 3072 + l16); r2 = acc[4] + ldsf4(L, xs + 4096 + l16); }
    else { r0 = acc[2] + ldsf4(L, xs + l16); r1 = acc[3] + ldsf4(L, xs + 1024 + l16); }
    const f32x4 g0 = ldsf4(L, L_MODV + 5440 + (32 * kh + 4 * g4) * 4), g1 = ldsf4(L, L_MODV + 5440 + (32 * kh + 16 + 4 * g4) * 4), g2 = ldsf4(L, L_MODV + 5440 + (64 + 4 * g4) * 4);
    GAS char* o = (GAS char*)P.out + (size_t)(16 * tt + i16) * 1280 + (80 * c8 + 4 * g4) * 2;
    *(GAS v2u*)(o + kh * 64) = v2u{pk2(bflo(x2k[0]) + g0[0] * r0[0], bfhi(x2k[0]) + g0[1] * r0[1]), pk2(bflo(x2k[1]) + g0[2] * r0[2], bfhi(x2k[1]) + g0[3] * r0[3])};
    *(GAS v2u*)(o + kh * 64 + 32) = v2u{pk2(bflo(x2k[2]) + g1[0] * r1[0], bfhi(x2k[2]) + g1[1] * r1[1]), pk2(bflo(x2k[3]) + g1[2] * r1[2], bfhi(x2k[3]) + g1[3] * r1[3])};
    if (kh == 0) *(GAS v2u*)(o + 128) = v2u{pk2(bflo(x2k[4]) + g2[0] * r2[0], bfhi(x2k[4]) + g2[1] * r2[1]), pk2(bflo(x2k[5]) + g2[2] * r2[2], bfhi(x2k[5]) + g2[3] * r2[3])};
    if (b == 0 && tid == 0) __hip_atomic_store((GAS unsigned*)(P.ws + CT_E), cbase + 1u, RLX_AGENT);
    STAMP(21);
  }
}

#if defined(__HIP_DEVICE_COMPILE__)
#pragma clang attribute pop
#endif
extern "C" void kernel_launch(void* const* d_in, const int* in_sizes, int n_in, void* d_out, int out_size, void* d_ws, size_t ws_size,
                              hipStream_t stream) {
  Params P;
  P.x = (const u16*)d_in[0]; P.wqkv = (const u16*)d_in[1]; P.wo = (const u16*)d_in[2];
  P.wg = (const u16*)d_in[4]; P.wu = (const u16*)d_in[5]; P.wd = (const u16*)d_in[6];
  P.c = (const u16*)d_in[7]; P.wmod = (const u16*)d_in[8];
  P.out = (u16*)d_out; P.ws = (char*)d_ws;
  fused_block<<<256, 512, 0, stream>>>(P);
}
```
